```python
import torch

def _softmax(x, axis=-1):
    return torch.softmax(x, dim=axis)
def _gelu(x):
    return torch.nn.functional.gelu(x, approximate="tanh")
def _silu(x):
    return torch.nn.functional.silu(x)
def _sigmoid(x):
    return torch.sigmoid(x)
def _layernorm(x, axis=-1, eps=1e-5):
    m = x.mean(dim=axis, keepdim=True)
    v = x.var(dim=axis, keepdim=True, unbiased=False)
    return (x - m) / torch.sqrt(v + eps)
def _scatter_add(tgt, idx, src, axis=0):
    return tgt.clone().index_add_(axis, idx, src)
def _segment_sum(src, seg, n):
    out = torch.zeros((n,) + src.shape[1:], dtype=src.dtype)
    return out.index_add_(0, seg, src)
def _pad(x, pw):
    flat = tuple(v for pair in reversed(pw) for v in pair)
    return torch.nn.functional.pad(x, flat)

kernel_name = 'gated_mix__ssm_conv_h3__compressed_kv_cla__prompt'

def setup_inputs():

    _w = (lambda a: (lambda t: t.to(torch.bfloat16) if t.is_floating_point() else t)(torch.as_tensor(a)))
    def _A_setup():
        B, S, D = 1, 2048, 448
        ks = 4
        return {
            "q": _w((lambda *s: torch.randn(*s, dtype=torch.bfloat16) * 0.05)(B, S, D)),
            "k": _w((lambda *s: torch.randn(*s, dtype=torch.bfloat16) * 0.4)(B, S, D)),
            "v": _w((lambda *s: torch.randn(*s, dtype=torch.bfloat16) * 0.4)(B, S, D)),
            "shift_k": _w((lambda *s: torch.randn(*s, dtype=torch.bfloat16) * 0.4)(D, ks) * 0.5),
            "ssm_k": _w((lambda *s: torch.randn(*s, dtype=torch.bfloat16) * 0.8)(D, S) * (1.0 / S ** 0.5)),
            "ssm_D": _w((lambda *s: torch.randn(*s, dtype=torch.bfloat16) * 0.4)(D)),
        }
    _A_in = _A_setup()

    def _B_setup():
        B, H, Sq, Sk, D, M = 1, 16, 2048, 2048, 256, 448
        Dc = 4 * D
        return {
            "x": _w((lambda *s: torch.randn(*s, dtype=torch.bfloat16) * 0.05)(B, Sq, M)),
            "c_kv": _w((lambda *s: torch.randn(*s, dtype=torch.bfloat16) * 0.4)(B, Sk, Dc)),
            "w_q": _w((lambda *s: torch.randn(*s, dtype=torch.bfloat16) * 6.4)(M, H, D) * (M ** -0.5)),
            "w_uk": _w((lambda *s: torch.randn(*s, dtype=torch.bfloat16) * 6.4)(Dc, H, D) * (Dc ** -0.5)),
            "w_uv": _w((lambda *s: torch.randn(*s, dtype=torch.bfloat16) * 0.4)(Dc, H, D) * (Dc ** -0.5)),
            "w_o": _w((lambda *s: torch.randn(*s, dtype=torch.bfloat16) * 0.4)(H, D, M) * ((H * D) ** -0.5)),
        }
    _B_in = _B_setup()

    return {
        "A_q": _A_in["q"],
        "A_k": _A_in["k"],
        "A_v": _A_in["v"],
        "A_shift_k": _A_in["shift_k"],
        "A_ssm_k": _A_in["ssm_k"],
        "A_ssm_D": _A_in["ssm_D"],
        "B_x": _B_in["x"],
        "B_c_kv": _B_in["c_kv"],
        "B_w_q": _B_in["w_q"],
        "B_w_uk": _B_in["w_uk"],
        "B_w_uv": _B_in["w_uv"],
        "B_w_o": _B_in["w_o"],
        "gate_w": _w((lambda *s: torch.randn(*s, dtype=torch.bfloat16) * 0.05)(448) * 16.0),
    }

def reference(A_q, A_k, A_v, A_shift_k, A_ssm_k, A_ssm_D, B_x, B_c_kv, B_w_q, B_w_uk, B_w_uv, B_w_o, gate_w):
    def _A_ref(q, k, v, shift_k, ssm_k, ssm_D):
        B, S, D = q.shape
        ks = shift_k.shape[-1]
        k_pad = _pad(k, ((0, 0), (ks - 1, 0), (0, 0)))
        k_shift = torch.zeros_like(k)
        for t in range(ks):
            k_shift = k_shift + k_pad[:, t:t + S, :] * shift_k[:, ks - 1 - t][None, None, :]
        kv = k_shift * v
        L = 2 * S
        kv_pad = _pad(kv, ((0, 0), (0, S), (0, 0)))
        kf_pad = _pad(ssm_k, ((0, 0), (0, S)))
        kv_f = (lambda x, axes=(-2,-1): torch.fft.rfft2(x.float(), dim=axes))(kv_pad[:, None, :, :], axes=(1, 2))
        kf_f = (lambda x, axes=(-2,-1): torch.fft.rfft2(x.float(), dim=axes))(kf_pad[:, None, :], axes=(1, 2))
        kf_f = (lambda x, *d: x.permute(*d))(kf_f, 1, 2, 0)
        y_f = kv_f * kf_f[None, :, :, :]
        y = (lambda x, s, axes=(-2,-1): torch.fft.irfft2(x, s=s, dim=axes).to(torch.bfloat16))(y_f, (1, L), axes=(1, 2))[:, 0, :S, :]
        y = y + kv * ssm_D[None, None, :]
        return q * y

    def _B_ref(x, c_kv, w_q, w_uk, w_uv, w_o):
        B, Sq, M = x.shape[0], x.shape[1], x.shape[2]
        Sk = c_kv.shape[1]
        H, D = w_q.shape[1], w_q.shape[2]
        q = torch.einsum("bqm,mhd->bhqd", x, w_q)
        k = torch.einsum("bkc,chd->bhkd", c_kv, w_uk)
        v = torch.einsum("bkc,chd->bhkd", c_kv, w_uv)
        scores = torch.einsum("bhqd,bhkd->bhqk", q, k) * (D ** -0.5)
        off = Sk - Sq
        qi = torch.arange(Sq)[:, None]
        kj = torch.arange(Sk)[None, :]
        cmask = kj <= (qi + off)
        scores = torch.where(cmask[None, None], scores, -1e9)
        p = _softmax(scores, axis=-1)
        ctx = torch.einsum("bhqk,bhkd->bhqd", p, v)
        return torch.einsum("bhqd,hdm->bqm", ctx, w_o)

    x = A_q
    g = _sigmoid((x * gate_w).sum(-1, keepdims=True))
    a = _A_ref(x, A_k, A_v, A_shift_k, A_ssm_k, A_ssm_D)
    b = _B_ref(x, B_c_kv, B_w_q, B_w_uk, B_w_uv, B_w_o)
    return g * a + (1 - g) * b
```

```cpp
#include <hip/hip_runtime.h>
#include <hip/hip_bf16.h>
#include <stdint.h>
#include <utility>

#define LAS __attribute__((address_space(3)))
#define GAS __attribute__((address_space(1)))
#define DI __device__ __forceinline__
typedef unsigned short bf16_t;
typedef short bf16x8 __attribute__((ext_vector_type(8)));
typedef short s16x4 __attribute__((ext_vector_type(4)));
typedef float f32x2 __attribute__((ext_vector_type(2)));
typedef float f32x4 __attribute__((ext_vector_type(4)));
typedef float f32x16 __attribute__((ext_vector_type(16)));
typedef unsigned u32x2 __attribute__((ext_vector_type(2)));
typedef unsigned u32x4 __attribute__((ext_vector_type(4)));
typedef __bf16 bfx2 __attribute__((ext_vector_type(2)));
typedef LAS unsigned char* ldsp;
typedef const LAS unsigned char* ldscp;

constexpr int SEQ = 2048, DM = 448, NH = 16, HD = 256, DC = 1024, NW_ = 4096;

constexpr size_t MB = 1024 * 1024;
constexpr size_t WS_Q = 0, WS_K = 16 * MB, WS_V = 32 * MB, WS_CTX = 48 * MB, WS_PART = 64 * MB, WS_ML = 80 * MB,
                 WS_Y2 = 81 * MB, WS_G = 84 * MB, WS_OPART = 85 * MB, WS_FLAGS = 100 * MB;
constexpr int FLAG_STRIDE = 64;
constexpr int N_AFLAGS = 128, N_OFLAGS = 64;

#define SBAR() __builtin_amdgcn_sched_barrier(0)
#define PIN(x) asm volatile("" : "+v"(x))
#define RLX_AGENT __ATOMIC_RELAXED, __HIP_MEMORY_SCOPE_AGENT

DI unsigned cvtpk(float lo, float hi) { f32x2 v = {lo, hi}; bfx2 b = __builtin_convertvector(v, bfx2); return __builtin_bit_cast(unsigned, b); }
DI float bf_lo(unsigned w) { return __uint_as_float(w << 16); }
DI float bf_hi(unsigned w) { return __uint_as_float(w & 0xffff0000u); }
DI float rbf(float x) { return bf_lo(cvtpk(x, 0.f)); }
DI unsigned rfl(unsigned x) { return __builtin_amdgcn_readfirstlane(x); }
DI s16x4 trread(ldscp p) { return __builtin_amdgcn_ds_read_tr16_b64_v4i16((LAS s16x4*)p); }
DI bf16x8 cat8(s16x4 lo, s16x4 hi) { return __builtin_shufflevector(lo, hi, 0, 1, 2, 3, 4, 5, 6, 7); }
DI bf16x8 ldsrd16(ldscp p) { return *(const LAS bf16x8*)p; }
#define MFMA16(a, b, c) __builtin_amdgcn_mfma_f32_16x16x32_bf16((a), (b), (c), 0, 0, 0)
#define MFMA32(a, b, c) __builtin_amdgcn_mfma_f32_32x32x16_bf16((a), (b), (c), 0, 0, 0)

constexpr int HTB = 128 * 64 * 2;
constexpr int K1_LDS = 8 * HTB;

DI int lds_byte(int r, int c) { const int st = (r >> 4) * 2 + (c >> 5), rr = r & 15, cc = c & 31, ob = rr * 64 + cc * 2; return st * 1024 + (ob ^ (((ob >> 9) & 1) << 5)); }
DI void stage_rc(int b, int& R, int& C) { const int st = b / 1024, sb = b % 1024, swz = sb ^ (((sb >> 9) & 1) << 5); R = (st >> 1) * 16 + swz / 64; C = (st & 1) * 32 + (swz % 64) / 2; }

struct K1Args {
    const bf16_t *x, *ak, *av, *shift_k, *ssm_k, *ssm_d, *ckv, *wq, *wuk, *wuv, *gate_w;
    unsigned char* ws;
};

DI void epi_kq(const f32x4 (&acc)[2][2][4][2], unsigned char* img  , int pm, float scale, int wr, int wc, int fr, int fq) {
#pragma unroll
    for (int ai = 0; ai < 2; ++ai)
#pragma unroll
        for (int m = 0; m < 4; ++m) {
            const int s = pm * 256 + ai * 128 + wr * 64 + m * 16 + fr;
            unsigned char* rowp = img + (size_t)(s >> 5) * 16384 + (s & 31) * 16 + (2 * wc + (fq & 1)) * 1024 + (fq >> 1) * 512;
#pragma unroll
            for (int bj = 0; bj < 2; ++bj) {
                const f32x4 a0 = acc[ai][bj][m][0] * scale, a1 = acc[ai][bj][m][1] * scale;
                const unsigned w0 = cvtpk(a0[0], a0[1]), w1 = cvtpk(a0[2], a0[3]), x0 = cvtpk(a1[0], a1[1]), x1 = cvtpk(a1[2], a1[3]);
                auto r0 = __builtin_amdgcn_permlane16_swap(w0, x0, false, false);
                auto r1 = __builtin_amdgcn_permlane16_swap(w1, x1, false, false);
                u32x4 w = {r0[0], r1[0], r0[1], r1[1]};
                *(u32x4*)(rowp + bj * 8192) = w;
            }
        }
}
DI void epi_v(const f32x4 (&acc)[2][2][4][2], unsigned char* img, int pm, int wr, int wc, int fr, int fq) {
#pragma unroll
    for (int ai = 0; ai < 2; ++ai) {
        unsigned char* tp = img + (size_t)(pm * 4 + ai * 2 + wr) * 32768 + wc * 4096 + (fq >> 1) * 1024 + (fq & 1) * 512 + fr * 16;
#pragma unroll
        for (int bj = 0; bj < 2; ++bj)
#pragma unroll
            for (int n = 0; n < 2; ++n)
#pragma unroll
                for (int mm = 0; mm < 2; ++mm) {
                    const f32x4 a0 = acc[ai][bj][2 * mm][n], a1 = acc[ai][bj][2 * mm + 1][n];
                    const unsigned w0 = cvtpk(a0[0], a0[1]), w1 = cvtpk(a0[2], a0[3]), x0 = cvtpk(a1[0], a1[1]), x1 = cvtpk(a1[2], a1[3]);
                    auto r0 = __builtin_amdgcn_permlane32_swap(w0, x0, false, false);
                    auto r1 = __builtin_amdgcn_permlane32_swap(w1, x1, false, false);
                    u32x4 w = {r0[0], r1[0], r0[1], r1[1]};
                    *(u32x4*)(tp + bj * 16384 + mm * 2048 + n * 256) = w;
                }
    }
}

template <bool VT>
DI void gemm_stream(ldsp lds, const K1Args& P, int pm, int head) {
    const int tid = threadIdx.x, wid = rfl(tid >> 6), lane = tid & 63, wr = wid >> 2, wc = wid & 3, fr = lane & 15, fq = lane >> 4;
    constexpr int NPAIR = VT ? 8 : 11;
    constexpr int LASTT = VT ? 15 : 22;
    unsigned voffA_kv, voffA_q, voffB;
    { int R, C; stage_rc(tid * 16, R, C); voffA_kv = (unsigned)(R * DC + C) * 2u; voffA_q = (unsigned)(R * DM + C) * 2u; }
    { const int kg = wid >> 1, row = (lane >> 2) & 7, ch = (lane & 3) ^ (2 * (kg & 1)), n = 32 * (2 * (wid & 1) + (lane >> 5)) + 8 * ch;
      voffB = (unsigned)((8 * kg + row) * NW_ + n) * 2u; }
    const unsigned ldsw = (unsigned)wid * 1024u;
    const int aoff = lds_byte(wr * 64 + fr, fq * 8);
    int boff[2];
#pragma unroll
    for (int n = 0; n < 2; ++n) boff[n] = fq * 2048 + wc * 512 + (fr >> 2) * 64 + ((32 * n) ^ (32 * (fq & 1))) + 8 * (fr & 3);
    const char* A_kv = (const char*)P.ckv + (size_t)pm * 256 * DC * 2;
    const char* A_q = (const char*)P.x + (size_t)pm * 256 * DM * 2;
    const char* B_kv = (const char*)(VT ? P.wuv : P.wuk) + head * 512;
    const char* B_q = (const char*)P.wq + head * 512;
#define TILE_A(gt) ((gt) < 16 ? A_kv + (gt) * 128 : A_q + ((gt) - 16) * 128)
#define TILE_B(gt) ((gt) < 16 ? B_kv + (size_t)(gt) * (64 * NW_ * 2) : B_q + (size_t)((gt) - 16) * (64 * NW_ * 2))
#define STRA(gt) ((gt) < 16 ? DC * 2 : DM * 2)
#define VOFA(gt) ((gt) < 16 ? voffA_kv : voffA_q)
#define SA(b, h) (((b) * 2 + (h)) * HTB)
#define SB(b, h) ((4 + (b) * 2 + (h)) * HTB)
#define GLDS(g, l) __builtin_amdgcn_global_load_lds((const GAS unsigned*)(g), (LAS unsigned*)(l), 16, 0, 0)
#define STAGE_A(bufoff, base, stride, voff, h) do { const char* _b = (base) + (size_t)(h) * 128 * (stride); \
        GLDS(_b + (voff), lds + (bufoff) + ldsw); GLDS(_b + (size_t)64 * (stride) + (voff), lds + (bufoff) + ldsw + 8192); } while (0)
#define STAGE_B(bufoff, base, h) do { const char* _b = (base) + (h) * 256; \
        GLDS(_b + voffB, lds + (bufoff) + ldsw); GLDS(_b + (size_t)32 * NW_ * 2 + voffB, lds + (bufoff) + ldsw + 8192); } while (0)
#define LDA(dst, b, h) do { _Pragma("unroll") for (int m = 0; m < 4; ++m) _Pragma("unroll") for (int k = 0; k < 2; ++k) dst[m][k] = ldsrd16(lds + SA(b, h) + aoff + m * 2048 + k * 1024); } while (0)
#define LDB(dst, b, h) do { _Pragma("unroll") for (int n = 0; n < 2; ++n) _Pragma("unroll") for (int k = 0; k < 2; ++k) \
        dst[n][k] = cat8(trread(lds + SB(b, h) + boff[n] + k * 8192), trread(lds + SB(b, h) + boff[n] + k * 8192 + 256)); } while (0)
#define MMA(ai, bj, At, Bt) do { __builtin_amdgcn_s_setprio(1); _Pragma("unroll") for (int m = 0; m < 4; ++m) _Pragma("unroll") for (int n = 0; n < 2; ++n) _Pragma("unroll") for (int k = 0; k < 2; ++k) \
        acc[ai][bj][m][n] = VT ? MFMA16(At[m][k], Bt[n][k], acc[ai][bj][m][n]) : MFMA16(Bt[n][k], At[m][k], acc[ai][bj][m][n]); __builtin_amdgcn_s_setprio(0); } while (0)
#define WAIT_V(n) asm volatile("s_waitcnt vmcnt(" #n ")" ::: "memory")
#define WAIT_L(n) asm volatile("s_waitcnt lgkmcnt(" #n ")" ::: "memory")
#define BAR __builtin_amdgcn_s_barrier()
    f32x4 acc[2][2][4][2];
#pragma unroll
    for (int a = 0; a < 2; ++a)
#pragma unroll
        for (int b = 0; b < 2; ++b)
#pragma unroll
            for (int m = 0; m < 4; ++m)
#pragma unroll
                for (int n = 0; n < 2; ++n) acc[a][b][m][n] = (f32x4){0.f, 0.f, 0.f, 0.f};
    bf16x8 At[4][2], B0[2][2], B1[2][2];
    STAGE_B(SB(0, 0), B_kv, 0); STAGE_A(SA(0, 0), A_kv, DC * 2, voffA_kv, 0); STAGE_B(SB(0, 1), B_kv, 1); STAGE_A(SA(0, 1), A_kv, DC * 2, voffA_kv, 1);
    if (wr == 1) BAR;
    WAIT_V(4); BAR;
    STAGE_B(SB(1, 0), TILE_B(1), 0); STAGE_A(SA(1, 0), TILE_A(1), DC * 2, voffA_kv, 0); STAGE_B(SB(1, 1), TILE_B(1), 1);
    WAIT_V(6); BAR;
    for (int p = 0; p < NPAIR; ++p) {
        const int t = 2 * p;
        const int t1 = t + 1, t2 = (t + 2 > LASTT) ? LASTT : t + 2, t3 = (t + 3 > LASTT) ? LASTT : t + 3;
        const char* a1 = TILE_A(t1); const int s1 = STRA(t1); const unsigned v1 = VOFA(t1);
        const char* a2 = TILE_A(t2); const char* a3 = TILE_A(t3); const int s2 = STRA(t2); const unsigned v2 = VOFA(t2);
        const char* b2 = TILE_B(t2); const char* b3 = TILE_B(t3);
        LDB(B0, 0, 0); SBAR(); LDA(At, 0, 0); STAGE_A(SA(1, 1), a1, s1, v1, 1);
        WAIT_L(8); BAR; WAIT_L(0); MMA(0, 0, At, B0); BAR; SBAR();
        LDB(B1, 0, 1); STAGE_B(SB(0, 0), b2, 0);
        BAR; WAIT_L(0); MMA(0, 1, At, B1); BAR;
        LDA(At, 0, 1); STAGE_A(SA(0, 0), a2, s2, v2, 0);
        BAR; WAIT_L(0); MMA(1, 0, At, B0); BAR; SBAR();
        STAGE_B(SB(0, 1), b2, 1);
        WAIT_V(6); BAR; MMA(1, 1, At, B1); BAR;
        LDB(B0, 1, 0); SBAR(); LDA(At, 1, 0); STAGE_A(SA(0, 1), a2, s2, v2, 1);
        WAIT_L(8); BAR; WAIT_L(0); MMA(0, 0, At, B0); BAR; SBAR();
        LDB(B1, 1, 1); STAGE_B(SB(1, 0), b3, 0);
        BAR; WAIT_L(0); MMA(0, 1, At, B1); BAR;
        LDA(At, 1, 1); STAGE_A(SA(1, 0), a3, s2, v2, 0);
        BAR; WAIT_L(0); MMA(1, 0, At, B0); BAR; SBAR();
        STAGE_B(SB(1, 1), b3, 1);
        WAIT_V(6); BAR; MMA(1, 1, At, B1); BAR;
        if (!VT && p == 7) {
            epi_kq(acc, P.ws + WS_K + (size_t)head * MB, pm, 1.0f, wr, wc, fr, fq);
#pragma unroll
            for (int a = 0; a < 2; ++a)
#pragma unroll
                for (int b = 0; b < 2; ++b)
#pragma unroll
                    for (int m = 0; m < 4; ++m)
#pragma unroll
                        for (int n = 0; n < 2; ++n) acc[a][b][m][n] = (f32x4){0.f, 0.f, 0.f, 0.f};
        }
    }
    if (VT) {
        epi_v(acc, P.ws + WS_V + (size_t)head * MB, pm, wr, wc, fr, fq);
    } else {
        LDB(B0, 0, 0); LDA(At, 0, 0); BAR; WAIT_L(0); MMA(0, 0, At, B0); BAR;
        LDB(B1, 0, 1); BAR; WAIT_L(0); MMA(0, 1, At, B1); BAR;
        LDA(At, 0, 1); BAR; WAIT_L(0); MMA(1, 0, At, B0); MMA(1, 1, At, B1); BAR;
        epi_kq(acc, P.ws + WS_Q + (size_t)head * MB, pm, 0.0625f * 1.4426950408889634f, wr, wc, fr, fq);
    }
    WAIT_V(0);
    if (wr == 0) BAR;
    BAR;
#undef TILE_A
#undef TILE_B
#undef STRA
#undef VOFA
#undef SA
#undef SB
#undef STAGE_A
#undef STAGE_B
#undef LDA
#undef LDB
#undef MMA
}

constexpr int CV_HSTR = 4160;
constexpr int CV_H = 0;
constexpr int CV_USTR = 4608;
constexpr int CV_U = 16 * CV_HSTR;
constexpr int CV_Y = CV_U + 4 * CV_USTR;
static_assert(CV_Y + 4 * 4096 <= K1_LDS, "lds");

template <int HW>
DI void conv_wave(ldscp hbase, ldscp ubase, ldsp ybase, int fr, int fq) {
    constexpr int NV = HW == 0 ? 8 : 6;
    f32x4 acc[4];
#pragma unroll
    for (int i = 0; i < 4; ++i) acc[i] = (f32x4){0.f, 0.f, 0.f, 0.f};
    for (int rho = 0; rho < 8; ++rho) {
        bf16x8 A[NV], B[NV];
        ldscp hp = hbase + rho * 64; ldscp up = ubase + rho * 64;
#pragma unroll
        for (int v = 0; v < NV; ++v) {
            const s16x4 lo = *(const LAS s16x4*)(hp + v * 512), hi = *(const LAS s16x4*)(hp + v * 512 + 8);
            A[v] = cat8(lo, hi);
            B[v] = ldsrd16(up + (7 - v) * 512);
        }
#pragma unroll
        for (int i = 0; i < 4; ++i) {
            constexpr int JS0[4] = {7, 6, 1, 0}, JS1[4] = {5, 4, 3, 2};
            const int J = HW == 0 ? JS0[i] : JS1[i];
#pragma unroll
            for (int v = 0; v < NV; ++v) if (v <= J) acc[i] = MFMA16(A[v], B[J - v], acc[i]);
        }
    }
#pragma unroll
    for (int i = 0; i < 4; ++i) {
        constexpr int JS0[4] = {7, 6, 1, 0}, JS1[4] = {5, 4, 3, 2};
        const int J = HW == 0 ? JS0[i] : JS1[i];
        u32x2 w = {cvtpk(acc[i][0], acc[i][1]), cvtpk(acc[i][2], acc[i][3])};
        *(LAS u32x2*)(ybase + (256 * J + 16 * fr + 4 * fq) * 2) = w;
    }
}

DI void conv_part(ldsp lds, const K1Args& P, int c0) {
    const int tid = threadIdx.x, wid = rfl(tid >> 6), lane = tid & 63, fr = lane & 15, fq = lane >> 4;
    const int t0 = 4 * tid;
    u32x2 kr[7], vr[4];
#pragma unroll
    for (int i = 0; i < 7; ++i) { const int t = t0 - 3 + i; kr[i] = t >= 0 ? *(const u32x2*)(P.ak + (size_t)t * DM + c0) : (u32x2){0u, 0u}; }
#pragma unroll
    for (int i = 0; i < 4; ++i) vr[i] = *(const u32x2*)(P.av + (size_t)(t0 + i) * DM + c0);
    for (int it = tid; it < 4 * 260; it += 512) {
        const int ch = it / 260, ck = it - ch * 260;
        const bf16_t* hrow = P.ssm_k + (size_t)(c0 + ch) * SEQ;
        const int i0 = 8 * ck - 16;
        u32x4 d = {0u, 0u, 0u, 0u}; u32x2 e = {0u, 0u};
        if (i0 >= 0 && i0 < SEQ) d = *(const u32x4*)(hrow + i0);
        if (i0 + 8 >= 0 && i0 + 8 < SEQ) e = *(const u32x2*)(hrow + i0 + 8);
        const unsigned a0 = __builtin_amdgcn_alignbit(d[1], d[0], 16), a1 = __builtin_amdgcn_alignbit(d[2], d[1], 16), a2 = __builtin_amdgcn_alignbit(d[3], d[2], 16),
                       a3 = __builtin_amdgcn_alignbit(e[0], d[3], 16), a4 = __builtin_amdgcn_alignbit(e[1], e[0], 16);
        ldsp q = lds + CV_H + ch * 4 * CV_HSTR + ck * 16;
        *(LAS u32x4*)(q) = d;
        *(LAS u32x4*)(q + CV_HSTR) = (u32x4){a0, a1, a2, a3};
        *(LAS u32x4*)(q + 2 * CV_HSTR) = (u32x4){d[1], d[2], d[3], e[0]};
        *(LAS u32x4*)(q + 3 * CV_HSTR) = (u32x4){a1, a2, a3, a4};
    }
    float kvv[4][4];
    {
        const u32x4 s01 = *(const u32x4*)(P.shift_k + c0 * 4), s23 = *(const u32x4*)(P.shift_k + c0 * 4 + 8);
        float w[4][4];
        w[0][0] = bf_lo(s01[0]); w[0][1] = bf_hi(s01[0]); w[0][2] = bf_lo(s01[1]); w[0][3] = bf_hi(s01[1]);
        w[1][0] = bf_lo(s01[2]); w[1][1] = bf_hi(s01[2]); w[1][2] = bf_lo(s01[3]); w[1][3] = bf_hi(s01[3]);
        w[2][0] = bf_lo(s23[0]); w[2][1] = bf_hi(s23[0]); w[2][2] = bf_lo(s23[1]); w[2][3] = bf_hi(s23[1]);
        w[3][0] = bf_lo(s23[2]); w[3][1] = bf_hi(s23[2]); w[3][2] = bf_lo(s23[3]); w[3][3] = bf_hi(s23[3]);
#pragma unroll
        for (int c = 0; c < 4; ++c) {
            float kc[7];
#pragma unroll
            for (int i = 0; i < 7; ++i) kc[i] = (c & 1) ? bf_hi(kr[i][c >> 1]) : bf_lo(kr[i][c >> 1]);
#pragma unroll
            for (int i = 0; i < 4; ++i) {
                float a = rbf(kc[i] * w[c][3]);
                a = rbf(a + rbf(kc[i + 1] * w[c][2]));
                a = rbf(a + rbf(kc[i + 2] * w[c][1]));
                a = rbf(a + rbf(kc[i + 3] * w[c][0]));
                const float vv = (c & 1) ? bf_hi(vr[i][c >> 1]) : bf_lo(vr[i][c >> 1]);
                kvv[i][c] = rbf(a * vv);
            }
            u32x2 wv = {cvtpk(kvv[3][c], kvv[2][c]), cvtpk(kvv[1][c], kvv[0][c])};
            *(LAS u32x2*)(lds + CV_U + c * CV_USTR + (2044 - t0) * 2) = wv;
        }
        if (tid < 240) { const int c = tid / 60, j = tid - c * 60; *(LAS u32x2*)(lds + CV_U + c * CV_USTR + 4096 + j * 8) = (u32x2){0u, 0u}; }
    }
    __syncthreads();
    {
        const int ch = wid >> 1;
        const int y0 = 25 + fr - 8 * fq;
        ldscp hb = lds + CV_H + ch * 4 * CV_HSTR + (y0 & 3) * CV_HSTR + (y0 & ~3) * 2;
        ldscp ub = lds + CV_U + ch * CV_USTR + (2056 - 256 * 7 - 16 * fr - 8 * fq) * 2;
        ldsp yb = lds + CV_Y + ch * 4096;
        if (wid & 1) conv_wave<1>(hb, ub, yb, fr, fq); else conv_wave<0>(hb, ub, yb, fr, fq);
    }
    __syncthreads();
    {
        const u32x2 dw = *(const u32x2*)(P.ssm_d + c0);
        const float dd[4] = {bf_lo(dw[0]), bf_hi(dw[0]), bf_lo(dw[1]), bf_hi(dw[1])};
        float y2[4][4];
#pragma unroll
        for (int c = 0; c < 4; ++c) {
            const u32x2 yw = *(const LAS u32x2*)(lds + CV_Y + c * 4096 + t0 * 2);
            const float y[4] = {bf_lo(yw[0]), bf_hi(yw[0]), bf_lo(yw[1]), bf_hi(yw[1])};
#pragma unroll
            for (int i = 0; i < 4; ++i) y2[i][c] = y[i] + rbf(kvv[i][c] * dd[c]);
        }
        bf16_t* y2p = (bf16_t*)(P.ws + WS_Y2);
#pragma unroll
        for (int i = 0; i < 4; ++i) { u32x2 w = {cvtpk(y2[i][0], y2[i][1]), cvtpk(y2[i][2], y2[i][3])}; *(u32x2*)(y2p + (size_t)(t0 + i) * DM + c0) = w; }
    }
}

DI void gate_part(const K1Args& P, int row0) {
    const int tid = threadIdx.x, wid = tid >> 6, lane = tid & 63;
    u32x4 gw = {0u, 0u, 0u, 0u};
    if (lane < 56) gw = *(const u32x4*)(P.gate_w + lane * 8);
    float* gp = (float*)(P.ws + WS_G);
    for (int r = 0; r < 16; ++r) {
        const int row = row0 + wid * 16 + r;
        u32x4 xv = {0u, 0u, 0u, 0u};
        if (lane < 56) xv = *(const u32x4*)(P.x + (size_t)row * DM + lane * 8);
        float s = 0.f;
#pragma unroll
        for (int j = 0; j < 4; ++j) { s += rbf(bf_lo(xv[j]) * bf_lo(gw[j])); s += rbf(bf_hi(xv[j]) * bf_hi(gw[j])); }
#pragma unroll
        for (int o = 32; o >= 1; o >>= 1) s += __shfl_xor(s, o);
        const float z = rbf(s);
        const float g = rbf(1.0f / (1.0f + __expf(-z)));
        if (lane == 0) gp[row] = g;
    }
}

__global__ __launch_bounds__(512, 2) void k1_proj(K1Args P) {
    extern __shared__ __attribute__((aligned(16))) unsigned char shm[];
    ldsp lds = (ldsp)shm;
    const int b = blockIdx.x, xcd = b & 7, j = b >> 3, li = j & 15, pm = li & 7, head = 2 * xcd + (li >> 3);
    if (b == 0 && threadIdx.x < N_AFLAGS + N_OFLAGS) ((unsigned*)(P.ws + WS_FLAGS))[threadIdx.x * FLAG_STRIDE] = 0u;
    if (j < 16) {
        gemm_stream<false>(lds, P, pm, head);
    } else {
        gemm_stream<true>(lds, P, pm, head);
        if (li < 14) conv_part(lds, P, (xcd * 14 + li) * 4);
        else gate_part(P, (xcd * 2 + (li - 14)) * 128);
    }
}

constexpr int AT_K = 0, AT_V = 65536, AT_LDS = 163840;
constexpr float THR = 8.0f;
constexpr int PD = 6;
constexpr int NEB = 24;
template <int... Is> using iseq = std::integer_sequence<int, Is...>;
template <int N> using make_iseq = std::make_integer_sequence<int, N>;
#define ACLOB "a0","a1","a2","a3","a4","a5","a6","a7","a8","a9","a10","a11","a12","a13","a14","a15","a16","a17","a18","a19","a20","a21","a22","a23","a24","a25","a26","a27","a28","a29","a30","a31","a32","a33","a34","a35","a36","a37","a38","a39","a40","a41","a42","a43","a44","a45","a46","a47","a48","a49","a50","a51","a52","a53","a54","a55","a56","a57","a58","a59","a60","a61","a62","a63","a64","a65","a66","a67","a68","a69","a70","a71","a72","a73","a74","a75","a76","a77","a78","a79","a80","a81","a82","a83","a84","a85","a86","a87","a88","a89","a90","a91","a92","a93","a94","a95","a96","a97","a98","a99","a100","a101","a102","a103","a104","a105","a106","a107","a108","a109","a110","a111","a112","a113","a114","a115","a116","a117","a118","a119","a120","a121","a122","a123","a124","a125","a126","a127","a128","a129","a130","a131","a132","a133","a134","a135","a136","a137","a138","a139","a140","a141","a142","a143","a144","a145","a146","a147","a148","a149","a150","a151","a152","a153","a154","a155","a156","a157","a158","a159","a160","a161","a162","a163","a164","a165","a166","a167","a168","a169","a170","a171","a172","a173","a174","a175","a176","a177","a178","a179","a180","a181","a182","a183","a184","a185","a186","a187","a188","a189","a190","a191","a192","a193","a194","a195","a196","a197","a198","a199","a200","a201","a202","a203","a204","a205","a206","a207","a208","a209","a210","a211","a212","a213","a214","a215","a216","a217","a218","a219","a220","a221","a222","a223","a224","a225","a226","a227","a228","a229","a230","a231","a232","a233","a234","a235","a236","a237","a238","a239","a240","a241","a242","a243","a244","a245","a246","a247","a248","a249","a250","a251","a252","a253","a254","a255"

struct AT {
    f32x16 s[2][2];
    f32x16 cm[2];
    u32x4 p[4];
    bf16x8 fr[8];
    float m, l, rs0, rs1, mx0, mx1, alpha, aone;
    bool pend, first, drain;
    ldscp kb;
    ldscp vcur;
    unsigned goff;
    unsigned m0k, m0v;
    const char *gk, *gv;
};

DI void dma_piece(unsigned m0v, unsigned goff, const char* g, const int off) {
    switch (off) {
    case 0: asm volatile("s_mov_b32 m0, %0\n\ts_nop 0\n\tglobal_load_lds_dwordx4 %1, %2" :: "s"(m0v), "v"(goff), "s"(g) : "memory"); break;
    case 1: asm volatile("s_mov_b32 m0, %0\n\ts_nop 0\n\tglobal_load_lds_dwordx4 %1, %2 offset:1024" :: "s"(m0v), "v"(goff), "s"(g) : "memory"); break;
    case 2: asm volatile("s_mov_b32 m0, %0\n\ts_nop 0\n\tglobal_load_lds_dwordx4 %1, %2 offset:2048" :: "s"(m0v), "v"(goff), "s"(g) : "memory"); break;
    default: asm volatile("s_mov_b32 m0, %0\n\ts_nop 0\n\tglobal_load_lds_dwordx4 %1, %2 offset:3072" :: "s"(m0v), "v"(goff), "s"(g) : "memory"); break;
    }
}
template <int I> DI void dma8(unsigned m0base, unsigned goff, const char* g) { dma_piece(m0base + (I >> 2) * 4096, goff, g + (I >> 2) * 4096, I & 3); }
DI void dma_tile(unsigned m0base, unsigned goff, const char* g) {
    dma8<0>(m0base, goff, g); dma8<1>(m0base, goff, g); dma8<2>(m0base, goff, g); dma8<3>(m0base, goff, g);
    dma8<4>(m0base, goff, g); dma8<5>(m0base, goff, g); dma8<6>(m0base, goff, g); dma8<7>(m0base, goff, g);
}
DI float ex2(float x) { return __builtin_amdgcn_exp2f(x); }
DI float MX3(float a, float b, float c) { float r; asm("v_max3_f32 %0, %1, %2, %3" : "=v"(r) : "v"(a), "v"(b), "v"(c)); return r; }
DI float MX2(float a, float b) { float r; asm("v_max_f32_e32 %0, %1, %2" : "=v"(r) : "v"(a), "v"(b)); return r; }
template <int D0> DI void qk_init(f32x16& s, const bf16x8& k, const f32x16& c) {
    asm volatile("v_mfma_f32_32x32x16_bf16 %0, %1, a[%c3:%c4], %2" : "=&v"(s) : "v"(k), "v"(c), "i"(128 + 4 * D0), "i"(131 + 4 * D0)); }
template <int D0> DI void qk_acc(f32x16& s, const bf16x8& k) {
    asm volatile("v_mfma_f32_32x32x16_bf16 %0, %1, a[%c2:%c3], %0" : "+v"(s) : "v"(k), "i"(128 + 4 * D0), "i"(131 + 4 * D0)); }
template <int DB> DI void pv_acc(const bf16x8& v, const u32x4& p) {
    asm volatile("v_mfma_f32_32x32x16_bf16 a[%c2:%c3], %0, %1, a[%c2:%c3]" :: "v"(v), "v"(p), "i"(16 * DB), "i"(16 * DB + 15)); }
DI void add_bcast(f32x16& x, float aone, float b) { asm volatile("s_nop 1\n\tv_mfma_f32_32x32x2_f32 %0, %1, %2, %0" : "+v"(x) : "v"(aone), "v"(b)); }
#define NOPS16() asm volatile("s_nop 7\n\ts_nop 7" ::: "memory")
#define NOPS2() asm volatile("s_nop 1" ::: "memory")
template <int I> DI void a_zero() { asm volatile("v_accvgpr_write_b32 a[%c0], 0" :: "i"(I)); }
template <int I> DI void a_scale(float f) { float t; asm volatile("v_accvgpr_read_b32 %0, a[%c2]\n\tv_mul_f32 %0, %1, %0\n\tv_accvgpr_write_b32 a[%c2], %0" : "=&v"(t) : "v"(f), "i"(I)); }
template <int I> DI void a_qmov() { asm volatile("v_accvgpr_mov_b32 a[%c0], a[%c1]" :: "i"(128 + I), "i"(192 + I)); }
template <int I> DI float a_read() { float t; asm volatile("v_accvgpr_read_b32 %0, a[%c1]" : "=v"(t) : "i"(I)); return t; }
template <int F> DI void q_load(unsigned voff, const char* g) {
    asm volatile("global_load_dwordx4 a[%c2:%c3], %0, %1 offset:%c4" :: "v"(voff), "s"(g + ((F & 15) >> 2) * 4096), "i"(128 + 4 * F), "i"(131 + 4 * F), "i"((F & 3) * 1024) : "memory"); }
template <int Q> DI void o_store(unsigned voff, const char* g) {
    asm volatile("global_store_dwordx4 %0, a[%c2:%c3], %1 offset:%c4 sc1" :: "v"(voff), "s"(g + (Q >> 2) * 4096), "i"(4 * Q), "i"(4 * Q + 3), "i"((Q & 3) * 1024) : "memory"); }
template <int... Is> DI void a_zeros(iseq<Is...>) { (a_zero<Is>(), ...); }
template <int... Is> DI void a_scales(float f, iseq<Is...>) { (a_scale<Is>(f), ...); }
template <int... Is> DI void a_qmovs(iseq<Is...>) { (a_qmov<Is>(), ...); }
template <int B, int... Is> DI void q_loads(unsigned voff, const char* g, iseq<Is...>) { (q_load<B + Is>(voff, g), ...); }
template <int... Is> DI void o_stores(unsigned voff, const char* g, iseq<Is...>) { (o_store<Is>(voff, g), ...); }
template <int B, int... Is> DI void a_reads(float (&o)[16], iseq<Is...>) { ((o[Is] = a_read<B + Is>()), ...); }

template <int KS, int R> DI bf16x8 kfrag(const AT& c) { return ldsrd16(c.kb + KS * 32768 + ((R & 1) * 16 + (R >> 1)) * 1024); }
template <int R> DI bf16x8 vfrag(ldscp v) { return ldsrd16(v + (R & 7) * 4096 + (R >> 3) * 1024); }
template <int X, int E> DI void exp_sum(AT& c) {
    const float e = ex2(c.s[X][E >> 4][E & 15]); c.s[X][E >> 4][E & 15] = e;
    if constexpr (E & 1) c.rs1 += e; else c.rs0 += e;
}
template <int X, int W> DI void pack1(AT& c) {
    c.p[W >> 2][W & 3] = cvtpk(c.s[X][W >> 3][(2 * W) & 15], c.s[X][W >> 3][(2 * W + 1) & 15]);
}
template <int X, int MODE, int N> DI void gapA(AT& c) {
    constexpr int d0 = N >> 1, kb = N & 1, Y = X ^ 1;
    if constexpr (d0 == 0) qk_init<0>(c.s[Y][kb], c.fr[N & 7], c.cm[(MODE & 4) ? kb : 0]);
    else qk_acc<d0>(c.s[Y][kb], c.fr[N & 7]);
    constexpr int R = N + PD;
    if constexpr (R < 32) c.fr[R & 7] = kfrag<Y, R>(c); else if constexpr (MODE & 1) c.fr[R & 7] = vfrag<R - 32>(c.vcur);
    if constexpr (MODE & 1) {
        if constexpr (N < 32 - NEB) { exp_sum<X, NEB + N>(c); PIN(c.rs0); PIN(c.rs1); }
        if constexpr (N >= 10 && N < 26) { pack1<X, N - 10>(c); PIN(c.p[(N - 10) >> 2]); }
    }
    if constexpr ((MODE & 2) && (N & 1)) {
        if constexpr (N < 16) dma8<(N >> 1)>(c.m0k, c.goff, c.gk); else dma8<((N - 16) >> 1)>(c.m0v, c.goff, c.gv);
    }
    SBAR();
}
template <int X> DI void decide(AT& c) {
    constexpr int Y = X ^ 1;
    float mm = MX2(c.mx0, c.mx1);
    auto rr = __builtin_amdgcn_permlane32_swap(__float_as_uint(mm), __float_as_uint(mm), false, false);
    const float rm = MX2(__uint_as_float(rr[0]), __uint_as_float(rr[1]));
    if (__builtin_expect(c.first || __any(rm > THR), 0)) {
        const float dl = c.first ? rm : MX2(rm, 0.f);
        c.m += dl; c.alpha = ex2(-dl); c.pend = !c.first;
        const float nd = -dl;
        add_bcast(c.s[Y][0], c.aone, nd); add_bcast(c.s[Y][1], c.aone, nd); add_bcast(c.cm[0], c.aone, nd);
        asm volatile("s_nop 7\n\ts_nop 7\n\ts_nop 3" : "+v"(c.s[Y][0]), "+v"(c.s[Y][1]), "+v"(c.cm[0]));
    }
}
template <int X, int I> DI void maxstep(AT& c) {
    constexpr int Y = X ^ 1, h = I >> 2, r = 4 * (I & 3);
    if constexpr (I == 0) { c.mx0 = MX2(c.s[Y][0][0], c.s[Y][0][1]); c.mx1 = MX2(c.s[Y][0][2], c.s[Y][0][3]); }
    else { c.mx0 = MX3(c.mx0, c.s[Y][h][r], c.s[Y][h][r + 1]); c.mx1 = MX3(c.mx1, c.s[Y][h][r + 2], c.s[Y][h][r + 3]); }
}
template <int X, int MODE, int N> DI void gapB(AT& c) {
    constexpr int ks = N >> 3, db = N & 7, Y = X ^ 1;
    pv_acc<db>(c.fr[N & 7], c.p[ks]);
    constexpr int R = N + PD;
    if constexpr (R < 32) c.fr[R & 7] = vfrag<R>(c.vcur);
    if constexpr (MODE & 1) {
        if constexpr (MODE & 2) {
            if constexpr (N == 28) { c.fr[0] = kfrag<X, 0>(c); c.fr[1] = kfrag<X, 1>(c); }
            if constexpr (N == 29) { c.fr[2] = kfrag<X, 2>(c); c.fr[3] = kfrag<X, 3>(c); }
            if constexpr (N == 30) { c.fr[4] = kfrag<X, 4>(c); }
            if constexpr (N == 31) { c.fr[5] = kfrag<X, 5>(c); }
        }
        if constexpr (N == 1) { asm volatile("s_nop 7" : "+v"(c.s[Y][0]), "+v"(c.s[Y][1])); }
        if constexpr (N >= 2 && N < 6) { maxstep<X, 2 * (N - 2)>(c); maxstep<X, 2 * (N - 2) + 1>(c); PIN(c.mx0); PIN(c.mx1); }
        if constexpr (N == 6) { c.l += c.rs0 + c.rs1; c.rs0 = 0.f; c.rs1 = 0.f; PIN(c.l); }
        if constexpr (N == 7) decide<X>(c);
        if constexpr (N >= 8) { exp_sum<Y, N - 8>(c); PIN(c.rs0); PIN(c.rs1); }
    }
    SBAR();
    if constexpr ((MODE & 1) && N == 27) {
        if constexpr (MODE & 2) {
            if (X == 1 && c.drain) asm volatile("s_waitcnt vmcnt(0)" ::: "memory");
            asm volatile("s_waitcnt vmcnt(8) lgkmcnt(0)\n\ts_barrier" ::: "memory");
        } else asm volatile("s_waitcnt vmcnt(0) lgkmcnt(0)\n\ts_barrier" ::: "memory");
        SBAR();
    }
}
template <int X, int MODE, int... Ns> DI void phaseA(AT& c, iseq<Ns...>) { (gapA<X, MODE, Ns>(c), ...); }
template <int X, int MODE, int... Ns> DI void phaseB(AT& c, iseq<Ns...>) { (gapB<X, MODE, Ns>(c), ...); }
template <int X, int B, int... Es> DI void exps_from(AT& c, iseq<Es...>) { (exp_sum<X, B + Es>(c), ...); }
template <int X, int... Ws> DI void packs(AT& c, iseq<Ws...>) { (pack1<X, Ws>(c), ...); }
template <int... Is> DI void maxsteps1(AT& c, iseq<Is...>) { (maxstep<1, Is>(c), ...); }
DI void rescale_o(AT& c) {
    if (__builtin_expect(c.pend, 0)) {
        NOPS16();
        a_scales(c.alpha, make_iseq<128>{});
        NOPS2();
        c.l *= c.alpha; c.pend = false;
    }
}
DI float halfsum(float x) { auto rr = __builtin_amdgcn_permlane32_swap(__float_as_uint(x), __float_as_uint(x), false, false); return __uint_as_float(rr[0]) + __uint_as_float(rr[1]); }
DI void build_mask(AT& c, int mrel) {
    const float NEG = -__builtin_inff(), nm = -c.m;
#pragma unroll
    for (int r = 0; r < 16; ++r) { const int kk = (r & 3) + 8 * (r >> 2); c.cm[0][r] = kk > mrel ? NEG : nm; c.cm[1][r] = kk + 32 > mrel ? NEG : nm; }
    PIN(c.cm[0]); PIN(c.cm[1]); NOPS2();
}

struct ATW {
    const char *Kh, *Vh; ldsp lds; unsigned mk, mv; int n1, vs, lane, wid, r32, hi, qrel; bool isA;
    const char* part; f32x2* mlp; unsigned* flag;
};
DI int tile_of(const ATW& w, int g) { return w.isA ? w.n1 + g : (g < w.n1 ? g : g - w.n1); }
template <int X, bool DMA> DI void setup(AT& c, const ATW& w, int g) {
    c.vcur = w.lds + AT_V + w.vs * 32768 + w.lane * 16;
    if constexpr (DMA) {
        const int v2 = w.vs == 0 ? 2 : w.vs - 1, tn = tile_of(w, g + 2);
        c.gk = w.Kh + (size_t)tn * 32768; c.gv = w.Vh + (size_t)tn * 32768; c.m0k = w.mk + X * 32768; c.m0v = w.mv + v2 * 32768;
    }
}
template <bool LAST> DI void pair(AT& c, ATW& w, int g0) {
    setup<0, true>(c, w, g0);
    const bool seam = !w.isA && (g0 + 1 == w.n1);
    c.first = seam;
    const float m_old = c.m;
    if (seam) { a_qmovs(make_iseq<64>{}); NOPS2(); }
    if constexpr (LAST) build_mask(c, w.qrel);
    SBAR();
    phaseA<0, LAST ? 7 : 3>(c, make_iseq<32>{});
    phaseB<0, 3>(c, make_iseq<32>{});
    rescale_o(c);
    if (seam) {
        NOPS16();
        o_stores((unsigned)(w.wid * 32768 + w.lane * 16), w.part, make_iseq<32>{});
        const float lt = halfsum(c.l);
        if (w.hi == 0) __hip_atomic_store((unsigned long long*)(w.mlp + w.wid * 32 + w.r32), ((unsigned long long)__float_as_uint(lt) << 32) | __float_as_uint(m_old), RLX_AGENT);
        NOPS2();
        a_zeros(make_iseq<128>{});
        NOPS2();
        c.l = 0.f;
    }
    c.drain = seam; c.first = false;
    w.vs = w.vs == 2 ? 0 : w.vs + 1;
    setup<1, !LAST>(c, w, g0 + 1);
    if constexpr (LAST) build_mask(c, w.qrel - 64);
    SBAR();
    phaseA<1, LAST ? 5 : 3>(c, make_iseq<32>{});
    phaseB<1, LAST ? 1 : 3>(c, make_iseq<32>{});
    rescale_o(c);
    if (seam) { if (threadIdx.x == 0) __hip_atomic_store(w.flag, 1u, RLX_AGENT); }
    w.vs = w.vs == 2 ? 0 : w.vs + 1;
}

__global__ __launch_bounds__(256, 1) void k2_attn(unsigned char* ws) {
    extern __shared__ __attribute__((aligned(16))) unsigned char shm[];
    ldsp lds = (ldsp)shm;
    const int tid = threadIdx.x, wid = rfl(tid >> 6), lane = tid & 63, r32 = lane & 31, hi = lane >> 5;
    const int b = blockIdx.x, xcd = b & 7, j = b >> 3, head = 2 * xcd + (j >> 4), w16 = j & 15;
    const bool isA = w16 >= 8; const int i = w16 & 7, bq = 15 - i;
    const int seg = head * 8 + i;
    ATW w;
    w.Kh = (const char*)ws + WS_K + (size_t)head * MB; w.Vh = (const char*)ws + WS_V + (size_t)head * MB;
    const char* Qh = (const char*)ws + WS_Q + (size_t)head * MB;
    w.lds = lds; w.n1 = 15 - 2 * i; w.vs = 0; w.lane = lane; w.wid = wid; w.r32 = r32; w.hi = hi; w.isA = isA; w.qrel = wid * 32 + r32 - 4 * hi;
    w.part = (const char*)ws + WS_PART + (size_t)seg * 131072;
    w.mlp = (f32x2*)(ws + WS_ML) + seg * 128;
    w.flag = (unsigned*)(ws + WS_FLAGS) + seg * FLAG_STRIDE;
    AT c;
    const unsigned lds0 = (unsigned)(uintptr_t)lds;
    c.goff = (unsigned)wid * 8192u + (unsigned)lane * 16u;
    c.kb = lds + AT_K + lane * 16;
    w.mk = lds0 + AT_K + wid * 8192; w.mv = lds0 + AT_V + wid * 8192;
    asm volatile("s_nop 4" ::: ACLOB);
    dma_tile(w.mk, c.goff, w.Kh + (size_t)tile_of(w, 0) * 32768);
    q_loads<0>((unsigned)lane * 16u, Qh + (size_t)(bq * 4 + wid) * 16384, make_iseq<16>{});
    dma_tile(w.mk + 32768, c.goff, w.Kh + (size_t)tile_of(w, 1) * 32768);
    dma_tile(w.mv, c.goff, w.Vh + (size_t)tile_of(w, 0) * 32768);
    dma_tile(w.mv + 32768, c.goff, w.Vh + (size_t)tile_of(w, 1) * 32768);
    q_loads<16>((unsigned)lane * 16u, Qh + (size_t)(i * 4 + wid) * 16384, make_iseq<16>{});
    a_zeros(make_iseq<128>{});
    c.cm[0] = f32x16{}; c.cm[1] = f32x16{}; c.m = 0.f; c.l = 0.f; c.rs0 = 0.f; c.rs1 = 0.f; c.alpha = 1.f; c.pend = false; c.drain = false;
    c.aone = hi ? 0.f : 1.f;
    PIN(c.cm[0]);
    asm volatile("s_waitcnt vmcnt(40)\n\ts_barrier" ::: "memory");
    SBAR();
    c.fr[0] = kfrag<0, 0>(c); c.fr[1] = kfrag<0, 1>(c); c.fr[2] = kfrag<0, 2>(c); c.fr[3] = kfrag<0, 3>(c); c.fr[4] = kfrag<0, 4>(c); c.fr[5] = kfrag<0, 5>(c);
    SBAR();
    phaseA<1, 0>(c, make_iseq<32>{});
    SBAR(); asm volatile("s_nop 7\n\ts_nop 7" : "+v"(c.s[0][0]), "+v"(c.s[0][1])); SBAR();
    c.first = true;
    maxsteps1(c, make_iseq<8>{});
    decide<1>(c);
    exps_from<0, 0>(c, make_iseq<NEB>{});
    asm volatile("s_waitcnt vmcnt(0) lgkmcnt(0)\n\ts_barrier" ::: "memory");
    SBAR();
    c.fr[0] = kfrag<1, 0>(c); c.fr[1] = kfrag<1, 1>(c); c.fr[2] = kfrag<1, 2>(c); c.fr[3] = kfrag<1, 3>(c); c.fr[4] = kfrag<1, 4>(c); c.fr[5] = kfrag<1, 5>(c);
    for (int g0 = 0; g0 < 14; g0 += 2) pair<false>(c, w, g0);
    pair<true>(c, w, 14);
    c.vcur = lds + AT_V + w.vs * 32768 + lane * 16;
    c.fr[0] = vfrag<0>(c.vcur); c.fr[1] = vfrag<1>(c.vcur); c.fr[2] = vfrag<2>(c.vcur); c.fr[3] = vfrag<3>(c.vcur); c.fr[4] = vfrag<4>(c.vcur); c.fr[5] = vfrag<5>(c.vcur);
    exps_from<0, NEB>(c, make_iseq<32 - NEB>{});
    packs<0>(c, make_iseq<16>{});
    c.l += c.rs0 + c.rs1;
    PIN(c.p[0]); PIN(c.p[1]); PIN(c.p[2]); PIN(c.p[3]);
    SBAR(); NOPS2(); SBAR();
    phaseB<0, 0>(c, make_iseq<32>{});
    SBAR(); NOPS16(); SBAR();
    const float lt = halfsum(c.l);
    float f1, f2 = 0.f;
    if (isA) {
        if (wid == 0) {
            unsigned spins = 0;
            while (rfl(__hip_atomic_load(w.flag, RLX_AGENT)) == 0u) { __builtin_amdgcn_s_sleep(2); if (++spins > (1u << 22)) break; }
            __builtin_amdgcn_fence(__ATOMIC_ACQUIRE, "agent");
            asm volatile("s_waitcnt vmcnt(0)" ::: "memory");
        }
        __syncthreads();
        const f32x2 ml2 = w.mlp[wid * 32 + r32];
        const float mn = __builtin_fmaxf(c.m, ml2[0]);
        const float w1 = ex2(c.m - mn), w2 = ex2(ml2[0] - mn);
        const float inv = 1.0f / (lt * w1 + ml2[1] * w2);
        f1 = w1 * inv; f2 = w2 * inv;
    } else {
        f1 = 1.0f / lt;
    }
    {
        const int row = (isA ? bq : i) * 128 + wid * 32 + r32;
        unsigned char* cp = ws + WS_CTX + ((size_t)(row >> 4) * 128 + head * 8) * 1024 + hi * 256 + (row & 15) * 16;
        const f32x4* pp = (const f32x4*)(w.part + wid * 32768 + lane * 16);
        f32x4 pv[32];
        if (isA) {
#pragma unroll
            for (int q = 0; q < 32; ++q) pv[q] = pp[q * 64];
        } else {
#pragma unroll
            for (int q = 0; q < 32; ++q) pv[q] = (f32x4){0.f, 0.f, 0.f, 0.f};
        }
        auto fin = [&](auto dtag) {
            constexpr int d = decltype(dtag)::value;
            float o[16]; a_reads<16 * d>(o, make_iseq<16>{});
#pragma unroll
            for (int s2 = 0; s2 < 2; ++s2) {
                u32x4 wv;
#pragma unroll
                for (int e = 0; e < 4; ++e) { const int r = 8 * s2 + 2 * e;
                    wv[e] = cvtpk(o[r] * f1 + pv[4 * d + (r >> 2)][r & 3] * f2, o[r + 1] * f1 + pv[4 * d + ((r + 1) >> 2)][(r + 1) & 3] * f2); }
                *(u32x4*)(cp + d * 1024 + s2 * 512) = wv;
            }
        };
        fin(std::integral_constant<int, 0>{}); fin(std::integral_constant<int, 1>{}); fin(std::integral_constant<int, 2>{}); fin(std::integral_constant<int, 3>{});
        fin(std::integral_constant<int, 4>{}); fin(std::integral_constant<int, 5>{}); fin(std::integral_constant<int, 6>{}); fin(std::integral_constant<int, 7>{});
    }
}

constexpr int OP_WB = 128 * 224, OP_CB = 32768, OP_STAGE = OP_WB + OP_CB, OP_LDS = 2 * OP_STAGE;

DI void dma_v(unsigned m0v, unsigned goff, const char* g) {
    asm volatile("s_mov_b32 m0, %0\n\ts_nop 0\n\tglobal_load_lds_dwordx4 %1, %2" :: "s"(m0v), "v"(goff), "s"(g) : "memory");
}

__global__ __launch_bounds__(256, 1) void k3_oproj(unsigned char* ws, const bf16_t* wo, const bf16_t* x, bf16_t* out) {
    extern __shared__ __attribute__((aligned(16))) unsigned char shm[];
    ldsp lds = (ldsp)shm;
    const int tid = threadIdx.x, wid = rfl(tid >> 6), lane = tid & 63, fr = lane & 15, fq = lane >> 4;
    const int b = blockIdx.x, xcd = b & 7, j = b >> 3, sk = xcd & 3, rb = 8 * (xcd >> 2) + (j >> 2), cn = j & 3;
    const int grp = rb * 4 + cn;
    const unsigned lds0 = (unsigned)(uintptr_t)lds;
    unsigned wv[7];
#pragma unroll
    for (int p = 0; p < 7; ++p) { const int ci = (wid * 7 + p) * 64 + lane, k = ci / 14, cc = ci - k * 14; wv[p] = (unsigned)(k * DM + cc * 8) * 2u; }
    const char* wbase = (const char*)wo + ((size_t)sk * 1024 * DM + cn * 112) * 2;
    const char* cbase = (const char*)ws + WS_CTX + ((size_t)(rb * 8 + 2 * wid) * 128 + sk * 32) * 1024;
    const unsigned cv = (unsigned)lane * 16u;
    auto stage = [&](int s, int slot) {
        const unsigned lw = lds0 + slot * OP_STAGE + wid * 7168, lc = lds0 + slot * OP_STAGE + OP_WB + wid * 8192;
        const char* wg = wbase + (size_t)s * 128 * DM * 2;
#pragma unroll
        for (int p = 0; p < 7; ++p) dma_v(lw + p * 1024, wv[p], wg);
#pragma unroll
        for (int tb = 0; tb < 2; ++tb) {
            const char* cg = cbase + (size_t)tb * 131072 + s * 4096;
            dma_piece(lc + tb * 4096, cv, cg, 0); dma_piece(lc + tb * 4096, cv, cg, 1); dma_piece(lc + tb * 4096, cv, cg, 2); dma_piece(lc + tb * 4096, cv, cg, 3);
        }
    };
    f32x4 acc[2][7];
#pragma unroll
    for (int tb = 0; tb < 2; ++tb)
#pragma unroll
        for (int cb = 0; cb < 7; ++cb) acc[tb][cb] = (f32x4){0.f, 0.f, 0.f, 0.f};
    const int woff = (16 * (fq >> 1) + 4 * (fq & 1) + (fr >> 2)) * 224 + (fr & 3) * 8;
    stage(0, 0); stage(1, 1);
    for (int s = 0; s < 8; ++s) {
        if (s < 7) asm volatile("s_waitcnt vmcnt(15)\n\ts_barrier" ::: "memory"); else asm volatile("s_waitcnt vmcnt(0)\n\ts_barrier" ::: "memory");
        ldscp wp = lds + (s & 1) * OP_STAGE + woff;
        ldscp cp = lds + (s & 1) * OP_STAGE + OP_WB + wid * 8192 + lane * 16;
#pragma unroll
        for (int kk = 0; kk < 4; ++kk) {
            const bf16x8 c0 = ldsrd16(cp + kk * 1024), c1 = ldsrd16(cp + 4096 + kk * 1024);
#pragma unroll
            for (int cb = 0; cb < 7; ++cb) {
                const bf16x8 w = cat8(trread(wp + kk * 7168 + cb * 32), trread(wp + kk * 7168 + cb * 32 + 1792));
                acc[0][cb] = MFMA16(w, c0, acc[0][cb]);
                acc[1][cb] = MFMA16(w, c1, acc[1][cb]);
            }
        }
        asm volatile("s_waitcnt lgkmcnt(0)\n\ts_barrier" ::: "memory");
        if (s + 2 < 8) stage(s + 2, s & 1);
    }
    float* slab = (float*)(ws + WS_OPART) + (size_t)grp * 4 * 14336;
    const __amdgpu_buffer_rsrc_t srs = __builtin_amdgcn_make_buffer_rsrc((void*)slab, (short)0, 4 * 57344, 0x00020000);
#pragma unroll
    for (int tb = 0; tb < 2; ++tb)
#pragma unroll
        for (int cb = 0; cb < 7; ++cb)
            __builtin_amdgcn_raw_buffer_store_b128(__builtin_bit_cast(u32x4, acc[tb][cb]), srs, (((sk * 4 + wid) * 14 + tb * 7 + cb) * 64 + lane) * 16, 0, 16);
    asm volatile("s_waitcnt vmcnt(0)" ::: "memory");
    __syncthreads();
    unsigned* cnt = (unsigned*)(ws + WS_FLAGS) + (N_AFLAGS + grp) * FLAG_STRIDE;
    if (wid == 0) {
        if (lane == 0) __hip_atomic_fetch_add(cnt, 1u, RLX_AGENT);
        unsigned spins = 0;
        while (rfl(__hip_atomic_load(cnt, RLX_AGENT)) < 4u) { __builtin_amdgcn_s_sleep(1); if (++spins > (1u << 22)) break; }
        __builtin_amdgcn_fence(__ATOMIC_ACQUIRE, "agent");
        asm volatile("s_waitcnt vmcnt(0)" ::: "memory");
    }
    __syncthreads();
    const float* gp = (const float*)(ws + WS_G);
    const bf16_t* y2p = (const bf16_t*)(ws + WS_Y2);
    for (int f = wid; f < 14; f += 4) {
        const int tb = f / 7, cb = f - tb * 7;
        f32x4 sum = {0.f, 0.f, 0.f, 0.f};
#pragma unroll
        for (int q = 0; q < 4; ++q) sum += *(const f32x4*)(slab + ((size_t)((q * 4 + sk) * 14 + f) * 64 + lane) * 4);
        const int t = rb * 128 + sk * 32 + tb * 16 + fr, c = cn * 112 + cb * 16 + 4 * fq;
        const float g = gp[t], og = rbf(1.0f - g);
        const u32x2 xw = *(const u32x2*)(x + (size_t)t * DM + c), yw = *(const u32x2*)(y2p + (size_t)t * DM + c);
        const float xv[4] = {bf_lo(xw[0]), bf_hi(xw[0]), bf_lo(xw[1]), bf_hi(xw[1])};
        const float yv[4] = {bf_lo(yw[0]), bf_hi(yw[0]), bf_lo(yw[1]), bf_hi(yw[1])};
        float r[4];
#pragma unroll
        for (int e = 0; e < 4; ++e) r[e] = rbf(g * rbf(xv[e] * yv[e])) + rbf(og * rbf(sum[e]));
        u32x2 w = {cvtpk(r[0], r[1]), cvtpk(r[2], r[3])};
        *(u32x2*)(out + (size_t)t * DM + c) = w;
    }
}

extern "C" void kernel_launch(void* const* d_in, const int* in_sizes, int n_in, void* d_out, int out_size, void* d_ws, size_t ws_size,
                              hipStream_t stream) {
    (void)in_sizes; (void)n_in; (void)out_size; (void)ws_size;
    (void)hipFuncSetAttribute((const void*)k1_proj, hipFuncAttributeMaxDynamicSharedMemorySize, K1_LDS);
    (void)hipFuncSetAttribute((const void*)k2_attn, hipFuncAttributeMaxDynamicSharedMemorySize, AT_LDS);
    (void)hipFuncSetAttribute((const void*)k3_oproj, hipFuncAttributeMaxDynamicSharedMemorySize, OP_LDS);
    K1Args P;
    P.x = (const bf16_t*)d_in[0]; P.ak = (const bf16_t*)d_in[1]; P.av = (const bf16_t*)d_in[2]; P.shift_k = (const bf16_t*)d_in[3];
    P.ssm_k = (const bf16_t*)d_in[4]; P.ssm_d = (const bf16_t*)d_in[5]; P.ckv = (const bf16_t*)d_in[7]; P.wq = (const bf16_t*)d_in[8];
    P.wuk = (const bf16_t*)d_in[9]; P.wuv = (const bf16_t*)d_in[10]; P.gate_w = (const bf16_t*)d_in[12]; P.ws = (unsigned char*)d_ws;
#define DEV_STAGES 7
    if (DEV_STAGES & 1) k1_proj<<<256, 512, K1_LDS, stream>>>(P);
    if (DEV_STAGES & 2) k2_attn<<<256, 256, AT_LDS, stream>>>((unsigned char*)d_ws);
    if (DEV_STAGES & 4) k3_oproj<<<256, 256, OP_LDS, stream>>>((unsigned char*)d_ws, (const bf16_t*)d_in[11], (const bf16_t*)d_in[0], (bf16_t*)d_out);
}
```

```cpp
#include <hip/hip_runtime.h>
#include <hip/hip_bf16.h>
#include <stdint.h>
#include <utility>

#define LAS __attribute__((address_space(3)))
#define GAS __attribute__((address_space(1)))
#define CAS __attribute__((address_space(4)))
#define DI __device__ __forceinline__
typedef unsigned short bf16_t;
typedef short bf16x8 __attribute__((ext_vector_type(8)));
typedef short s16x4 __attribute__((ext_vector_type(4)));
typedef float f32x2 __attribute__((ext_vector_type(2)));
typedef float f32x4 __attribute__((ext_vector_type(4)));
typedef float f32x16 __attribute__((ext_vector_type(16)));
typedef unsigned u32x2 __attribute__((ext_vector_type(2)));
typedef unsigned u32x4 __attribute__((ext_vector_type(4)));
typedef __bf16 bfx2 __attribute__((ext_vector_type(2)));
typedef LAS unsigned char* ldsp;
typedef const LAS unsigned char* ldscp;

constexpr int SEQ = 2048, DM = 448, NH = 16, HD = 256, DC = 1024, NW_ = 4096;

constexpr size_t MB = 1024 * 1024;
constexpr size_t WS_Q = 0, WS_K = 16 * MB, WS_V = 32 * MB, WS_CTX = 48 * MB, WS_PART = 64 * MB, WS_ML = 80 * MB,
                 WS_Y2 = 81 * MB, WS_G = 84 * MB, WS_OPART = 85 * MB, WS_FLAGS = 100 * MB, WS_WI = 102 * MB;
constexpr int FLAG_STRIDE = 64;
constexpr int N_AFLAGS = 128, N_OFLAGS = 64;

#define TS(wsp, kern, slot) do { } while (0)
#define TSI(wsp, idx) do { } while (0)
#define SBAR() __builtin_amdgcn_sched_barrier(0)
#define PIN(x) asm volatile("" : "+v"(x))
#define RLX_AGENT __ATOMIC_RELAXED, __HIP_MEMORY_SCOPE_AGENT

DI unsigned cvtpk(float lo, float hi) { f32x2 v = {lo, hi}; bfx2 b = __builtin_convertvector(v, bfx2); return __builtin_bit_cast(unsigned, b); }
DI float bf_lo(unsigned w) { return __uint_as_float(w << 16); }
DI float bf_hi(unsigned w) { return __uint_as_float(w & 0xffff0000u); }
DI float rbf(float x) { return bf_lo(cvtpk(x, 0.f)); }
DI unsigned rfl(unsigned x) { return __builtin_amdgcn_readfirstlane(x); }
DI s16x4 trread(ldscp p) { return __builtin_amdgcn_ds_read_tr16_b64_v4i16((LAS s16x4*)p); }
DI bf16x8 cat8(s16x4 lo, s16x4 hi) { return __builtin_shufflevector(lo, hi, 0, 1, 2, 3, 4, 5, 6, 7); }
DI bf16x8 ldsrd16(ldscp p) { return *(const LAS bf16x8*)p; }
#define MFMA16(a, b, c) __builtin_amdgcn_mfma_f32_16x16x32_bf16((a), (b), (c), 0, 0, 0)
#define MFMA32(a, b, c) __builtin_amdgcn_mfma_f32_32x32x16_bf16((a), (b), (c), 0, 0, 0)

constexpr int HTB = 128 * 64 * 2;
constexpr int K1_X = 8 * HTB;
constexpr int K1_LDS = K1_X + 16384 + 2048;

DI int lds_byte(int r, int c) { const int st = (r >> 4) * 2 + (c >> 5), rr = r & 15, cc = c & 31, ob = rr * 64 + cc * 2; return st * 1024 + (ob ^ (((ob >> 9) & 1) << 5)); }
DI void stage_rc(int b, int& R, int& C) { const int st = b / 1024, sb = b % 1024, swz = sb ^ (((sb >> 9) & 1) << 5); R = (st >> 1) * 16 + swz / 64; C = (st & 1) * 32 + (swz % 64) / 2; }

struct K1Args {
    const bf16_t *x, *ak, *av, *shift_k, *ssm_k, *ssm_d, *ckv, *wq, *wuk, *wuv, *gate_w, *wo;
    unsigned char* ws;
};

#define SA(b, h) (((b) * 2 + (h)) * HTB)
#define SB(b, h) ((4 + (b) * 2 + (h)) * HTB)
constexpr int ring_slot(int u) {
    switch (u & 7) { case 5: return SB(0, 0); case 6: return SA(0, 0); case 7: return SB(0, 1); case 0: return SA(0, 1);
                     case 1: return SB(1, 0); case 2: return SA(1, 0); case 3: return SB(1, 1); default: return SA(1, 1); }
}
struct G1 {
    ldsp lds; unsigned ldsw  , voffA_kv, voffA_q, voffB, sv; int aoff, boff0, boff1;
    const char *A_kv, *A_q, *B_kv, *B_q, *sbase; unsigned sstep, sdst, sdstep; int sadd;
};
DI int lane_id() { int l; asm volatile("v_mbcnt_lo_u32_b32 %0, -1, 0\n\tv_mbcnt_hi_u32_b32 %0, -1, %0" : "=v"(l)); return l; }
template <int OFF> DI void dma16(unsigned lbase, unsigned voff, const char* g) {
    asm volatile("s_add_i32 m0, %0, %c3\n\ts_nop 0\n\tglobal_load_lds_dwordx4 %1, %2" :: "s"(lbase), "v"(voff), "s"(g), "i"(OFF) : "memory", "scc");
}
template <int BUF, int STRIDE, int H> DI void stage_a(const G1& g, const char* base, unsigned voff) {
    dma16<BUF>(g.ldsw, voff, base + H * 128 * STRIDE); dma16<BUF + 8192>(g.ldsw, voff, base + (H * 128 + 64) * STRIDE);
}
template <int BUF, int H> DI void stage_b(const G1& g, const char* base) {
    dma16<BUF>(g.ldsw, g.voffB, base + H * 256); dma16<BUF + 8192>(g.ldsw, g.voffB, base + H * 256 + 32 * NW_ * 2);
}
template <int U> DI void stage_unit(const G1& g) {
    if constexpr (U < 21) {
        constexpr int t = U / 3, r = U % 3;
        if constexpr (r == 0) stage_b<ring_slot(U), 0>(g, g.B_q + (size_t)t * (64 * NW_ * 2));
        else stage_a<ring_slot(U), DM * 2, r - 1>(g, g.A_q + t * 128, g.voffA_q);
    } else {
        constexpr int t = (U - 21) >> 2, r = (U - 21) & 3;
        if constexpr ((r & 1) == 0) stage_b<ring_slot(U), (r >> 1)>(g, g.B_kv + (size_t)t * (64 * NW_ * 2));
        else stage_a<ring_slot(U), DC * 2, (r >> 1)>(g, g.A_kv + t * 128, g.voffA_kv);
    }
}
DI void side(const G1& g, int t) {
    const int tt = t + g.sadd > 15 ? 15 : t + g.sadd;
    asm volatile("s_mov_b32 m0, %0\n\ts_nop 0\n\tglobal_load_lds_dword %1, %2" :: "s"(g.sdst + (unsigned)t * g.sdstep), "v"(g.sv), "s"(g.sbase + rfl((unsigned)tt * g.sstep)) : "memory");
}
DI void touch(unsigned sink, unsigned voff, const char* g) {
    asm volatile("s_mov_b32 m0, %0\n\ts_nop 0\n\tglobal_load_lds_dword %1, %2" :: "s"(sink), "v"(voff), "s"(g) : "memory");
}
DI void lda(bf16x8 (&dst)[4][2], const G1& g, int buf) {
#pragma unroll
    for (int m = 0; m < 4; ++m)
#pragma unroll
        for (int k = 0; k < 2; ++k) dst[m][k] = ldsrd16(g.lds + buf + g.aoff + m * 2048 + k * 1024);
}
DI void ldb(bf16x8 (&dst)[2][2], const G1& g, int buf) {
#pragma unroll
    for (int n = 0; n < 2; ++n)
#pragma unroll
        for (int k = 0; k < 2; ++k) { ldscp p = g.lds + buf + (n ? g.boff1 : g.boff0) + k * 8192; dst[n][k] = cat8(trread(p), trread(p + 256)); }
}
template <bool VT> DI void mma(f32x4 (&acc)[4][2], const bf16x8 (&At)[4][2], const bf16x8 (&Bt)[2][2]) {
    __builtin_amdgcn_s_setprio(1);
#pragma unroll
    for (int m = 0; m < 4; ++m)
#pragma unroll
        for (int n = 0; n < 2; ++n)
#pragma unroll
            for (int k = 0; k < 2; ++k) acc[m][n] = VT ? MFMA16(At[m][k], Bt[n][k], acc[m][n]) : MFMA16(Bt[n][k], At[m][k], acc[m][n]);
    __builtin_amdgcn_s_setprio(0);
}
#define WAIT_V(n) asm volatile("s_waitcnt vmcnt(" #n ")" ::: "memory")
#define WAIT_L(n) asm volatile("s_waitcnt lgkmcnt(" #n ")" ::: "memory")
#define BAR __builtin_amdgcn_s_barrier()
DI void zero8(f32x4 (&a)[4][2]) {
#pragma unroll
    for (int m = 0; m < 4; ++m) { a[m][0] = (f32x4){0.f, 0.f, 0.f, 0.f}; a[m][1] = (f32x4){0.f, 0.f, 0.f, 0.f}; }
}

DI void pack_kq(const f32x4 (&acc)[4][2], u32x4* pk, float scale) {
#pragma unroll
    for (int m = 0; m < 4; ++m) {
        const f32x4 a0 = acc[m][0] * scale, a1 = acc[m][1] * scale;
        const unsigned w0 = cvtpk(a0[0], a0[1]), w1 = cvtpk(a0[2], a0[3]), x0 = cvtpk(a1[0], a1[1]), x1 = cvtpk(a1[2], a1[3]);
        auto r0 = __builtin_amdgcn_permlane16_swap(w0, x0, false, false);
        auto r1 = __builtin_amdgcn_permlane16_swap(w1, x1, false, false);
        pk[m] = (u32x4){r0[0], r1[0], r0[1], r1[1]};
    }
}
DI unsigned kq_off(int pm, int wr, int wc, int fr, int fq) {
    return (unsigned)((pm * 8 + wr * 2) * 16384 + fr * 16 + (2 * wc + (fq & 1)) * 1024 + (fq >> 1) * 512);
}
DI void pack_v(const f32x4 (&acc)[4][2], u32x4* pk) {
#pragma unroll
    for (int n = 0; n < 2; ++n)
#pragma unroll
        for (int mm = 0; mm < 2; ++mm) {
            const f32x4 a0 = acc[2 * mm][n], a1 = acc[2 * mm + 1][n];
            const unsigned w0 = cvtpk(a0[0], a0[1]), w1 = cvtpk(a0[2], a0[3]), x0 = cvtpk(a1[0], a1[1]), x1 = cvtpk(a1[2], a1[3]);
            auto r0 = __builtin_amdgcn_permlane32_swap(w0, x0, false, false);
            auto r1 = __builtin_amdgcn_permlane32_swap(w1, x1, false, false);
            pk[2 * mm + n] = (u32x4){r0[0], r1[0], r0[1], r1[1]};
        }
}
#define ST_AUX 16
struct Out1 { u32x4 pk[16]; __amdgpu_buffer_rsrc_t rs; unsigned voff; int sbj, shi; };
template <int I> DI void st_piece(const Out1& o) {
    __builtin_amdgcn_raw_buffer_store_b128(o.pk[I], o.rs, o.voff + (I & 1) * 256, (I >> 3) * 65536 + ((I >> 2) & 1) * o.sbj + ((I >> 1) & 1) * o.shi, ST_AUX);
}
template <int A, int B> DI void st_range(const Out1& o) { if constexpr (A < B) { st_piece<A>(o); st_range<A + 1, B>(o); } }

template <int N, class W> DI void qphase(const G1& g, f32x4 (&a0)[4][2], f32x4 (&a1)[4][2], bf16x8 (&At)[4][2], bf16x8 (&B0)[2][2], W& warm) {
    constexpr int t = N >> 1;
    if constexpr ((N & 1) == 0) {
        ldb(B0, g, ring_slot(3 * t)); SBAR(); lda(At, g, ring_slot(3 * t + 1));
        if constexpr (t < 5) warm(t);
        if constexpr (N >= 2) stage_unit<3 * t + 6>(g);
        if constexpr (N == 0) WAIT_V(10); else WAIT_V(8);
        WAIT_L(8); BAR; WAIT_L(0); mma<false>(a0, At, B0); BAR; SBAR();
    } else {
        lda(At, g, ring_slot(3 * t + 2));
        if constexpr (t >= 1) stage_unit<3 * t + 7>(g);
        stage_unit<3 * t + 8>(g);
        WAIT_V(8);
        if constexpr (N == 13) WAIT_L(0);
        BAR; WAIT_L(0); mma<false>(a1, At, B0); BAR; SBAR();
    }
}
template <class W, int... Ns> DI void qphases(const G1& g, f32x4 (&a0)[4][2], f32x4 (&a1)[4][2], bf16x8 (&At)[4][2], bf16x8 (&B0)[2][2], W& warm, std::integer_sequence<int, Ns...>) {
    (qphase<Ns>(g, a0, a1, At, B0, warm), ...);
}
template <int... Us> DI void stage_units(const G1& g, std::integer_sequence<int, Us...>) { (stage_unit<Us>(g), ...); }

template <bool VT, int POS, class W>
DI void mainpair(const G1& g, int t, f32x4 (&acc)[2][2][4][2], bf16x8 (&At)[4][2], bf16x8 (&B0)[2][2], bf16x8 (&B1)[2][2], W& lastside) {
    const char* a1 = g.A_kv + (t + 1) * 128; const char* a2 = a1 + 128; const char* a3 = a1 + 256;
    const char* b2 = g.B_kv + (size_t)(t + 2) * (64 * NW_ * 2); const char* b3 = b2 + 64 * NW_ * 2;
    ldb(B0, g, SB(0, 0)); SBAR(); lda(At, g, SA(0, 0));
    if constexpr (POS != 0) stage_a<SA(1, 1), DC * 2, 1>(g, a1, g.voffA_kv); else WAIT_V(18);
    WAIT_L(8); BAR; WAIT_L(0); mma<VT>(acc[0][0], At, B0); BAR; SBAR();
    ldb(B1, g, SB(0, 1));
    if constexpr (POS == 2) lastside(0); else side(g, t);
    if constexpr (POS != 2) stage_b<SB(0, 0), 0>(g, b2);
    if constexpr (POS == 0) WAIT_V(19);
    BAR; WAIT_L(0); mma<VT>(acc[0][1], At, B1); BAR;
    lda(At, g, SA(0, 1));
    if constexpr (POS != 2) stage_a<SA(0, 0), DC * 2, 0>(g, a2, g.voffA_kv);
    BAR; WAIT_L(0); mma<VT>(acc[1][0], At, B0); BAR; SBAR();
    if constexpr (POS != 2) stage_b<SB(0, 1), 1>(g, b2);
    if constexpr (POS == 0) WAIT_V(15); else if constexpr (POS == 1) WAIT_V(7); else WAIT_V(1);
    BAR; mma<VT>(acc[1][1], At, B1); BAR;
    ldb(B0, g, SB(1, 0)); SBAR(); lda(At, g, SA(1, 0));
    if constexpr (POS != 2) stage_a<SA(0, 1), DC * 2, 1>(g, a2, g.voffA_kv);
    WAIT_L(8); BAR; WAIT_L(0); mma<VT>(acc[0][0], At, B0); BAR; SBAR();
    ldb(B1, g, SB(1, 1));
    if constexpr (POS == 2) lastside(1); else side(g, t + 1);
    if constexpr (POS != 2) stage_b<SB(1, 0), 0>(g, b3);
    BAR; WAIT_L(0); mma<VT>(acc[0][1], At, B1); BAR;
    lda(At, g, SA(1, 1));
    if constexpr (POS != 2) stage_a<SA(1, 0), DC * 2, 0>(g, a3, g.voffA_kv);
    BAR; WAIT_L(0); mma<VT>(acc[1][0], At, B0); BAR; SBAR();
    if constexpr (POS != 2) { stage_b<SB(1, 1), 1>(g, b3); WAIT_V(7); }
    BAR; mma<VT>(acc[1][1], At, B1); BAR;
}

constexpr int CV_RAW = SA(0, 0);
constexpr int K1_WO = SA(0, 1);
constexpr int CV_HSTR = 4256;
constexpr int CV_H = 2 * HTB;
constexpr int CV_USTR = 4608;
constexpr int CV_U = CV_H + 8 * CV_HSTR;
constexpr int CV_R = CV_U + 2 * CV_USTR;
static_assert(CV_R + 65536 <= K1_LDS && CV_R <= K1_X, "lds");

DI void tail_stage(const G1& g, const K1Args& P, int c0, int e, int wid, int lane) {
    dma16<CV_RAW>(g.ldsw, (unsigned)lane * 16u, (const char*)P.ssm_k + (size_t)c0 * (SEQ * 2) + wid * 1024);
    if (wid < 7) {
        const char* wg = (const char*)P.wo + ((size_t)((e >> 6) * 1024 + (e & 15) * 64) * DM + ((e >> 4) & 3) * 112) * 2;
        { const int ci = wid * 64 + lane, k = ci / 14, cc = ci - k * 14; dma16<K1_WO>(g.ldsw, (unsigned)(k * DM + cc * 8) * 2u, wg); }
        { const int ci = (wid + 7) * 64 + lane, k = ci / 14, cc = ci - k * 14; dma16<K1_WO + 7168>(g.ldsw, (unsigned)(k * DM + cc * 8) * 2u, wg); }
    }
}
DI void wo_image(ldsp lds, const K1Args& P, int e, int wid, int lane) {
    const int fr = lane & 15, fq = lane >> 4;
    if (wid < 7) {
        ldscp wp = lds + K1_WO + (16 * (fq >> 1) + 4 * (fq & 1) + (fr >> 2)) * 224 + (fr & 3) * 8 + wid * 32;
        unsigned char* op = P.ws + WS_WI + (size_t)e * 14336 + wid * 1024 + lane * 16;
#pragma unroll
        for (int kk = 0; kk < 2; ++kk) *(bf16x8*)(op + kk * 7168) = cat8(trread(wp + kk * 7168), trread(wp + kk * 7168 + 1792));
    }
}
DI void conv_copies(ldsp q, const u32x4 d, const u32x2 e) {
    const unsigned a0 = __builtin_amdgcn_alignbit(d[1], d[0], 16), a1 = __builtin_amdgcn_alignbit(d[2], d[1], 16), a2 = __builtin_amdgcn_alignbit(d[3], d[2], 16),
                   a3 = __builtin_amdgcn_alignbit(e[0], d[3], 16), a4 = __builtin_amdgcn_alignbit(e[1], e[0], 16);
    *(LAS u32x4*)(q) = (u32x4){a0, a1, a2, a3};
    *(LAS u32x4*)(q + CV_HSTR) = (u32x4){d[1], d[2], d[3], e[0]};
    *(LAS u32x4*)(q + 2 * CV_HSTR) = (u32x4){a1, a2, a3, a4};
    *(LAS u32x4*)(q + 3 * CV_HSTR) = (u32x4){d[2], d[3], e[0], e[1]};
}
DI void conv_frags(bf16x8 (&A)[8], bf16x8 (&B)[8], ldscp hp, ldscp up) {
#pragma unroll
    for (int v = 0; v < 8; ++v) {
        B[v] = ldsrd16(up + (7 - v) * 512);
        const s16x4 lo = *(const LAS s16x4*)(hp + v * 512), hi = *(const LAS s16x4*)(hp + v * 512 + 8);
        A[v] = cat8(lo, hi);
    }
}
DI void conv_mma(f32x4 (&acc)[8], const bf16x8 (&A)[8], const bf16x8 (&B)[8]) {
#pragma unroll
    for (int J = 0; J < 8; ++J)
#pragma unroll
        for (int v = 0; v <= J; ++v) acc[J] = MFMA16(A[v], B[J - v], acc[J]);
}
DI void conv_part(ldsp lds, const K1Args& P, int c0, const u32x4 sw, const unsigned dw, int wid, int lane, const Out1& o) {
    const int tid = wid * 64 + lane, fr = lane & 15, fq = lane >> 4;
    {
        const int cq = tid & 255;
        const u32x4 d = *(const LAS u32x4*)(lds + CV_RAW + tid * 16);
        u32x2 e = *(const LAS u32x2*)(lds + CV_RAW + tid * 16 + 16);
        const u32x4 z4 = {0u, 0u, 0u, 0u}; const u32x2 z2 = {0u, 0u};
        if (cq == 255) e = z2;
        ldsp q = lds + CV_H + (tid >> 8) * 4 * CV_HSTR + (cq + 2) * 16;
        conv_copies(q, d, e);
        if (cq == 0) { conv_copies(q - 16, z4, (u32x2){d[0], d[1]}); conv_copies(q - 32, z4, z2); }
        if (cq == 255) { conv_copies(q + 16, z4, z2); conv_copies(q + 32, z4, z2); }
    }
    SBAR(); st_range<4, 6>(o); SBAR();
    {
        ldscp kp = lds + K1_X + tid * 16;
        u32x4 a = *(const LAS u32x4*)(kp - 16);
        const u32x4 b = *(const LAS u32x4*)(kp), vr = *(const LAS u32x4*)(kp + 8192);
        if (tid == 0) a = (u32x4){0u, 0u, 0u, 0u};
        const unsigned kr[7] = {a[1], a[2], a[3], b[0], b[1], b[2], b[3]};
#pragma unroll
        for (int c = 0; c < 2; ++c) {
            const float w0 = bf_lo(sw[2 * c]), w1 = bf_hi(sw[2 * c]), w2 = bf_lo(sw[2 * c + 1]), w3 = bf_hi(sw[2 * c + 1]);
            float kc[7], kv[4];
#pragma unroll
            for (int i = 0; i < 7; ++i) kc[i] = c ? bf_hi(kr[i]) : bf_lo(kr[i]);
#pragma unroll
            for (int i = 0; i < 4; ++i) {
                float s = rbf(kc[i] * w3);
                s = rbf(s + rbf(kc[i + 1] * w2));
                s = rbf(s + rbf(kc[i + 2] * w1));
                s = rbf(s + rbf(kc[i + 3] * w0));
                kv[i] = s * (c ? bf_hi(vr[i]) : bf_lo(vr[i]));
            }
            u32x2 wv = {cvtpk(kv[3], kv[2]), cvtpk(kv[1], kv[0])};
            *(LAS u32x2*)(lds + CV_U + c * CV_USTR + (2044 - 4 * tid) * 2) = wv;
            SBAR(); if (c == 0) st_range<6, 8>(o); else st_range<8, 10>(o); SBAR();
        }
        if (tid < 120) { const int c = tid / 60, j = tid - c * 60; *(LAS u32x2*)(lds + CV_U + c * CV_USTR + 4096 + j * 8) = (u32x2){0u, 0u}; }
    }
    __syncthreads();
    TS(P.ws, 0, 5);
    const int ch = wid >> 2, qw = wid & 3;
    {
        const int y1 = 24 + fr - 8 * fq;
        ldscp hb = lds + CV_H + ch * 4 * CV_HSTR + (y1 & 3) * CV_HSTR + (y1 & ~3) * 2 + qw * 128;
        ldscp ub = lds + CV_U + ch * CV_USTR + (2056 - 256 * 7 - 16 * fr - 8 * fq) * 2 + qw * 128;
        f32x4 acc[8];
#pragma unroll
        for (int i = 0; i < 8; ++i) acc[i] = (f32x4){0.f, 0.f, 0.f, 0.f};
        bf16x8 A0[8], B0[8], A1[8], B1[8];
        conv_frags(A0, B0, hb, ub); SBAR();
        conv_frags(A1, B1, hb + 64, ub + 64); SBAR();
        st_range<10, 12>(o); SBAR();
        conv_mma(acc, A0, B0); SBAR();
        st_range<12, 14>(o); SBAR();
        conv_mma(acc, A1, B1); SBAR();
        st_range<14, 16>(o); SBAR();
#pragma unroll
        for (int i = 0; i < 8; ++i) *(LAS f32x4*)(lds + CV_R + wid * 8192 + i * 1024 + lane * 16) = acc[i];
    }
    __syncthreads();
    TS(P.ws, 0, 6);
    {
        const float dd = ch ? bf_hi(dw) : bf_lo(dw);
#pragma unroll
        for (int i = 0; i < 2; ++i) {
            ldscp rp = lds + CV_R + ch * 32768 + (2 * qw + i) * 1024 + lane * 16;
            const f32x4 y = ((*(const LAS f32x4*)rp + *(const LAS f32x4*)(rp + 8192)) + *(const LAS f32x4*)(rp + 16384)) + *(const LAS f32x4*)(rp + 24576);
            const int t = 256 * (2 * qw + i) + 16 * fr + 4 * fq;
            const u32x2 uw = *(const LAS u32x2*)(lds + CV_U + ch * CV_USTR + (2044 - t) * 2);
            const float kv[4] = {bf_hi(uw[1]), bf_lo(uw[1]), bf_hi(uw[0]), bf_lo(uw[0])};
            float r[4];
#pragma unroll
            for (int e = 0; e < 4; ++e) r[e] = rbf(y[e]) + rbf(kv[e] * dd);
            u32x2 w = {cvtpk(r[0], r[1]), cvtpk(r[2], r[3])};
            *(u32x2*)(P.ws + WS_Y2 + ((size_t)(c0 + ch) * SEQ + t) * 2) = w;
        }
    }
}

DI void gate_part(const K1Args& P, int row0, int wid, int lane) {
    const int lc = lane < 56 ? lane : 0;
    u32x4 gw = *(const u32x4*)(P.gate_w + lc * 8);
    float* gp = (float*)(P.ws + WS_G);
    u32x4 xv[8];
#pragma unroll
    for (int r = 0; r < 8; ++r) xv[r] = *(const u32x4*)(P.x + (size_t)(row0 + wid * 8 + r) * DM + lc * 8);
    float mine = 0.f;
#pragma unroll
    for (int r = 0; r < 8; ++r) {
        float s = 0.f;
#pragma unroll
        for (int j = 0; j < 4; ++j) { s += rbf(bf_lo(xv[r][j]) * bf_lo(gw[j])); s += rbf(bf_hi(xv[r][j]) * bf_hi(gw[j])); }
        if (lane >= 56) s = 0.f;
#pragma unroll
        for (int o = 32; o >= 1; o >>= 1) s += __shfl_xor(s, o);
        if (lane == r) mine = s;
    }
    const float z = rbf(mine);
    const float g = rbf(1.0f / (1.0f + __expf(-z)));
    if (lane < 8) gp[row0 + wid * 8 + lane] = g;
}

template <bool VT>
DI void big_tile(const G1& g, const K1Args& P, int pm, int head, int c0, int e, int wid, bf16x8 (&At)[4][2], bf16x8 (&B0)[2][2], Out1& o) {
    f32x4 acc[2][2][4][2];
    zero8(acc[0][0]); zero8(acc[0][1]); zero8(acc[1][0]); zero8(acc[1][1]);
    bf16x8 B1[2][2];
    auto lastside = [&](int s) {
        if (wid < 4) side(g, 14 + s);
        else if (s == 0) {
            const unsigned l = (unsigned)lane_id();
            if (wid == 4) touch(g.sdst, l * 128u, (const char*)P.ssm_k + (size_t)c0 * (SEQ * 2));
            else touch(g.sdst, l * (DM * 2), (const char*)P.wo + ((size_t)((e >> 6) * 1024 + (e & 15) * 64) * DM + ((e >> 4) & 3) * 112) * 2 + (wid - 5) * 110);
        }
    };
    mainpair<VT, 0>(g, 0, acc, At, B0, B1, lastside);
    for (int t = 2; t < 14; t += 2) mainpair<VT, 1>(g, t, acc, At, B0, B1, lastside);
    mainpair<VT, 2>(g, 14, acc, At, B0, B1, lastside);
    TS(P.ws, 0, 3);
    const int lane = lane_id();
    tail_stage(g, P, c0, e, wid, lane); SBAR();
    const int wr = wid >> 2, wc = wid & 3, fr = lane & 15, fq = lane >> 4;
    if (VT) {
        o.voff = (unsigned)((pm * 4 + wr) * 32768 + wc * 4096 + (fq >> 1) * 1024 + (fq & 1) * 512 + fr * 16);
        pack_v(acc[0][0], o.pk); pack_v(acc[0][1], o.pk + 4); pack_v(acc[1][0], o.pk + 8); pack_v(acc[1][1], o.pk + 12);
    } else {
        o.voff = kq_off(pm, wr, wc, fr, fq);
        pack_kq(acc[0][0], o.pk, 1.0f); pack_kq(acc[0][1], o.pk + 4, 1.0f); pack_kq(acc[1][0], o.pk + 8, 1.0f); pack_kq(acc[1][1], o.pk + 12, 1.0f);
    }
}

__global__ __launch_bounds__(512, 2) void k1_proj(K1Args P) {
    extern __shared__ __attribute__((aligned(16))) unsigned char shm[];
    ldsp lds = (ldsp)shm;
    const int b = blockIdx.x, xcd = b & 7, j = b >> 3, li = j & 15, pm = li & 7, head = 2 * xcd + (li >> 3), vt = j >> 4;
    const int tid = threadIdx.x, wid = rfl(tid >> 6), lane = tid & 63, wr = wid >> 2, wc = wid & 3, fr = lane & 15, fq = lane >> 4;
    const int c0 = (xcd * 28 + (j < 28 ? j : 0)) * 2, e = xcd * 32 + j;
    TS(P.ws, 0, 0);
    u32x4 sw = *(const CAS u32x4*)(uintptr_t)(P.shift_k + c0 * 4); unsigned dw = *(const CAS unsigned*)(uintptr_t)(P.ssm_d + c0);
    G1 g;
    const unsigned lds0 = (unsigned)(uintptr_t)lds;
    g.lds = lds; g.ldsw = lds0 + (unsigned)wid * 1024u;
    { int R, C; stage_rc(tid * 16, R, C); g.voffA_kv = (unsigned)(R * DC + C) * 2u; g.voffA_q = (unsigned)(R * DM + C) * 2u; }
    { const int kg = wid >> 1, row = (lane >> 2) & 7, ch = (lane & 3) ^ (2 * (kg & 1)), n = 32 * (2 * (wid & 1) + (lane >> 5)) + 8 * ch;
      g.voffB = (unsigned)((8 * kg + row) * NW_ + n) * 2u; }
    g.aoff = lds_byte(wr * 64 + fr, fq * 8);
    g.boff0 = fq * 2048 + wc * 512 + (fr >> 2) * 64 + (32 * (fq & 1)) + 8 * (fr & 3);
    g.boff1 = g.boff0 ^ 32;
    g.A_kv = (const char*)P.ckv + (size_t)pm * 256 * DC * 2;
    g.A_q = (const char*)P.x + (size_t)pm * 256 * DM * 2;
    g.B_kv = (const char*)(vt ? P.wuv : P.wuk) + head * 512;
    g.B_q = (const char*)P.wq + head * 512 + vt * 256;
    if (wid < 4) {
        g.sbase = (const char*)(wid < 2 ? P.ak : P.av) + (wid & 1) * (64 * DM * 2) + c0 * 2; g.sstep = 128 * DM * 2; g.sv = (unsigned)lane * (DM * 2);
        g.sadd = 0; g.sdst = lds0 + K1_X + (wid >> 1) * 8192 + (wid & 1) * 256; g.sdstep = 512;
    } else {
        if (wid == 4) { g.sbase = g.A_kv + (size_t)((head & 1) * 2 + vt) * 64 * (DC * 2); g.sstep = 128; g.sv = (unsigned)lane * (DC * 2); }
        else if (wid == 5) { g.sbase = g.B_kv + (size_t)pm * 8 * (NW_ * 2); g.sstep = 64 * NW_ * 2; g.sv = (unsigned)((lane >> 2) & 7) * (NW_ * 2) + (lane & 3) * 128; }
        else { g.sbase = (const char*)(wid == 6 ? P.ak : P.av) + (size_t)j * 4 * (DM * 2) + xcd * 112; g.sstep = 128 * DM * 2; g.sv = (unsigned)(lane & 3) * (DM * 2) + ((lane >> 2) & 1) * 108; }
        g.sadd = 4; g.sdst = lds0 + K1_X + 16384 + wid * 256; g.sdstep = 0;
    }
    PIN(g.voffA_kv); PIN(g.voffB); PIN(g.sv); PIN(g.aoff); PIN(g.boff0); PIN(g.boff1);
    stage_units(g, std::make_integer_sequence<int, 8>{});
    WAIT_V(12);
    auto warm = [&](int slot) {
        const unsigned sink = lds0 + K1_X + 16384 + wid * 256;
        if (slot > 0) { if (wid >= 4) side(g, slot - 5); }
        else if (wid < 3) touch(sink, (unsigned)lane * (DM * 2), g.A_q + (size_t)((head & 1) * 2 + vt) * 64 * (DM * 2) + (4 + wid) * 128);
        else if (wid == 3) touch(sink, (unsigned)((lane < 48 ? lane : 0) >> 1) * (NW_ * 2) + (lane & 1) * 128, g.B_q + (size_t)(256 + pm * 24) * (NW_ * 2));
    };
    asm volatile("" : "+s"(sw), "+s"(dw));
    if (b == 0 && tid < N_AFLAGS + N_OFLAGS) *(u32x4*)((unsigned*)(P.ws + WS_FLAGS) + tid * FLAG_STRIDE) = (u32x4){0u, 0u, 0u, 0u};
    bf16x8 At[4][2], B0[2][2];
    BAR;
    if (wr == 1) BAR;
    TS(P.ws, 0, 1);
    {
        f32x4 q0[4][2], q1[4][2];
        zero8(q0); zero8(q1);
        qphases(g, q0, q1, At, B0, warm, std::make_integer_sequence<int, 14>{});
        stage_unit<27>(g); stage_unit<28>(g); SBAR();
        Out1 o;
        o.rs = __builtin_amdgcn_make_buffer_rsrc(P.ws + WS_Q + (size_t)head * MB, (short)0, (int)MB, 0x00020000);
        o.voff = kq_off(pm, wr, wc, fr, fq) + vt * 8192; o.sbj = 65536; o.shi = 16384;
        pack_kq(q0, o.pk, 0.0625f * 1.4426950408889634f); pack_kq(q1, o.pk + 4, 0.0625f * 1.4426950408889634f);
        st_range<0, 8>(o);
        SBAR();
    }
    TS(P.ws, 0, 2);
    Out1 o;
    if (vt) big_tile<true>(g, P, pm, head, c0, e, wid, At, B0, o); else big_tile<false>(g, P, pm, head, c0, e, wid, At, B0, o);
    o.rs = __builtin_amdgcn_make_buffer_rsrc(P.ws + (vt ? WS_V : WS_K) + (size_t)head * MB, (short)0, (int)MB, 0x00020000);
    o.sbj = vt ? 16384 : 8192; o.shi = vt ? 2048 : 16384;
    SBAR(); st_range<0, 4>(o); SBAR();
    WAIT_V(4);
    if (wr == 0) BAR;
    BAR;
    TS(P.ws, 0, 4);
    const int lane2 = lane_id();
    wo_image(lds, P, e, wid, lane2);
    if (j < 28) conv_part(lds, P, c0, sw, dw, wid, lane2, o);
    else { st_range<4, 16>(o); gate_part(P, (xcd * 4 + (j - 28)) * 64, wid, lane2); }
    TS(P.ws, 0, 7);
}
#undef SA
#undef SB

constexpr int AT_K = 0, AT_V = 65536, AT_LDS = 163840;
constexpr float THR = 8.0f;
constexpr int PD = 6;
constexpr int NEB = 24;
template <int... Is> using iseq = std::integer_sequence<int, Is...>;
template <int N> using make_iseq = std::make_integer_sequence<int, N>;
#define ACLOB "a0","a1","a2","a3","a4","a5","a6","a7","a8","a9","a10","a11","a12","a13","a14","a15","a16","a17","a18","a19","a20","a21","a22","a23","a24","a25","a26","a27","a28","a29","a30","a31","a32","a33","a34","a35","a36","a37","a38","a39","a40","a41","a42","a43","a44","a45","a46","a47","a48","a49","a50","a51","a52","a53","a54","a55","a56","a57","a58","a59","a60","a61","a62","a63","a64","a65","a66","a67","a68","a69","a70","a71","a72","a73","a74","a75","a76","a77","a78","a79","a80","a81","a82","a83","a84","a85","a86","a87","a88","a89","a90","a91","a92","a93","a94","a95","a96","a97","a98","a99","a100","a101","a102","a103","a104","a105","a106","a107","a108","a109","a110","a111","a112","a113","a114","a115","a116","a117","a118","a119","a120","a121","a122","a123","a124","a125","a126","a127","a128","a129","a130","a131","a132","a133","a134","a135","a136","a137","a138","a139","a140","a141","a142","a143","a144","a145","a146","a147","a148","a149","a150","a151","a152","a153","a154","a155","a156","a157","a158","a159","a160","a161","a162","a163","a164","a165","a166","a167","a168","a169","a170","a171","a172","a173","a174","a175","a176","a177","a178","a179","a180","a181","a182","a183","a184","a185","a186","a187","a188","a189","a190","a191","a192","a193","a194","a195","a196","a197","a198","a199","a200","a201","a202","a203","a204","a205","a206","a207","a208","a209","a210","a211","a212","a213","a214","a215","a216","a217","a218","a219","a220","a221","a222","a223","a224","a225","a226","a227","a228","a229","a230","a231","a232","a233","a234","a235","a236","a237","a238","a239","a240","a241","a242","a243","a244","a245","a246","a247","a248","a249","a250","a251","a252","a253","a254","a255"

struct AT {
    f32x16 s[2][2];
    f32x16 cm[2];
    u32x4 p[4];
    bf16x8 fr[8];
    float m, l, rs0, rs1, mx0, mx1, alpha, aone;
    bool pend, first, drain;
    ldscp kb;
    ldscp vcur;
    unsigned goff;
    unsigned m0k, m0v;
    const char *gk, *gv;
};

DI void dma_piece(unsigned m0v, unsigned goff, const char* g, const int off) {
    switch (off) {
    case 0: asm volatile("s_mov_b32 m0, %0\n\ts_nop 0\n\tglobal_load_lds_dwordx4 %1, %2" :: "s"(m0v), "v"(goff), "s"(g) : "memory"); break;
    case 1: asm volatile("s_mov_b32 m0, %0\n\ts_nop 0\n\tglobal_load_lds_dwordx4 %1, %2 offset:1024" :: "s"(m0v), "v"(goff), "s"(g) : "memory"); break;
    case 2: asm volatile("s_mov_b32 m0, %0\n\ts_nop 0\n\tglobal_load_lds_dwordx4 %1, %2 offset:2048" :: "s"(m0v), "v"(goff), "s"(g) : "memory"); break;
    default: asm volatile("s_mov_b32 m0, %0\n\ts_nop 0\n\tglobal_load_lds_dwordx4 %1, %2 offset:3072" :: "s"(m0v), "v"(goff), "s"(g) : "memory"); break;
    }
}
template <int I> DI void dma8(unsigned m0base, unsigned goff, const char* g) { dma_piece(m0base + (I >> 2) * 4096, goff, g + (I >> 2) * 4096, I & 3); }
DI void dma_tile(unsigned m0base, unsigned goff, const char* g) {
    dma8<0>(m0base, goff, g); dma8<1>(m0base, goff, g); dma8<2>(m0base, goff, g); dma8<3>(m0base, goff, g);
    dma8<4>(m0base, goff, g); dma8<5>(m0base, goff, g); dma8<6>(m0base, goff, g); dma8<7>(m0base, goff, g);
}
DI float ex2(float x) { return __builtin_amdgcn_exp2f(x); }
DI float MX3(float a, float b, float c) { float r; asm("v_max3_f32 %0, %1, %2, %3" : "=v"(r) : "v"(a), "v"(b), "v"(c)); return r; }
DI float MX2(float a, float b) { float r; asm("v_max_f32_e32 %0, %1, %2" : "=v"(r) : "v"(a), "v"(b)); return r; }
template <int D0> DI void qk_init(f32x16& s, const bf16x8& k, const f32x16& c) {
    asm volatile("v_mfma_f32_32x32x16_bf16 %0, %1, a[%c3:%c4], %2" : "=&v"(s) : "v"(k), "v"(c), "i"(128 + 4 * D0), "i"(131 + 4 * D0)); }
template <int D0> DI void qk_acc(f32x16& s, const bf16x8& k) {
    asm volatile("v_mfma_f32_32x32x16_bf16 %0, %1, a[%c2:%c3], %0" : "+v"(s) : "v"(k), "i"(128 + 4 * D0), "i"(131 + 4 * D0)); }
template <int DB> DI void pv_acc(const bf16x8& v, const u32x4& p) {
    asm volatile("v_mfma_f32_32x32x16_bf16 a[%c2:%c3], %0, %1, a[%c2:%c3]" :: "v"(v), "v"(p), "i"(16 * DB), "i"(16 * DB + 15)); }
DI void add_bcast(f32x16& x, float aone, float b) { asm volatile("s_nop 1\n\tv_mfma_f32_32x32x2_f32 %0, %1, %2, %0" : "+v"(x) : "v"(aone), "v"(b)); }
#define NOPS16() asm volatile("s_nop 7\n\ts_nop 7" ::: "memory")
#define NOPS2() asm volatile("s_nop 1" ::: "memory")
template <int I> DI void a_zero() { asm volatile("v_accvgpr_write_b32 a[%c0], 0" :: "i"(I)); }
template <int I> DI void a_scale(float f) { float t; asm volatile("v_accvgpr_read_b32 %0, a[%c2]\n\tv_mul_f32 %0, %1, %0\n\tv_accvgpr_write_b32 a[%c2], %0" : "=&v"(t) : "v"(f), "i"(I)); }
template <int I> DI void a_qmov() { asm volatile("v_accvgpr_mov_b32 a[%c0], a[%c1]" :: "i"(128 + I), "i"(192 + I)); }
template <int I> DI float a_read() { float t; asm volatile("v_accvgpr_read_b32 %0, a[%c1]" : "=v"(t) : "i"(I)); return t; }
template <int F> DI void q_load(unsigned voff, const char* g) {
    asm volatile("global_load_dwordx4 a[%c2:%c3], %0, %1 offset:%c4" :: "v"(voff), "s"(g + ((F & 15) >> 2) * 4096), "i"(128 + 4 * F), "i"(131 + 4 * F), "i"((F & 3) * 1024) : "memory"); }
template <int Q> DI void o_store(unsigned voff, const char* g) {
    asm volatile("global_store_dwordx4 %0, a[%c2:%c3], %1 offset:%c4 sc1" :: "v"(voff), "s"(g + (Q >> 2) * 4096), "i"(4 * Q), "i"(4 * Q + 3), "i"((Q & 3) * 1024) : "memory"); }
template <int... Is> DI void a_zeros(iseq<Is...>) { (a_zero<Is>(), ...); }
template <int... Is> DI void a_scales(float f, iseq<Is...>) { (a_scale<Is>(f), ...); }
template <int... Is> DI void a_qmovs(iseq<Is...>) { (a_qmov<Is>(), ...); }
template <int B, int... Is> DI void q_loads(unsigned voff, const char* g, iseq<Is...>) { (q_load<B + Is>(voff, g), ...); }
template <int... Is> DI void o_stores(unsigned voff, const char* g, iseq<Is...>) { (o_store<Is>(voff, g), ...); }
template <int B, int... Is> DI void a_reads(float (&o)[16], iseq<Is...>) { ((o[Is] = a_read<B + Is>()), ...); }

template <int KS, int R> DI bf16x8 kfrag(const AT& c) { return ldsrd16(c.kb + KS * 32768 + ((R & 1) * 16 + (R >> 1)) * 1024); }
template <int R> DI bf16x8 vfrag(ldscp v) { return ldsrd16(v + (R & 7) * 4096 + (R >> 3) * 1024); }
template <int X, int E> DI void exp_sum(AT& c) {
    const float e = ex2(c.s[X][E >> 4][E & 15]); c.s[X][E >> 4][E & 15] = e;
    if constexpr (E & 1) c.rs1 += e; else c.rs0 += e;
}
template <int X, int W> DI void pack1(AT& c) {
    c.p[W >> 2][W & 3] = cvtpk(c.s[X][W >> 3][(2 * W) & 15], c.s[X][W >> 3][(2 * W + 1) & 15]);
}
template <int X, int MODE, int N> DI void gapA(AT& c) {
    constexpr int d0 = N >> 1, kb = N & 1, Y = X ^ 1;
    if constexpr (d0 == 0) qk_init<0>(c.s[Y][kb], c.fr[N & 7], c.cm[(MODE & 4) ? kb : 0]);
    else qk_acc<d0>(c.s[Y][kb], c.fr[N & 7]);
    constexpr int R = N + PD;
    if constexpr (R < 32) c.fr[R & 7] = kfrag<Y, R>(c); else if constexpr (MODE & 1) c.fr[R & 7] = vfrag<R - 32>(c.vcur);
    if constexpr (MODE & 1) {
        if constexpr (N < 32 - NEB) { exp_sum<X, NEB + N>(c); PIN(c.rs0); PIN(c.rs1); }
        if constexpr (N >= 10 && N < 26) { pack1<X, N - 10>(c); PIN(c.p[(N - 10) >> 2]); }
    }
    if constexpr ((MODE & 2) && (N & 1)) {
        if constexpr (N < 16) dma8<(N >> 1)>(c.m0k, c.goff, c.gk); else dma8<((N - 16) >> 1)>(c.m0v, c.goff, c.gv);
    }
    SBAR();
}
template <int X> DI void decide(AT& c) {
    constexpr int Y = X ^ 1;
    float mm = MX2(c.mx0, c.mx1);
    auto rr = __builtin_amdgcn_permlane32_swap(__float_as_uint(mm), __float_as_uint(mm), false, false);
    const float rm = MX2(__uint_as_float(rr[0]), __uint_as_float(rr[1]));
    if (__builtin_expect(c.first || __any(rm > THR), 0)) {
        const float dl = c.first ? rm : MX2(rm, 0.f);
        c.m += dl; c.alpha = ex2(-dl); c.pend = !c.first;
        const float nd = -dl;
        add_bcast(c.s[Y][0], c.aone, nd); add_bcast(c.s[Y][1], c.aone, nd); add_bcast(c.cm[0], c.aone, nd);
        asm volatile("s_nop 7\n\ts_nop 7\n\ts_nop 3" : "+v"(c.s[Y][0]), "+v"(c.s[Y][1]), "+v"(c.cm[0]));
    }
}
template <int X, int I> DI void maxstep(AT& c) {
    constexpr int Y = X ^ 1, h = I >> 2, r = 4 * (I & 3);
    if constexpr (I == 0) { c.mx0 = MX2(c.s[Y][0][0], c.s[Y][0][1]); c.mx1 = MX2(c.s[Y][0][2], c.s[Y][0][3]); }
    else { c.mx0 = MX3(c.mx0, c.s[Y][h][r], c.s[Y][h][r + 1]); c.mx1 = MX3(c.mx1, c.s[Y][h][r + 2], c.s[Y][h][r + 3]); }
}
template <int X, int MODE, int N> DI void gapB(AT& c) {
    constexpr int ks = N >> 3, db = N & 7, Y = X ^ 1;
    pv_acc<db>(c.fr[N & 7], c.p[ks]);
    constexpr int R = N + PD;
    if constexpr (R < 32) c.fr[R & 7] = vfrag<R>(c.vcur);
    if constexpr (MODE & 1) {
        if constexpr (MODE & 2) {
            if constexpr (N == 28) { c.fr[0] = kfrag<X, 0>(c); c.fr[1] = kfrag<X, 1>(c); }
            if constexpr (N == 29) { c.fr[2] = kfrag<X, 2>(c); c.fr[3] = kfrag<X, 3>(c); }
            if constexpr (N == 30) { c.fr[4] = kfrag<X, 4>(c); }
            if constexpr (N == 31) { c.fr[5] = kfrag<X, 5>(c); }
        }
        if constexpr (N == 1) { asm volatile("s_nop 7" : "+v"(c.s[Y][0]), "+v"(c.s[Y][1])); }
        if constexpr (N >= 2 && N < 6) { maxstep<X, 2 * (N - 2)>(c); maxstep<X, 2 * (N - 2) + 1>(c); PIN(c.mx0); PIN(c.mx1); }
        if constexpr (N == 6) { c.l += c.rs0 + c.rs1; c.rs0 = 0.f; c.rs1 = 0.f; PIN(c.l); }
        if constexpr (N == 7) decide<X>(c);
        if constexpr (N >= 8) { exp_sum<Y, N - 8>(c); PIN(c.rs0); PIN(c.rs1); }
    }
    SBAR();
    if constexpr ((MODE & 1) && N == 27) {
        if constexpr (MODE & 2) {
            if (X == 1 && c.drain) asm volatile("s_waitcnt vmcnt(0)" ::: "memory");
            asm volatile("s_waitcnt vmcnt(8) lgkmcnt(0)\n\ts_barrier" ::: "memory");
        } else asm volatile("s_waitcnt vmcnt(0) lgkmcnt(0)\n\ts_barrier" ::: "memory");
        SBAR();
    }
}
template <int X, int MODE, int... Ns> DI void phaseA(AT& c, iseq<Ns...>) { (gapA<X, MODE, Ns>(c), ...); }
template <int X, int MODE, int... Ns> DI void phaseB(AT& c, iseq<Ns...>) { (gapB<X, MODE, Ns>(c), ...); }
template <int X, int B, int... Es> DI void exps_from(AT& c, iseq<Es...>) { (exp_sum<X, B + Es>(c), ...); }
template <int X, int... Ws> DI void packs(AT& c, iseq<Ws...>) { (pack1<X, Ws>(c), ...); }
template <int... Is> DI void maxsteps1(AT& c, iseq<Is...>) { (maxstep<1, Is>(c), ...); }
DI void rescale_o(AT& c) {
    if (__builtin_expect(c.pend, 0)) {
        NOPS16();
        a_scales(c.alpha, make_iseq<128>{});
        NOPS2();
        c.l *= c.alpha; c.pend = false;
    }
}
DI float halfsum(float x) { auto rr = __builtin_amdgcn_permlane32_swap(__float_as_uint(x), __float_as_uint(x), false, false); return __uint_as_float(rr[0]) + __uint_as_float(rr[1]); }
DI void build_mask(AT& c, int mrel) {
    const float NEG = -__builtin_inff(), nm = -c.m;
#pragma unroll
    for (int r = 0; r < 16; ++r) { const int kk = (r & 3) + 8 * (r >> 2); c.cm[0][r] = kk > mrel ? NEG : nm; c.cm[1][r] = kk + 32 > mrel ? NEG : nm; }
    PIN(c.cm[0]); PIN(c.cm[1]); NOPS2();
}

struct ATW {
    const char *Kh, *Vh; ldsp lds; unsigned mk, mv; int n1, vs, lane, wid, r32, hi, qrel; bool isA;
    const char* part; f32x2* mlp; unsigned* flag;
};
DI int tile_of(const ATW& w, int g) { return w.isA ? w.n1 + g : (g < w.n1 ? g : g - w.n1); }
template <int X, bool DMA> DI void setup(AT& c, const ATW& w, int g) {
    c.vcur = w.lds + AT_V + w.vs * 32768 + w.lane * 16;
    if constexpr (DMA) {
        const int v2 = w.vs == 0 ? 2 : w.vs - 1, tn = tile_of(w, g + 2);
        c.gk = w.Kh + (size_t)tn * 32768; c.gv = w.Vh + (size_t)tn * 32768; c.m0k = w.mk + X * 32768; c.m0v = w.mv + v2 * 32768;
    }
}
template <bool LAST> DI void pair(AT& c, ATW& w, int g0) {
    setup<0, true>(c, w, g0);
    const bool seam = !w.isA && (g0 + 1 == w.n1);
    c.first = seam;
    const float m_old = c.m;
    if (seam) { a_qmovs(make_iseq<64>{}); NOPS2(); }
    if constexpr (LAST) build_mask(c, w.qrel);
    SBAR();
    phaseA<0, LAST ? 7 : 3>(c, make_iseq<32>{});
    phaseB<0, 3>(c, make_iseq<32>{});
    rescale_o(c);
    if (seam) {
        NOPS16();
        o_stores((unsigned)(w.wid * 32768 + w.lane * 16), w.part, make_iseq<32>{});
        const float lt = halfsum(c.l);
        if (w.hi == 0) __hip_atomic_store((unsigned long long*)(w.mlp + w.wid * 32 + w.r32), ((unsigned long long)__float_as_uint(lt) << 32) | __float_as_uint(m_old), RLX_AGENT);
        NOPS2();
        a_zeros(make_iseq<128>{});
        NOPS2();
        c.l = 0.f;
    }
    c.drain = seam; c.first = false;
    w.vs = w.vs == 2 ? 0 : w.vs + 1;
    setup<1, !LAST>(c, w, g0 + 1);
    if constexpr (LAST) build_mask(c, w.qrel - 64);
    SBAR();
    phaseA<1, LAST ? 5 : 3>(c, make_iseq<32>{});
    phaseB<1, LAST ? 1 : 3>(c, make_iseq<32>{});
    rescale_o(c);
    if (seam) { if (threadIdx.x == 0) __hip_atomic_store(w.flag, 1u, RLX_AGENT); }
    w.vs = w.vs == 2 ? 0 : w.vs + 1;
}

__global__ __launch_bounds__(256, 1) void k2_attn(unsigned char* ws) {
    extern __shared__ __attribute__((aligned(16))) unsigned char shm[];
    ldsp lds = (ldsp)shm;
    const int tid = threadIdx.x, wid = rfl(tid >> 6), lane = tid & 63, r32 = lane & 31, hi = lane >> 5;
    const int b = blockIdx.x, xcd = b & 7, j = b >> 3, head = 2 * xcd + (j >> 4), w16 = j & 15;
    const bool isA = w16 >= 8; const int i = w16 & 7, bq = 15 - i;
    const int seg = head * 8 + i;
    ATW w;
    w.Kh = (const char*)ws + WS_K + (size_t)head * MB; w.Vh = (const char*)ws + WS_V + (size_t)head * MB;
    const char* Qh = (const char*)ws + WS_Q + (size_t)head * MB;
    w.lds = lds; w.n1 = 15 - 2 * i; w.vs = 0; w.lane = lane; w.wid = wid; w.r32 = r32; w.hi = hi; w.isA = isA; w.qrel = wid * 32 + r32 - 4 * hi;
    w.part = (const char*)ws + WS_PART + (size_t)seg * 131072;
    w.mlp = (f32x2*)(ws + WS_ML) + seg * 128;
    w.flag = (unsigned*)(ws + WS_FLAGS) + seg * FLAG_STRIDE;
    AT c;
    const unsigned lds0 = (unsigned)(uintptr_t)lds;
    c.goff = (unsigned)wid * 8192u + (unsigned)lane * 16u;
    c.kb = lds + AT_K + lane * 16;
    w.mk = lds0 + AT_K + wid * 8192; w.mv = lds0 + AT_V + wid * 8192;
    TS(ws, 1, 0);
    asm volatile("s_nop 4" ::: ACLOB);
    dma_tile(w.mk, c.goff, w.Kh + (size_t)tile_of(w, 0) * 32768);
    q_loads<0>((unsigned)lane * 16u, Qh + (size_t)(bq * 4 + wid) * 16384, make_iseq<16>{});
    dma_tile(w.mk + 32768, c.goff, w.Kh + (size_t)tile_of(w, 1) * 32768);
    dma_tile(w.mv, c.goff, w.Vh + (size_t)tile_of(w, 0) * 32768);
    dma_tile(w.mv + 32768, c.goff, w.Vh + (size_t)tile_of(w, 1) * 32768);
    q_loads<16>((unsigned)lane * 16u, Qh + (size_t)(i * 4 + wid) * 16384, make_iseq<16>{});
    a_zeros(make_iseq<128>{});
    c.cm[0] = f32x16{}; c.cm[1] = f32x16{}; c.m = 0.f; c.l = 0.f; c.rs0 = 0.f; c.rs1 = 0.f; c.alpha = 1.f; c.pend = false; c.drain = false;
    c.aone = hi ? 0.f : 1.f;
    PIN(c.cm[0]);
    asm volatile("s_waitcnt vmcnt(40)\n\ts_barrier" ::: "memory");
    SBAR();
    c.fr[0] = kfrag<0, 0>(c); c.fr[1] = kfrag<0, 1>(c); c.fr[2] = kfrag<0, 2>(c); c.fr[3] = kfrag<0, 3>(c); c.fr[4] = kfrag<0, 4>(c); c.fr[5] = kfrag<0, 5>(c);
    SBAR();
    phaseA<1, 0>(c, make_iseq<32>{});
    SBAR(); asm volatile("s_nop 7\n\ts_nop 7" : "+v"(c.s[0][0]), "+v"(c.s[0][1])); SBAR();
    c.first = true;
    maxsteps1(c, make_iseq<8>{});
    decide<1>(c);
    exps_from<0, 0>(c, make_iseq<NEB>{});
    asm volatile("s_waitcnt vmcnt(0) lgkmcnt(0)\n\ts_barrier" ::: "memory");
    SBAR();
    c.fr[0] = kfrag<1, 0>(c); c.fr[1] = kfrag<1, 1>(c); c.fr[2] = kfrag<1, 2>(c); c.fr[3] = kfrag<1, 3>(c); c.fr[4] = kfrag<1, 4>(c); c.fr[5] = kfrag<1, 5>(c);
    TS(ws, 1, 1);
    for (int g0 = 0; g0 < 14; g0 += 2) pair<false>(c, w, g0);
    TS(ws, 1, 2);
    pair<true>(c, w, 14);
    TS(ws, 1, 3);
    c.vcur = lds + AT_V + w.vs * 32768 + lane * 16;
    c.fr[0] = vfrag<0>(c.vcur); c.fr[1] = vfrag<1>(c.vcur); c.fr[2] = vfrag<2>(c.vcur); c.fr[3] = vfrag<3>(c.vcur); c.fr[4] = vfrag<4>(c.vcur); c.fr[5] = vfrag<5>(c.vcur);
    exps_from<0, NEB>(c, make_iseq<32 - NEB>{});
    packs<0>(c, make_iseq<16>{});
    c.l += c.rs0 + c.rs1;
    PIN(c.p[0]); PIN(c.p[1]); PIN(c.p[2]); PIN(c.p[3]);
    SBAR(); NOPS2(); SBAR();
    phaseB<0, 0>(c, make_iseq<32>{});
    SBAR(); NOPS16(); SBAR();
    const float lt = halfsum(c.l);
    TS(ws, 1, 4);
    float f1, f2 = 0.f;
    if (isA) {
        if (wid == 0) {
            unsigned spins = 0;
            while (rfl(__hip_atomic_load(w.flag, RLX_AGENT)) == 0u) { __builtin_amdgcn_s_sleep(2); if (++spins > (1u << 22)) break; }
            __builtin_amdgcn_fence(__ATOMIC_ACQUIRE, "agent");
            asm volatile("s_waitcnt vmcnt(0)" ::: "memory");
        }
        __syncthreads();
        const f32x2 ml2 = w.mlp[wid * 32 + r32];
        const float mn = __builtin_fmaxf(c.m, ml2[0]);
        const float w1 = ex2(c.m - mn), w2 = ex2(ml2[0] - mn);
        const float inv = 1.0f / (lt * w1 + ml2[1] * w2);
        f1 = w1 * inv; f2 = w2 * inv;
    } else {
        f1 = 1.0f / lt;
    }
    TS(ws, 1, 5);
    {
        const int row = (isA ? bq : i) * 128 + wid * 32 + r32;
        unsigned char* cp = ws + WS_CTX + ((size_t)(row >> 4) * 128 + head * 8) * 1024 + hi * 256 + (row & 15) * 16;
        const f32x4* pp = (const f32x4*)(w.part + wid * 32768 + lane * 16);
        f32x4 pv[32];
        if (isA) {
#pragma unroll
            for (int q = 0; q < 32; ++q) pv[q] = pp[q * 64];
        } else {
#pragma unroll
            for (int q = 0; q < 32; ++q) pv[q] = (f32x4){0.f, 0.f, 0.f, 0.f};
        }
        auto fin = [&](auto dtag) {
            constexpr int d = decltype(dtag)::value;
            float o[16]; a_reads<16 * d>(o, make_iseq<16>{});
#pragma unroll
            for (int s2 = 0; s2 < 2; ++s2) {
                u32x4 wv;
#pragma unroll
                for (int e = 0; e < 4; ++e) { const int r = 8 * s2 + 2 * e;
                    wv[e] = cvtpk(o[r] * f1 + pv[4 * d + (r >> 2)][r & 3] * f2, o[r + 1] * f1 + pv[4 * d + ((r + 1) >> 2)][(r + 1) & 3] * f2); }
                *(u32x4*)(cp + d * 1024 + s2 * 512) = wv;
            }
        };
        fin(std::integral_constant<int, 0>{}); fin(std::integral_constant<int, 1>{}); fin(std::integral_constant<int, 2>{}); fin(std::integral_constant<int, 3>{});
        fin(std::integral_constant<int, 4>{}); fin(std::integral_constant<int, 5>{}); fin(std::integral_constant<int, 6>{}); fin(std::integral_constant<int, 7>{});
    }
    TS(ws, 1, 7);
}

constexpr int OP_WST = 128 * 224, OP_NS = 5, OP_LDS = OP_NS * OP_WST;

template <int Q> DI void op_finish(const f32x4 (&acc)[14], const __amdgpu_buffer_rsrc_t srs, int loff, const u32x2 (&xw)[4], const u32x2 (&yw)[4], float g,
                                   bf16_t* outp) {
    constexpr int F0 = (Q >> 1) * 7 + (Q & 1) * 4, NF = 4 - (Q & 1);
    f32x4 pt[4][NF];
#pragma unroll
    for (int q = 0; q < 4; ++q)
#pragma unroll
        for (int f = 0; f < NF; ++f)
            if (q != Q) pt[q][f] = __builtin_bit_cast(f32x4, __builtin_amdgcn_raw_buffer_load_b128(srs, loff + q * 57344 + (F0 + f) * 1024, 0, 16));
    const float og = rbf(1.0f - g);
#pragma unroll
    for (int f = 0; f < NF; ++f) {
        pt[Q][f] = acc[F0 + f];
        const f32x4 sum = ((pt[0][f] + pt[1][f]) + pt[2][f]) + pt[3][f];
        const float xv[4] = {bf_lo(xw[f][0]), bf_hi(xw[f][0]), bf_lo(xw[f][1]), bf_hi(xw[f][1])};
        const float yv[4] = {bf_lo(yw[f][0]), bf_hi(yw[f][0]), bf_lo(yw[f][1]), bf_hi(yw[f][1])};
        float r[4];
#pragma unroll
        for (int e = 0; e < 4; ++e) r[e] = rbf(g * rbf(xv[e] * yv[e])) + rbf(og * rbf(sum[e]));
        u32x2 w = {cvtpk(r[0], r[1]), cvtpk(r[2], r[3])};
        *(u32x2*)(outp + f * 16) = w;
    }
}
template <int Q> DI void op_publish(const f32x4 (&acc)[14], const __amdgpu_buffer_rsrc_t srs, int off) {
    constexpr int F0 = (Q >> 1) * 7 + (Q & 1) * 4, NF = 4 - (Q & 1);
#pragma unroll
    for (int f = 0; f < 14; ++f)
        if (f < F0 || f >= F0 + NF) __builtin_amdgcn_raw_buffer_store_b128(__builtin_bit_cast(u32x4, acc[f]), srs, off + f * 1024, 0, 16);
}

__global__ __launch_bounds__(256, 1) void k3_oproj(unsigned char* ws, const bf16_t* x, bf16_t* out) {
    extern __shared__ __attribute__((aligned(16))) unsigned char shm[];
    ldsp lds = (ldsp)shm;
    const int tid = threadIdx.x, wid = rfl(tid >> 6), lane = tid & 63, fr = lane & 15, fq = lane >> 4;
    const int b = blockIdx.x, xcd = b & 7, j = b >> 3, sk = xcd & 3, rb = 8 * (xcd >> 2) + (j >> 2), cn = j & 3;
    const int grp = rb * 4 + cn;
    const char* wbase = (const char*)ws + WS_WI + (size_t)(sk * 4 + cn) * (8 * OP_WST) + wid * 7168;
    const char* cbase = (const char*)ws + WS_CTX + ((size_t)(rb * 8 + 2 * wid) * 128 + sk * 32) * 1024;
    const unsigned cv = (unsigned)lane * 16u;
    bf16x8 cf[8][2][4];
    const unsigned lds0 = (unsigned)(uintptr_t)lds + wid * 7168;
    auto issue = [&](const int s) {
        const char* wg = wbase + s * OP_WST;
        const unsigned lw = lds0 + (s % OP_NS) * OP_WST;
        dma_piece(lw, cv, wg, 0); dma_piece(lw, cv, wg, 1); dma_piece(lw, cv, wg, 2); dma_piece(lw, cv, wg, 3);
        dma_piece(lw + 4096, cv, wg + 4096, 0); dma_piece(lw + 4096, cv, wg + 4096, 1); dma_piece(lw + 4096, cv, wg + 4096, 2);
#pragma unroll
        for (int tb = 0; tb < 2; ++tb) {
            const char* cg = cbase + tb * 131072 + s * 4096;
            asm volatile("global_load_dwordx4 %0, %1, %2" : "=v"(cf[s][tb][0]) : "v"(cv), "s"(cg) : "memory");
            asm volatile("global_load_dwordx4 %0, %1, %2 offset:1024" : "=v"(cf[s][tb][1]) : "v"(cv), "s"(cg) : "memory");
            asm volatile("global_load_dwordx4 %0, %1, %2 offset:2048" : "=v"(cf[s][tb][2]) : "v"(cv), "s"(cg) : "memory");
            asm volatile("global_load_dwordx4 %0, %1, %2 offset:3072" : "=v"(cf[s][tb][3]) : "v"(cv), "s"(cg) : "memory");
        }
        SBAR();
    };
    f32x4 acc[14];
#pragma unroll
    for (int f = 0; f < 14; ++f) acc[f] = (f32x4){0.f, 0.f, 0.f, 0.f};
    const int trow = rb * 128 + wid * 32 + (sk >> 1) * 16 + fr, tcol = cn * 112 + (sk & 1) * 64 + 4 * fq;
    u32x2 xw[4], yw[4]; float gate;
    TS(ws, 2, 0);
    issue(0); issue(1); issue(2); issue(3);
#pragma unroll
    for (int s = 0; s < 8; ++s) {
#define CFT "+v"(cf[s][0][0]), "+v"(cf[s][0][1]), "+v"(cf[s][0][2]), "+v"(cf[s][0][3]), "+v"(cf[s][1][0]), "+v"(cf[s][1][1]), "+v"(cf[s][1][2]), "+v"(cf[s][1][3])
        switch (s) {
        case 0: case 1: case 2: case 3: case 4: asm volatile("s_waitcnt vmcnt(45)\n\ts_barrier" : CFT :: "memory"); break;
        case 5: asm volatile("s_waitcnt vmcnt(51)\n\ts_barrier" : CFT :: "memory"); break;
        case 6: asm volatile("s_waitcnt vmcnt(36)\n\ts_barrier" : CFT :: "memory"); break;
        default: asm volatile("s_waitcnt vmcnt(21)\n\ts_barrier" : CFT :: "memory"); break;
        }
#undef CFT
        SBAR();
        ldscp wp = lds + (s % OP_NS) * OP_WST + lane * 16;
        bf16x8 wf[8];
#pragma unroll
        for (int i = 0; i < 7; ++i) wf[i] = ldsrd16(wp + i * 1024);
        SBAR();
        if (s + 4 < 8) issue(s + 4);
        if (s == 4) {
            const bf16_t* y2p = (const bf16_t*)(ws + WS_Y2);
#pragma unroll
            for (int f = 0; f < 4; ++f) {
                const int c = tcol + ((f == 3 && (sk & 1)) ? 0 : f * 16);
                xw[f] = *(const u32x2*)(x + (size_t)trow * DM + c);
                const bf16_t* yp = y2p + (size_t)c * SEQ + trow;
                yw[f] = (u32x2){yp[0] | (unsigned)yp[SEQ] << 16, yp[2 * SEQ] | (unsigned)yp[3 * SEQ] << 16};
            }
            gate = ((const float*)(ws + WS_G))[trow];
            SBAR();
        }
#pragma unroll
        for (int i = 0; i < 28; ++i) {
            const int kk = i / 7, cb = i % 7;
            if (i + 7 < 28) wf[(i + 7) & 7] = ldsrd16(wp + (i + 7) * 1024);
            acc[cb] = MFMA16(wf[i & 7], cf[s][0][kk], acc[cb]);
            acc[7 + cb] = MFMA16(wf[i & 7], cf[s][1][kk], acc[7 + cb]);
            SBAR();
        }
    }
    TS(ws, 2, 1);
    float* slab = (float*)(ws + WS_OPART) + (size_t)grp * 4 * 14336;
    const __amdgpu_buffer_rsrc_t srs = __builtin_amdgcn_make_buffer_rsrc((void*)slab, (short)0, 4 * 57344, 0x00020000);
    const int soff = ((sk * 4 + wid) * 14 * 64 + lane) * 16;
    if (sk == 0) op_publish<0>(acc, srs, soff); else if (sk == 1) op_publish<1>(acc, srs, soff);
    else if (sk == 2) op_publish<2>(acc, srs, soff); else op_publish<3>(acc, srs, soff);
    asm volatile("s_waitcnt vmcnt(0)\n\ts_barrier" ::: "memory");
    TS(ws, 2, 2);
    unsigned* fl = (unsigned*)(ws + WS_FLAGS) + (N_AFLAGS + grp) * FLAG_STRIDE;
    if (wid == 0) {
        if (lane == 0) __hip_atomic_store(fl + sk, 1u, RLX_AGENT);
        unsigned spins = 0;
        while (true) {
            const unsigned v = __hip_atomic_load(fl + (lane & 3), RLX_AGENT);
            if (__all(v != 0u)) break;
            __builtin_amdgcn_s_sleep(1);
            if (++spins > (1u << 22)) break;
        }
    }
    asm volatile("s_barrier" ::: "memory");
    TS(ws, 2, 3);
    bf16_t* outp = out + (size_t)trow * DM + tcol;
    const int loff = (wid * 14 * 64 + lane) * 16;
    if (sk == 0) op_finish<0>(acc, srs, loff, xw, yw, gate, outp); else if (sk == 1) op_finish<1>(acc, srs, loff, xw, yw, gate, outp);
    else if (sk == 2) op_finish<2>(acc, srs, loff, xw, yw, gate, outp); else op_finish<3>(acc, srs, loff, xw, yw, gate, outp);
    TS(ws, 2, 7);
}

extern "C" void kernel_launch(void* const* d_in, const int* in_sizes, int n_in, void* d_out, int out_size, void* d_ws, size_t ws_size,
                              hipStream_t stream) {
    (void)in_sizes; (void)n_in; (void)out_size; (void)ws_size;
    (void)hipFuncSetAttribute((const void*)k1_proj, hipFuncAttributeMaxDynamicSharedMemorySize, K1_LDS);
    (void)hipFuncSetAttribute((const void*)k2_attn, hipFuncAttributeMaxDynamicSharedMemorySize, AT_LDS);
    (void)hipFuncSetAttribute((const void*)k3_oproj, hipFuncAttributeMaxDynamicSharedMemorySize, OP_LDS);
    K1Args P;
    P.x = (const bf16_t*)d_in[0]; P.ak = (const bf16_t*)d_in[1]; P.av = (const bf16_t*)d_in[2]; P.shift_k = (const bf16_t*)d_in[3];
    P.ssm_k = (const bf16_t*)d_in[4]; P.ssm_d = (const bf16_t*)d_in[5]; P.ckv = (const bf16_t*)d_in[7]; P.wq = (const bf16_t*)d_in[8];
    P.wuk = (const bf16_t*)d_in[9]; P.wuv = (const bf16_t*)d_in[10]; P.gate_w = (const bf16_t*)d_in[12]; P.wo = (const bf16_t*)d_in[11]; P.ws = (unsigned char*)d_ws;
#define DEV_STAGES 7
    if (DEV_STAGES & 1) k1_proj<<<256, 512, K1_LDS, stream>>>(P);
    if (DEV_STAGES & 2) k2_attn<<<256, 256, AT_LDS, stream>>>((unsigned char*)d_ws);
    if (DEV_STAGES & 4) k3_oproj<<<256, 256, OP_LDS, stream>>>((unsigned char*)d_ws, (const bf16_t*)d_in[0], (bf16_t*)d_out);
}
```

```cpp
#include <hip/hip_runtime.h>
#include <hip/hip_bf16.h>
#include <stdint.h>
#include <utility>

#define LAS __attribute__((address_space(3)))
#define GAS __attribute__((address_space(1)))
#define CAS __attribute__((address_space(4)))
#define DI __device__ __forceinline__
typedef unsigned short bf16_t;
typedef short bf16x8 __attribute__((ext_vector_type(8)));
typedef short s16x4 __attribute__((ext_vector_type(4)));
typedef float f32x2 __attribute__((ext_vector_type(2)));
typedef float f32x4 __attribute__((ext_vector_type(4)));
typedef float f32x16 __attribute__((ext_vector_type(16)));
typedef unsigned u32x2 __attribute__((ext_vector_type(2)));
typedef unsigned u32x4 __attribute__((ext_vector_type(4)));
typedef __bf16 bfx2 __attribute__((ext_vector_type(2)));
typedef LAS unsigned char* ldsp;
typedef const LAS unsigned char* ldscp;

constexpr int SEQ = 2048, DM = 448, NH = 16, HD = 256, DC = 1024, NW_ = 4096;

constexpr size_t MB = 1024 * 1024;
constexpr size_t WS_Q = 0, WS_K = 16 * MB, WS_V = 32 * MB, WS_CTX = 48 * MB, WS_PART = 64 * MB, WS_ML = 80 * MB,
                 WS_Y2 = 81 * MB, WS_G = 84 * MB, WS_OPART = 85 * MB, WS_FLAGS = 100 * MB, WS_WI = 102 * MB;
constexpr int FLAG_STRIDE = 64;
constexpr int N_AFLAGS = 128, N_OFLAGS = 64;

#define TS(wsp, kern, slot) do { } while (0)
#define TSI(wsp, idx) do { } while (0)
#define SBAR() __builtin_amdgcn_sched_barrier(0)
#define PIN(x) asm volatile("" : "+v"(x))
#define RLX_AGENT __ATOMIC_RELAXED, __HIP_MEMORY_SCOPE_AGENT

DI unsigned cvtpk(float lo, float hi) { f32x2 v = {lo, hi}; bfx2 b = __builtin_convertvector(v, bfx2); return __builtin_bit_cast(unsigned, b); }
DI float bf_lo(unsigned w) { return __uint_as_float(w << 16); }
DI float bf_hi(unsigned w) { return __uint_as_float(w & 0xffff0000u); }
DI float rbf(float x) { return bf_lo(cvtpk(x, 0.f)); }
DI unsigned rfl(unsigned x) { return __builtin_amdgcn_readfirstlane(x); }
DI s16x4 trread(ldscp p) { return __builtin_amdgcn_ds_read_tr16_b64_v4i16((LAS s16x4*)p); }
DI bf16x8 cat8(s16x4 lo, s16x4 hi) { return __builtin_shufflevector(lo, hi, 0, 1, 2, 3, 4, 5, 6, 7); }
DI bf16x8 ldsrd16(ldscp p) { return *(const LAS bf16x8*)p; }
#define MFMA16(a, b, c) __builtin_amdgcn_mfma_f32_16x16x32_bf16((a), (b), (c), 0, 0, 0)
#define MFMA32(a, b, c) __builtin_amdgcn_mfma_f32_32x32x16_bf16((a), (b), (c), 0, 0, 0)

DI void touch(unsigned sink, unsigned voff, const char* g) {
    asm volatile("s_mov_b32 m0, %0\n\ts_nop 0\n\tglobal_load_lds_dword %1, %2" :: "s"(sink), "v"(voff), "s"(g) : "memory");
}
#define CODE_TOUCH(SYM, END, sink, chunk, lane) do { const char* _p; unsigned _n; \
        asm volatile("s_getpc_b64 s[90:91]\n\ts_add_u32 s90, s90, " SYM "@rel32@lo+4\n\ts_addc_u32 s91, s91, " SYM "@rel32@hi+12\n\ts_mov_b64 %0, s[90:91]\n\ts_mov_b32 %1, " END "-" SYM \
                     : "=s"(_p), "=s"(_n) :: "s90", "s91", "scc"); \
        const unsigned _o = ((unsigned)(chunk) * 64u + (unsigned)(lane)) * 128u; \
        if ((unsigned)(chunk) * 8192u < _n && _n < 262144u) touch((sink), _o < _n - 4u ? _o : _n - 4u, _p); } while (0)
#define K1_SYM "_Z7k1_proj6K1Args", "_Z7k2_attnPh"
#define K2_SYM "_Z7k2_attnPh", "_Z8k3_oprojPhPKtPt"
#define K3_SYM "_Z8k3_oprojPhPKtPt", "text_end_mark"
#define CODE_TOUCH2(S, sink, chunk, lane) CODE_TOUCH(S, sink, chunk, lane)

constexpr int HTB = 128 * 64 * 2;
constexpr int K1_X = 8 * HTB;
constexpr int K1_LDS = K1_X + 16384 + 2048;

DI int lds_byte(int r, int c) { const int st = (r >> 4) * 2 + (c >> 5), rr = r & 15, cc = c & 31, ob = rr * 64 + cc * 2; return st * 1024 + (ob ^ (((ob >> 9) & 1) << 5)); }
DI void stage_rc(int b, int& R, int& C) { const int st = b / 1024, sb = b % 1024, swz = sb ^ (((sb >> 9) & 1) << 5); R = (st >> 1) * 16 + swz / 64; C = (st & 1) * 32 + (swz % 64) / 2; }

struct K1Args {
    const bf16_t *x, *ak, *av, *shift_k, *ssm_k, *ssm_d, *ckv, *wq, *wuk, *wuv, *gate_w, *wo;
    unsigned char* ws;
};

#define SA(b, h) (((b) * 2 + (h)) * HTB)
#define SB(b, h) ((4 + (b) * 2 + (h)) * HTB)
constexpr int ring_slot(int u) {
    switch (u & 7) { case 5: return SB(0, 0); case 6: return SA(0, 0); case 7: return SB(0, 1); case 0: return SA(0, 1);
                     case 1: return SB(1, 0); case 2: return SA(1, 0); case 3: return SB(1, 1); default: return SA(1, 1); }
}
struct G1 {
    ldsp lds; unsigned ldsw  , voffA_kv, voffA_q, voffB, sv; int aoff, boff0, boff1;
    const char *A_kv, *A_q, *B_kv, *B_q, *sbase; unsigned sstep, sdst, sdstep; int sadd;
};
DI int lane_id() { int l; asm volatile("v_mbcnt_lo_u32_b32 %0, -1, 0\n\tv_mbcnt_hi_u32_b32 %0, -1, %0" : "=v"(l)); return l; }
template <int OFF> DI void dma16(unsigned lbase, unsigned voff, const char* g) {
    asm volatile("s_add_i32 m0, %0, %c3\n\ts_nop 0\n\tglobal_load_lds_dwordx4 %1, %2" :: "s"(lbase), "v"(voff), "s"(g), "i"(OFF) : "memory", "scc");
}
template <int BUF, int STRIDE, int H> DI void stage_a(const G1& g, const char* base, unsigned voff) {
    dma16<BUF>(g.ldsw, voff, base + H * 128 * STRIDE); dma16<BUF + 8192>(g.ldsw, voff, base + (H * 128 + 64) * STRIDE);
}
template <int BUF, int H> DI void stage_b(const G1& g, const char* base) {
    dma16<BUF>(g.ldsw, g.voffB, base + H * 256); dma16<BUF + 8192>(g.ldsw, g.voffB, base + H * 256 + 32 * NW_ * 2);
}
template <int U> DI void stage_unit(const G1& g) {
    if constexpr (U < 21) {
        constexpr int t = U / 3, r = U % 3;
        if constexpr (r == 0) stage_b<ring_slot(U), 0>(g, g.B_q + (size_t)t * (64 * NW_ * 2));
        else stage_a<ring_slot(U), DM * 2, r - 1>(g, g.A_q + t * 128, g.voffA_q);
    } else {
        constexpr int t = (U - 21) >> 2, r = (U - 21) & 3;
        if constexpr ((r & 1) == 0) stage_b<ring_slot(U), (r >> 1)>(g, g.B_kv + (size_t)t * (64 * NW_ * 2));
        else stage_a<ring_slot(U), DC * 2, (r >> 1)>(g, g.A_kv + t * 128, g.voffA_kv);
    }
}
DI void side(const G1& g, int t) {
    const int tt = t + g.sadd > 15 ? 15 : t + g.sadd;
    asm volatile("s_mov_b32 m0, %0\n\ts_nop 0\n\tglobal_load_lds_dword %1, %2" :: "s"(g.sdst + (unsigned)t * g.sdstep), "v"(g.sv), "s"(g.sbase + rfl((unsigned)tt * g.sstep)) : "memory");
}
DI void lda(bf16x8 (&dst)[4][2], const G1& g, int buf) {
#pragma unroll
    for (int m = 0; m < 4; ++m)
#pragma unroll
        for (int k = 0; k < 2; ++k) dst[m][k] = ldsrd16(g.lds + buf + g.aoff + m * 2048 + k * 1024);
}
DI void ldb(bf16x8 (&dst)[2][2], const G1& g, int buf) {
#pragma unroll
    for (int n = 0; n < 2; ++n)
#pragma unroll
        for (int k = 0; k < 2; ++k) { ldscp p = g.lds + buf + (n ? g.boff1 : g.boff0) + k * 8192; dst[n][k] = cat8(trread(p), trread(p + 256)); }
}
template <bool VT> DI void mma(f32x4 (&acc)[4][2], const bf16x8 (&At)[4][2], const bf16x8 (&Bt)[2][2]) {
    __builtin_amdgcn_s_setprio(1);
#pragma unroll
    for (int m = 0; m < 4; ++m)
#pragma unroll
        for (int n = 0; n < 2; ++n)
#pragma unroll
            for (int k = 0; k < 2; ++k) acc[m][n] = VT ? MFMA16(At[m][k], Bt[n][k], acc[m][n]) : MFMA16(Bt[n][k], At[m][k], acc[m][n]);
    __builtin_amdgcn_s_setprio(0);
}
#define WAIT_V(n) asm volatile("s_waitcnt vmcnt(" #n ")" ::: "memory")
#define WAIT_L(n) asm volatile("s_waitcnt lgkmcnt(" #n ")" ::: "memory")
#define BAR __builtin_amdgcn_s_barrier()
DI void zero8(f32x4 (&a)[4][2]) {
#pragma unroll
    for (int m = 0; m < 4; ++m) { a[m][0] = (f32x4){0.f, 0.f, 0.f, 0.f}; a[m][1] = (f32x4){0.f, 0.f, 0.f, 0.f}; }
}

DI void pack_kq(const f32x4 (&acc)[4][2], u32x4* pk, float scale) {
#pragma unroll
    for (int m = 0; m < 4; ++m) {
        const f32x4 a0 = acc[m][0] * scale, a1 = acc[m][1] * scale;
        const unsigned w0 = cvtpk(a0[0], a0[1]), w1 = cvtpk(a0[2], a0[3]), x0 = cvtpk(a1[0], a1[1]), x1 = cvtpk(a1[2], a1[3]);
        auto r0 = __builtin_amdgcn_permlane16_swap(w0, x0, false, false);
        auto r1 = __builtin_amdgcn_permlane16_swap(w1, x1, false, false);
        pk[m] = (u32x4){r0[0], r1[0], r0[1], r1[1]};
    }
}
DI unsigned kq_off(int pm, int wr, int wc, int fr, int fq) {
    return (unsigned)((pm * 8 + wr * 2) * 16384 + fr * 16 + (2 * wc + (fq & 1)) * 1024 + (fq >> 1) * 512);
}
DI void pack_v(const f32x4 (&acc)[4][2], u32x4* pk) {
#pragma unroll
    for (int n = 0; n < 2; ++n)
#pragma unroll
        for (int mm = 0; mm < 2; ++mm) {
            const f32x4 a0 = acc[2 * mm][n], a1 = acc[2 * mm + 1][n];
            const unsigned w0 = cvtpk(a0[0], a0[1]), w1 = cvtpk(a0[2], a0[3]), x0 = cvtpk(a1[0], a1[1]), x1 = cvtpk(a1[2], a1[3]);
            auto r0 = __builtin_amdgcn_permlane32_swap(w0, x0, false, false);
            auto r1 = __builtin_amdgcn_permlane32_swap(w1, x1, false, false);
            pk[2 * mm + n] = (u32x4){r0[0], r1[0], r0[1], r1[1]};
        }
}
#define ST_AUX 16
struct Out1 { u32x4 pk[16]; __amdgpu_buffer_rsrc_t rs; unsigned voff; int sbj, shi; };
template <int I> DI void st_piece(const Out1& o) {
    __builtin_amdgcn_raw_buffer_store_b128(o.pk[I], o.rs, o.voff + (I & 1) * 256, (I >> 3) * 65536 + ((I >> 2) & 1) * o.sbj + ((I >> 1) & 1) * o.shi, ST_AUX);
}
template <int A, int B> DI void st_range(const Out1& o) { if constexpr (A < B) { st_piece<A>(o); st_range<A + 1, B>(o); } }

template <int N, class W> DI void qphase(const G1& g, f32x4 (&a0)[4][2], f32x4 (&a1)[4][2], bf16x8 (&At)[4][2], bf16x8 (&B0)[2][2], W& warm) {
    constexpr int t = N >> 1;
    if constexpr ((N & 1) == 0) {
        ldb(B0, g, ring_slot(3 * t)); SBAR(); lda(At, g, ring_slot(3 * t + 1));
        if constexpr (t < 5) warm(t);
        if constexpr (N >= 2) stage_unit<3 * t + 6>(g);
        if constexpr (N == 0) WAIT_V(10); else WAIT_V(8);
        WAIT_L(8); BAR; WAIT_L(0); mma<false>(a0, At, B0); BAR; SBAR();
    } else {
        lda(At, g, ring_slot(3 * t + 2));
        if constexpr (t >= 1) stage_unit<3 * t + 7>(g);
        stage_unit<3 * t + 8>(g);
        WAIT_V(8);
        if constexpr (N == 13) WAIT_L(0);
        BAR; WAIT_L(0); mma<false>(a1, At, B0); BAR; SBAR();
    }
}
template <class W, int... Ns> DI void qphases(const G1& g, f32x4 (&a0)[4][2], f32x4 (&a1)[4][2], bf16x8 (&At)[4][2], bf16x8 (&B0)[2][2], W& warm, std::integer_sequence<int, Ns...>) {
    (qphase<Ns>(g, a0, a1, At, B0, warm), ...);
}
template <int... Us> DI void stage_units(const G1& g, std::integer_sequence<int, Us...>) { (stage_unit<Us>(g), ...); }

template <bool VT, int POS, class W>
DI void mainpair(const G1& g, int t, f32x4 (&acc)[2][2][4][2], bf16x8 (&At)[4][2], bf16x8 (&B0)[2][2], bf16x8 (&B1)[2][2], W& lastside) {
    const char* a1 = g.A_kv + (t + 1) * 128; const char* a2 = a1 + 128; const char* a3 = a1 + 256;
    const char* b2 = g.B_kv + (size_t)(t + 2) * (64 * NW_ * 2); const char* b3 = b2 + 64 * NW_ * 2;
    ldb(B0, g, SB(0, 0)); SBAR(); lda(At, g, SA(0, 0));
    if constexpr (POS != 0) stage_a<SA(1, 1), DC * 2, 1>(g, a1, g.voffA_kv); else WAIT_V(18);
    WAIT_L(8); BAR; WAIT_L(0); mma<VT>(acc[0][0], At, B0); BAR; SBAR();
    ldb(B1, g, SB(0, 1));
    if constexpr (POS == 2) lastside(0); else side(g, t);
    if constexpr (POS != 2) stage_b<SB(0, 0), 0>(g, b2);
    if constexpr (POS == 0) WAIT_V(19);
    BAR; WAIT_L(0); mma<VT>(acc[0][1], At, B1); BAR;
    lda(At, g, SA(0, 1));
    if constexpr (POS != 2) stage_a<SA(0, 0), DC * 2, 0>(g, a2, g.voffA_kv);
    BAR; WAIT_L(0); mma<VT>(acc[1][0], At, B0); BAR; SBAR();
    if constexpr (POS != 2) stage_b<SB(0, 1), 1>(g, b2);
    if constexpr (POS == 0) WAIT_V(15); else if constexpr (POS == 1) WAIT_V(7); else WAIT_V(1);
    BAR; mma<VT>(acc[1][1], At, B1); BAR;
    ldb(B0, g, SB(1, 0)); SBAR(); lda(At, g, SA(1, 0));
    if constexpr (POS != 2) stage_a<SA(0, 1), DC * 2, 1>(g, a2, g.voffA_kv);
    WAIT_L(8); BAR; WAIT_L(0); mma<VT>(acc[0][0], At, B0); BAR; SBAR();
    ldb(B1, g, SB(1, 1));
    if constexpr (POS == 2) lastside(1); else side(g, t + 1);
    if constexpr (POS != 2) stage_b<SB(1, 0), 0>(g, b3);
    BAR; WAIT_L(0); mma<VT>(acc[0][1], At, B1); BAR;
    lda(At, g, SA(1, 1));
    if constexpr (POS != 2) stage_a<SA(1, 0), DC * 2, 0>(g, a3, g.voffA_kv);
    BAR; WAIT_L(0); mma<VT>(acc[1][0], At, B0); BAR; SBAR();
    if constexpr (POS != 2) { stage_b<SB(1, 1), 1>(g, b3); WAIT_V(7); }
    BAR; mma<VT>(acc[1][1], At, B1); BAR;
}

constexpr int CV_RAW = SA(0, 0);
constexpr int K1_WO = SA(0, 1);
constexpr int CV_HSTR = 4256;
constexpr int CV_H = 2 * HTB;
constexpr int CV_USTR = 4608;
constexpr int CV_U = CV_H + 8 * CV_HSTR;
constexpr int CV_R = CV_U + 2 * CV_USTR;
static_assert(CV_R + 65536 <= K1_LDS && CV_R <= K1_X, "lds");

DI void tail_stage(const G1& g, const K1Args& P, int c0, int e, int wid, int lane) {
    dma16<CV_RAW>(g.ldsw, (unsigned)lane * 16u, (const char*)P.ssm_k + (size_t)c0 * (SEQ * 2) + wid * 1024);
    if (wid < 7) {
        const char* wg = (const char*)P.wo + ((size_t)((e >> 6) * 1024 + (e & 15) * 64) * DM + ((e >> 4) & 3) * 112) * 2;
        { const int ci = wid * 64 + lane, k = ci / 14, cc = ci - k * 14; dma16<K1_WO>(g.ldsw, (unsigned)(k * DM + cc * 8) * 2u, wg); }
        { const int ci = (wid + 7) * 64 + lane, k = ci / 14, cc = ci - k * 14; dma16<K1_WO + 7168>(g.ldsw, (unsigned)(k * DM + cc * 8) * 2u, wg); }
    }
}
DI void wo_image(ldsp lds, const K1Args& P, int e, int wid, int lane) {
    const int fr = lane & 15, fq = lane >> 4;
    if (wid < 7) {
        ldscp wp = lds + K1_WO + (16 * (fq >> 1) + 4 * (fq & 1) + (fr >> 2)) * 224 + (fr & 3) * 8 + wid * 32;
        unsigned char* op = P.ws + WS_WI + (size_t)e * 14336 + wid * 1024 + lane * 16;
#pragma unroll
        for (int kk = 0; kk < 2; ++kk) *(bf16x8*)(op + kk * 7168) = cat8(trread(wp + kk * 7168), trread(wp + kk * 7168 + 1792));
    }
}
DI void conv_copies(ldsp q, const u32x4 d, const u32x2 e) {
    const unsigned a0 = __builtin_amdgcn_alignbit(d[1], d[0], 16), a1 = __builtin_amdgcn_alignbit(d[2], d[1], 16), a2 = __builtin_amdgcn_alignbit(d[3], d[2], 16),
                   a3 = __builtin_amdgcn_alignbit(e[0], d[3], 16), a4 = __builtin_amdgcn_alignbit(e[1], e[0], 16);
    *(LAS u32x4*)(q) = (u32x4){a0, a1, a2, a3};
    *(LAS u32x4*)(q + CV_HSTR) = (u32x4){d[1], d[2], d[3], e[0]};
    *(LAS u32x4*)(q + 2 * CV_HSTR) = (u32x4){a1, a2, a3, a4};
    *(LAS u32x4*)(q + 3 * CV_HSTR) = (u32x4){d[2], d[3], e[0], e[1]};
}
DI void conv_frags(bf16x8 (&A)[8], bf16x8 (&B)[8], ldscp hp, ldscp up) {
#pragma unroll
    for (int v = 0; v < 8; ++v) {
        B[v] = ldsrd16(up + (7 - v) * 512);
        const s16x4 lo = *(const LAS s16x4*)(hp + v * 512), hi = *(const LAS s16x4*)(hp + v * 512 + 8);
        A[v] = cat8(lo, hi);
    }
}
DI void conv_mma(f32x4 (&acc)[8], const bf16x8 (&A)[8], const bf16x8 (&B)[8]) {
#pragma unroll
    for (int J = 0; J < 8; ++J)
#pragma unroll
        for (int v = 0; v <= J; ++v) acc[J] = MFMA16(A[v], B[J - v], acc[J]);
}
DI void conv_part(ldsp lds, const K1Args& P, int c0, const u32x4 sw, const unsigned dw, int wid, int lane, const Out1& o) {
    const int tid = wid * 64 + lane, fr = lane & 15, fq = lane >> 4;
    {
        const int cq = tid & 255;
        const u32x4 d = *(const LAS u32x4*)(lds + CV_RAW + tid * 16);
        u32x2 e = *(const LAS u32x2*)(lds + CV_RAW + tid * 16 + 16);
        const u32x4 z4 = {0u, 0u, 0u, 0u}; const u32x2 z2 = {0u, 0u};
        if (cq == 255) e = z2;
        ldsp q = lds + CV_H + (tid >> 8) * 4 * CV_HSTR + (cq + 2) * 16;
        conv_copies(q, d, e);
        if (cq == 0) { conv_copies(q - 16, z4, (u32x2){d[0], d[1]}); conv_copies(q - 32, z4, z2); }
        if (cq == 255) { conv_copies(q + 16, z4, z2); conv_copies(q + 32, z4, z2); }
    }
    SBAR(); st_range<4, 6>(o); SBAR();
    {
        ldscp kp = lds + K1_X + tid * 16;
        u32x4 a = *(const LAS u32x4*)(kp - 16);
        const u32x4 b = *(const LAS u32x4*)(kp), vr = *(const LAS u32x4*)(kp + 8192);
        if (tid == 0) a = (u32x4){0u, 0u, 0u, 0u};
        const unsigned kr[7] = {a[1], a[2], a[3], b[0], b[1], b[2], b[3]};
#pragma unroll
        for (int c = 0; c < 2; ++c) {
            const float w0 = bf_lo(sw[2 * c]), w1 = bf_hi(sw[2 * c]), w2 = bf_lo(sw[2 * c + 1]), w3 = bf_hi(sw[2 * c + 1]);
            float kc[7], kv[4];
#pragma unroll
            for (int i = 0; i < 7; ++i) kc[i] = c ? bf_hi(kr[i]) : bf_lo(kr[i]);
#pragma unroll
            for (int i = 0; i < 4; ++i) {
                float s = rbf(kc[i] * w3);
                s = rbf(s + rbf(kc[i + 1] * w2));
                s = rbf(s + rbf(kc[i + 2] * w1));
                s = rbf(s + rbf(kc[i + 3] * w0));
                kv[i] = s * (c ? bf_hi(vr[i]) : bf_lo(vr[i]));
            }
            u32x2 wv = {cvtpk(kv[3], kv[2]), cvtpk(kv[1], kv[0])};
            *(LAS u32x2*)(lds + CV_U + c * CV_USTR + (2044 - 4 * tid) * 2) = wv;
            SBAR(); if (c == 0) st_range<6, 8>(o); else st_range<8, 10>(o); SBAR();
        }
        if (tid < 120) { const int c = tid / 60, j = tid - c * 60; *(LAS u32x2*)(lds + CV_U + c * CV_USTR + 4096 + j * 8) = (u32x2){0u, 0u}; }
    }
    __syncthreads();
    TS(P.ws, 0, 5);
    const int ch = wid >> 2, qw = wid & 3;
    {
        const int y1 = 24 + fr - 8 * fq;
        ldscp hb = lds + CV_H + ch * 4 * CV_HSTR + (y1 & 3) * CV_HSTR + (y1 & ~3) * 2 + qw * 128;
        ldscp ub = lds + CV_U + ch * CV_USTR + (2056 - 256 * 7 - 16 * fr - 8 * fq) * 2 + qw * 128;
        f32x4 acc[8];
#pragma unroll
        for (int i = 0; i < 8; ++i) acc[i] = (f32x4){0.f, 0.f, 0.f, 0.f};
        bf16x8 A0[8], B0[8], A1[8], B1[8];
        conv_frags(A0, B0, hb, ub); SBAR();
        conv_frags(A1, B1, hb + 64, ub + 64); SBAR();
        st_range<10, 12>(o); SBAR();
        conv_mma(acc, A0, B0); SBAR();
        st_range<12, 14>(o); SBAR();
        conv_mma(acc, A1, B1); SBAR();
        st_range<14, 16>(o); SBAR();
#pragma unroll
        for (int i = 0; i < 8; ++i) *(LAS f32x4*)(lds + CV_R + wid * 8192 + i * 1024 + lane * 16) = acc[i];
    }
    __syncthreads();
    TS(P.ws, 0, 6);
    {
        const float dd = ch ? bf_hi(dw) : bf_lo(dw);
#pragma unroll
        for (int i = 0; i < 2; ++i) {
            ldscp rp = lds + CV_R + ch * 32768 + (2 * qw + i) * 1024 + lane * 16;
            const f32x4 y = ((*(const LAS f32x4*)rp + *(const LAS f32x4*)(rp + 8192)) + *(const LAS f32x4*)(rp + 16384)) + *(const LAS f32x4*)(rp + 24576);
            const int t = 256 * (2 * qw + i) + 16 * fr + 4 * fq;
            const u32x2 uw = *(const LAS u32x2*)(lds + CV_U + ch * CV_USTR + (2044 - t) * 2);
            const float kv[4] = {bf_hi(uw[1]), bf_lo(uw[1]), bf_hi(uw[0]), bf_lo(uw[0])};
            float r[4];
#pragma unroll
            for (int e = 0; e < 4; ++e) r[e] = rbf(y[e]) + rbf(kv[e] * dd);
            u32x2 w = {cvtpk(r[0], r[1]), cvtpk(r[2], r[3])};
            *(u32x2*)(P.ws + WS_Y2 + ((size_t)(c0 + ch) * SEQ + t) * 2) = w;
        }
    }
}

DI void gate_part(const K1Args& P, int row0, int wid, int lane) {
    const int lc = lane < 56 ? lane : 0;
    u32x4 gw = *(const u32x4*)(P.gate_w + lc * 8);
    float* gp = (float*)(P.ws + WS_G);
    u32x4 xv[8];
#pragma unroll
    for (int r = 0; r < 8; ++r) xv[r] = *(const u32x4*)(P.x + (size_t)(row0 + wid * 8 + r) * DM + lc * 8);
    float mine = 0.f;
#pragma unroll
    for (int r = 0; r < 8; ++r) {
        float s = 0.f;
#pragma unroll
        for (int j = 0; j < 4; ++j) { s += rbf(bf_lo(xv[r][j]) * bf_lo(gw[j])); s += rbf(bf_hi(xv[r][j]) * bf_hi(gw[j])); }
        if (lane >= 56) s = 0.f;
#pragma unroll
        for (int o = 32; o >= 1; o >>= 1) s += __shfl_xor(s, o);
        if (lane == r) mine = s;
    }
    const float z = rbf(mine);
    const float g = rbf(1.0f / (1.0f + __expf(-z)));
    if (lane < 8) gp[row0 + wid * 8 + lane] = g;
}

template <bool VT>
DI void big_tile(const G1& g, const K1Args& P, int pm, int head, int c0, int e, int wid, bf16x8 (&At)[4][2], bf16x8 (&B0)[2][2], Out1& o) {
    f32x4 acc[2][2][4][2];
    zero8(acc[0][0]); zero8(acc[0][1]); zero8(acc[1][0]); zero8(acc[1][1]);
    bf16x8 B1[2][2];
    auto lastside = [&](int s) {
        if (wid < 4) side(g, 14 + s);
        else if (s == 0) {
            const unsigned l = (unsigned)lane_id();
            if (wid == 4) touch(g.sdst, l * 128u, (const char*)P.ssm_k + (size_t)c0 * (SEQ * 2));
            else touch(g.sdst, l * (DM * 2), (const char*)P.wo + ((size_t)((e >> 6) * 1024 + (e & 15) * 64) * DM + ((e >> 4) & 3) * 112) * 2 + (wid - 5) * 110);
        }
    };
    mainpair<VT, 0>(g, 0, acc, At, B0, B1, lastside);
    for (int t = 2; t < 14; t += 2) mainpair<VT, 1>(g, t, acc, At, B0, B1, lastside);
    mainpair<VT, 2>(g, 14, acc, At, B0, B1, lastside);
    TS(P.ws, 0, 3);
    const int lane = lane_id();
    tail_stage(g, P, c0, e, wid, lane);
    if (wid == 7) { if (e & 8) CODE_TOUCH2(K3_SYM, g.sdst, e & 7, lane); else CODE_TOUCH2(K2_SYM, g.sdst, e & 7, lane); }
    SBAR();
    const int wr = wid >> 2, wc = wid & 3, fr = lane & 15, fq = lane >> 4;
    if (VT) {
        o.voff = (unsigned)((pm * 4 + wr) * 32768 + wc * 4096 + (fq >> 1) * 1024 + (fq & 1) * 512 + fr * 16);
        pack_v(acc[0][0], o.pk); pack_v(acc[0][1], o.pk + 4); pack_v(acc[1][0], o.pk + 8); pack_v(acc[1][1], o.pk + 12);
    } else {
        o.voff = kq_off(pm, wr, wc, fr, fq);
        pack_kq(acc[0][0], o.pk, 1.0f); pack_kq(acc[0][1], o.pk + 4, 1.0f); pack_kq(acc[1][0], o.pk + 8, 1.0f); pack_kq(acc[1][1], o.pk + 12, 1.0f);
    }
}

__global__ __launch_bounds__(512, 2) void k1_proj(K1Args P) {
    extern __shared__ __attribute__((aligned(16))) unsigned char shm[];
    ldsp lds = (ldsp)shm;
    const int b = blockIdx.x, xcd = b & 7, j = b >> 3, li = j & 15, pm = li & 7, head = 2 * xcd + (li >> 3), vt = j >> 4;
    const int tid = threadIdx.x, wid = rfl(tid >> 6), lane = tid & 63, wr = wid >> 2, wc = wid & 3, fr = lane & 15, fq = lane >> 4;
    const int c0 = (xcd * 28 + (j < 28 ? j : 0)) * 2, e = xcd * 32 + j;
    TS(P.ws, 0, 0);
    if (wid == 7) CODE_TOUCH2(K1_SYM, (unsigned)(uintptr_t)lds + K1_X + 16384 + 7 * 256, j, lane);
    u32x4 sw = *(const CAS u32x4*)(uintptr_t)(P.shift_k + c0 * 4); unsigned dw = *(const CAS unsigned*)(uintptr_t)(P.ssm_d + c0);
    G1 g;
    const unsigned lds0 = (unsigned)(uintptr_t)lds;
    g.lds = lds; g.ldsw = lds0 + (unsigned)wid * 1024u;
    { int R, C; stage_rc(tid * 16, R, C); g.voffA_kv = (unsigned)(R * DC + C) * 2u; g.voffA_q = (unsigned)(R * DM + C) * 2u; }
    { const int kg = wid >> 1, row = (lane >> 2) & 7, ch = (lane & 3) ^ (2 * (kg & 1)), n = 32 * (2 * (wid & 1) + (lane >> 5)) + 8 * ch;
      g.voffB = (unsigned)((8 * kg + row) * NW_ + n) * 2u; }
    g.aoff = lds_byte(wr * 64 + fr, fq * 8);
    g.boff0 = fq * 2048 + wc * 512 + (fr >> 2) * 64 + (32 * (fq & 1)) + 8 * (fr & 3);
    g.boff1 = g.boff0 ^ 32;
    g.A_kv = (const char*)P.ckv + (size_t)pm * 256 * DC * 2;
    g.A_q = (const char*)P.x + (size_t)pm * 256 * DM * 2;
    g.B_kv = (const char*)(vt ? P.wuv : P.wuk) + head * 512;
    g.B_q = (const char*)P.wq + head * 512 + vt * 256;
    if (wid < 4) {
        g.sbase = (const char*)(wid < 2 ? P.ak : P.av) + (wid & 1) * (64 * DM * 2) + c0 * 2; g.sstep = 128 * DM * 2; g.sv = (unsigned)lane * (DM * 2);
        g.sadd = 0; g.sdst = lds0 + K1_X + (wid >> 1) * 8192 + (wid & 1) * 256; g.sdstep = 512;
    } else {
        if (wid == 4) { g.sbase = g.A_kv + (size_t)((head & 1) * 2 + vt) * 64 * (DC * 2); g.sstep = 128; g.sv = (unsigned)lane * (DC * 2); }
        else if (wid == 5) { g.sbase = g.B_kv + (size_t)pm * 8 * (NW_ * 2); g.sstep = 64 * NW_ * 2; g.sv = (unsigned)((lane >> 2) & 7) * (NW_ * 2) + (lane & 3) * 128; }
        else { g.sbase = (const char*)(wid == 6 ? P.ak : P.av) + (size_t)j * 4 * (DM * 2) + xcd * 112; g.sstep = 128 * DM * 2; g.sv = (unsigned)(lane & 3) * (DM * 2) + ((lane >> 2) & 1) * 108; }
        g.sadd = 4; g.sdst = lds0 + K1_X + 16384 + wid * 256; g.sdstep = 0;
    }
    PIN(g.voffA_kv); PIN(g.voffB); PIN(g.sv); PIN(g.aoff); PIN(g.boff0); PIN(g.boff1);
    stage_units(g, std::make_integer_sequence<int, 8>{});
    WAIT_V(12);
    auto warm = [&](int slot) {
        const unsigned sink = lds0 + K1_X + 16384 + wid * 256;
        if (slot > 0) { if (wid >= 4) side(g, slot - 5); }
        else if (wid < 3) touch(sink, (unsigned)lane * (DM * 2), g.A_q + (size_t)((head & 1) * 2 + vt) * 64 * (DM * 2) + (4 + wid) * 128);
        else if (wid == 3) touch(sink, (unsigned)((lane < 48 ? lane : 0) >> 1) * (NW_ * 2) + (lane & 1) * 128, g.B_q + (size_t)(256 + pm * 24) * (NW_ * 2));
    };
    asm volatile("" : "+s"(sw), "+s"(dw));
    if (b == 0 && tid < N_AFLAGS + N_OFLAGS) *(u32x4*)((unsigned*)(P.ws + WS_FLAGS) + tid * FLAG_STRIDE) = (u32x4){0u, 0u, 0u, 0u};
    bf16x8 At[4][2], B0[2][2];
    BAR;
    if (wr == 1) BAR;
    TS(P.ws, 0, 1);
    {
        f32x4 q0[4][2], q1[4][2];
        zero8(q0); zero8(q1);
        qphases(g, q0, q1, At, B0, warm, std::make_integer_sequence<int, 14>{});
        stage_unit<27>(g); stage_unit<28>(g); SBAR();
        Out1 o;
        o.rs = __builtin_amdgcn_make_buffer_rsrc(P.ws + WS_Q + (size_t)head * MB, (short)0, (int)MB, 0x00020000);
        o.voff = kq_off(pm, wr, wc, fr, fq) + vt * 8192; o.sbj = 65536; o.shi = 16384;
        pack_kq(q0, o.pk, 0.0625f * 1.4426950408889634f); pack_kq(q1, o.pk + 4, 0.0625f * 1.4426950408889634f);
        st_range<0, 8>(o);
        SBAR();
    }
    TS(P.ws, 0, 2);
    Out1 o;
    if (vt) big_tile<true>(g, P, pm, head, c0, e, wid, At, B0, o); else big_tile<false>(g, P, pm, head, c0, e, wid, At, B0, o);
    o.rs = __builtin_amdgcn_make_buffer_rsrc(P.ws + (vt ? WS_V : WS_K) + (size_t)head * MB, (short)0, (int)MB, 0x00020000);
    o.sbj = vt ? 16384 : 8192; o.shi = vt ? 2048 : 16384;
    SBAR(); st_range<0, 4>(o); SBAR();
    WAIT_V(4);
    if (wr == 0) BAR;
    BAR;
    TS(P.ws, 0, 4);
    const int lane2 = lane_id();
    wo_image(lds, P, e, wid, lane2);
    if (j < 28) conv_part(lds, P, c0, sw, dw, wid, lane2, o);
    else { st_range<4, 16>(o); gate_part(P, (xcd * 4 + (j - 28)) * 64, wid, lane2); }
    TS(P.ws, 0, 7);
}
#undef SA
#undef SB

constexpr int AT_K = 0, AT_V = 65536, AT_LDS = 163840;
constexpr float THR = 8.0f;
constexpr int PD = 6;
constexpr int NEB = 24;
template <int... Is> using iseq = std::integer_sequence<int, Is...>;
template <int N> using make_iseq = std::make_integer_sequence<int, N>;
#define ACLOB "a0","a1","a2","a3","a4","a5","a6","a7","a8","a9","a10","a11","a12","a13","a14","a15","a16","a17","a18","a19","a20","a21","a22","a23","a24","a25","a26","a27","a28","a29","a30","a31","a32","a33","a34","a35","a36","a37","a38","a39","a40","a41","a42","a43","a44","a45","a46","a47","a48","a49","a50","a51","a52","a53","a54","a55","a56","a57","a58","a59","a60","a61","a62","a63","a64","a65","a66","a67","a68","a69","a70","a71","a72","a73","a74","a75","a76","a77","a78","a79","a80","a81","a82","a83","a84","a85","a86","a87","a88","a89","a90","a91","a92","a93","a94","a95","a96","a97","a98","a99","a100","a101","a102","a103","a104","a105","a106","a107","a108","a109","a110","a111","a112","a113","a114","a115","a116","a117","a118","a119","a120","a121","a122","a123","a124","a125","a126","a127","a128","a129","a130","a131","a132","a133","a134","a135","a136","a137","a138","a139","a140","a141","a142","a143","a144","a145","a146","a147","a148","a149","a150","a151","a152","a153","a154","a155","a156","a157","a158","a159","a160","a161","a162","a163","a164","a165","a166","a167","a168","a169","a170","a171","a172","a173","a174","a175","a176","a177","a178","a179","a180","a181","a182","a183","a184","a185","a186","a187","a188","a189","a190","a191","a192","a193","a194","a195","a196","a197","a198","a199","a200","a201","a202","a203","a204","a205","a206","a207","a208","a209","a210","a211","a212","a213","a214","a215","a216","a217","a218","a219","a220","a221","a222","a223","a224","a225","a226","a227","a228","a229","a230","a231","a232","a233","a234","a235","a236","a237","a238","a239","a240","a241","a242","a243","a244","a245","a246","a247","a248","a249","a250","a251","a252","a253","a254","a255"

#define PH(i) do { } while (0)
struct AT {
    f32x16 s[2][2];
    f32x16 cm[2];
    u32x4 p[4];
    bf16x8 fr[8];
    float m, l, rs0, rs1, mx0, mx1, alpha, aone;
    bool pend, first, drain;
    ldscp kb;
    ldscp vcur;
    unsigned goff;
    unsigned m0k, m0v;
    const char *gk, *gv;
};

DI void dma_piece(unsigned m0v, unsigned goff, const char* g, const int off) {
    switch (off) {
    case 0: asm volatile("s_mov_b32 m0, %0\n\ts_nop 0\n\tglobal_load_lds_dwordx4 %1, %2" :: "s"(m0v), "v"(goff), "s"(g) : "memory"); break;
    case 1: asm volatile("s_mov_b32 m0, %0\n\ts_nop 0\n\tglobal_load_lds_dwordx4 %1, %2 offset:1024" :: "s"(m0v), "v"(goff), "s"(g) : "memory"); break;
    case 2: asm volatile("s_mov_b32 m0, %0\n\ts_nop 0\n\tglobal_load_lds_dwordx4 %1, %2 offset:2048" :: "s"(m0v), "v"(goff), "s"(g) : "memory"); break;
    default: asm volatile("s_mov_b32 m0, %0\n\ts_nop 0\n\tglobal_load_lds_dwordx4 %1, %2 offset:3072" :: "s"(m0v), "v"(goff), "s"(g) : "memory"); break;
    }
}
template <int I> DI void dma8(unsigned m0base, unsigned goff, const char* g) { dma_piece(m0base + (I >> 2) * 4096, goff, g + (I >> 2) * 4096, I & 3); }
DI void dma_tile(unsigned m0base, unsigned goff, const char* g) {
    dma8<0>(m0base, goff, g); dma8<1>(m0base, goff, g); dma8<2>(m0base, goff, g); dma8<3>(m0base, goff, g);
    dma8<4>(m0base, goff, g); dma8<5>(m0base, goff, g); dma8<6>(m0base, goff, g); dma8<7>(m0base, goff, g);
}
DI float ex2(float x) { return __builtin_amdgcn_exp2f(x); }
DI float MX3(float a, float b, float c) { float r; asm("v_max3_f32 %0, %1, %2, %3" : "=v"(r) : "v"(a), "v"(b), "v"(c)); return r; }
DI float MX2(float a, float b) { float r; asm("v_max_f32_e32 %0, %1, %2" : "=v"(r) : "v"(a), "v"(b)); return r; }
template <int D0> DI void qk_init(f32x16& s, const bf16x8& k, const f32x16& c) {
    asm volatile("v_mfma_f32_32x32x16_bf16 %0, %1, a[%c3:%c4], %2" : "=&v"(s) : "v"(k), "v"(c), "i"(128 + 4 * D0), "i"(131 + 4 * D0)); }
template <int D0> DI void qk_acc(f32x16& s, const bf16x8& k) {
    asm volatile("v_mfma_f32_32x32x16_bf16 %0, %1, a[%c2:%c3], %0" : "+v"(s) : "v"(k), "i"(128 + 4 * D0), "i"(131 + 4 * D0)); }
template <int DB> DI void pv_acc(const bf16x8& v, const u32x4& p) {
    asm volatile("v_mfma_f32_32x32x16_bf16 a[%c2:%c3], %0, %1, a[%c2:%c3]" :: "v"(v), "v"(p), "i"(16 * DB), "i"(16 * DB + 15)); }
DI void add_bcast(f32x16& x, float aone, float b) { asm volatile("s_nop 1\n\tv_mfma_f32_32x32x2_f32 %0, %1, %2, %0" : "+v"(x) : "v"(aone), "v"(b)); }
#define NOPS16() asm volatile("s_nop 7\n\ts_nop 7" ::: "memory")
#define NOPS2() asm volatile("s_nop 1" ::: "memory")
template <int I> DI void a_zero() { asm volatile("v_accvgpr_write_b32 a[%c0], 0" :: "i"(I)); }
template <int I> DI void a_scale(float f) { float t; asm volatile("v_accvgpr_read_b32 %0, a[%c2]\n\tv_mul_f32 %0, %1, %0\n\tv_accvgpr_write_b32 a[%c2], %0" : "=&v"(t) : "v"(f), "i"(I)); }
template <int I> DI void a_qmov() { asm volatile("v_accvgpr_mov_b32 a[%c0], a[%c1]" :: "i"(128 + I), "i"(192 + I)); }
template <int I> DI float a_read() { float t; asm volatile("v_accvgpr_read_b32 %0, a[%c1]" : "=v"(t) : "i"(I)); return t; }
template <int F> DI void q_load(unsigned voff, const char* g) {
    asm volatile("global_load_dwordx4 a[%c2:%c3], %0, %1 offset:%c4" :: "v"(voff), "s"(g + ((F & 15) >> 2) * 4096), "i"(128 + 4 * F), "i"(131 + 4 * F), "i"((F & 3) * 1024) : "memory"); }
template <int Q> DI void o_store(unsigned voff, const char* g) {
    asm volatile("global_store_dwordx4 %0, a[%c2:%c3], %1 offset:%c4 sc1" :: "v"(voff), "s"(g + (Q >> 2) * 4096), "i"(4 * Q), "i"(4 * Q + 3), "i"((Q & 3) * 1024) : "memory"); }
template <int... Is> DI void a_zeros(iseq<Is...>) { (a_zero<Is>(), ...); }
template <int... Is> DI void a_scales(float f, iseq<Is...>) { (a_scale<Is>(f), ...); }
template <int... Is> DI void a_qmovs(iseq<Is...>) { (a_qmov<Is>(), ...); }
template <int B, int... Is> DI void q_loads(unsigned voff, const char* g, iseq<Is...>) { (q_load<B + Is>(voff, g), ...); }
template <int... Is> DI void o_stores(unsigned voff, const char* g, iseq<Is...>) { (o_store<Is>(voff, g), ...); }
template <int B, int... Is> DI void a_reads(float (&o)[16], iseq<Is...>) { ((o[Is] = a_read<B + Is>()), ...); }

template <int KS, int R> DI bf16x8 kfrag(const AT& c) { return ldsrd16(c.kb + KS * 32768 + ((R & 1) * 16 + (R >> 1)) * 1024); }
template <int R> DI bf16x8 vfrag(ldscp v) { return ldsrd16(v + (R & 7) * 4096 + (R >> 3) * 1024); }
template <int X, int E> DI void exp_sum(AT& c) {
    const float e = ex2(c.s[X][E >> 4][E & 15]); c.s[X][E >> 4][E & 15] = e;
    if constexpr (E & 1) c.rs1 += e; else c.rs0 += e;
}
template <int X, int W> DI void pack1(AT& c) {
    c.p[W >> 2][W & 3] = cvtpk(c.s[X][W >> 3][(2 * W) & 15], c.s[X][W >> 3][(2 * W + 1) & 15]);
}
template <int X, int MODE, int N> DI void gapA(AT& c) {
    constexpr int d0 = N >> 1, kb = N & 1, Y = X ^ 1;
    if constexpr (d0 == 0) qk_init<0>(c.s[Y][kb], c.fr[N & 7], c.cm[(MODE & 4) ? kb : 0]);
    else qk_acc<d0>(c.s[Y][kb], c.fr[N & 7]);
    constexpr int R = N + PD;
    if constexpr (R < 32) c.fr[R & 7] = kfrag<Y, R>(c); else if constexpr (MODE & 1) c.fr[R & 7] = vfrag<R - 32>(c.vcur);
    if constexpr (MODE & 1) {
        if constexpr (N < 32 - NEB) { exp_sum<X, NEB + N>(c); PIN(c.rs0); PIN(c.rs1); }
        if constexpr (N >= 10 && N < 26) { pack1<X, N - 10>(c); PIN(c.p[(N - 10) >> 2]); }
    }
    if constexpr ((MODE & 2) && (N & 1)) {
        if constexpr (N < 16) dma8<(N >> 1)>(c.m0k, c.goff, c.gk); else dma8<((N - 16) >> 1)>(c.m0v, c.goff, c.gv);
    }
    SBAR();
}
template <int X> DI void decide(AT& c) {
    constexpr int Y = X ^ 1;
    float mm = MX2(c.mx0, c.mx1);
    auto rr = __builtin_amdgcn_permlane32_swap(__float_as_uint(mm), __float_as_uint(mm), false, false);
    const float rm = MX2(__uint_as_float(rr[0]), __uint_as_float(rr[1]));
    if (__builtin_expect(c.first || __any(rm > THR), 0)) {
        const float dl = c.first ? rm : MX2(rm, 0.f);
        c.m += dl; c.alpha = ex2(-dl); c.pend = !c.first;
        const float nd = -dl;
        add_bcast(c.s[Y][0], c.aone, nd); add_bcast(c.s[Y][1], c.aone, nd); add_bcast(c.cm[0], c.aone, nd);
        asm volatile("s_nop 7\n\ts_nop 7\n\ts_nop 3" : "+v"(c.s[Y][0]), "+v"(c.s[Y][1]), "+v"(c.cm[0]));
    }
}
template <int X, int I> DI void maxstep(AT& c) {
    constexpr int Y = X ^ 1, h = I >> 2, r = 4 * (I & 3);
    if constexpr (I == 0) { c.mx0 = MX2(c.s[Y][0][0], c.s[Y][0][1]); c.mx1 = MX2(c.s[Y][0][2], c.s[Y][0][3]); }
    else { c.mx0 = MX3(c.mx0, c.s[Y][h][r], c.s[Y][h][r + 1]); c.mx1 = MX3(c.mx1, c.s[Y][h][r + 2], c.s[Y][h][r + 3]); }
}
template <int X, int MODE, int N> DI void gapB(AT& c) {
    constexpr int ks = N >> 3, db = N & 7, Y = X ^ 1;
    pv_acc<db>(c.fr[N & 7], c.p[ks]);
    constexpr int R = N + PD;
    if constexpr (R < 32) c.fr[R & 7] = vfrag<R>(c.vcur);
    if constexpr (MODE & 1) {
        if constexpr (MODE & 2) {
            if constexpr (N == 28) { c.fr[0] = kfrag<X, 0>(c); c.fr[1] = kfrag<X, 1>(c); }
            if constexpr (N == 29) { c.fr[2] = kfrag<X, 2>(c); c.fr[3] = kfrag<X, 3>(c); }
            if constexpr (N == 30) { c.fr[4] = kfrag<X, 4>(c); }
            if constexpr (N == 31) { c.fr[5] = kfrag<X, 5>(c); }
        }
        if constexpr (N == 1) { asm volatile("s_nop 7" : "+v"(c.s[Y][0]), "+v"(c.s[Y][1])); }
        if constexpr (N >= 2 && N < 6) { maxstep<X, 2 * (N - 2)>(c); maxstep<X, 2 * (N - 2) + 1>(c); PIN(c.mx0); PIN(c.mx1); }
        if constexpr (N == 6) { c.l += c.rs0 + c.rs1; c.rs0 = 0.f; c.rs1 = 0.f; PIN(c.l); }
        if constexpr (N == 7) decide<X>(c);
        if constexpr (N >= 8) { exp_sum<Y, N - 8>(c); PIN(c.rs0); PIN(c.rs1); }
    }
    SBAR();
    if constexpr (N == 8 && MODE == 3) PH(6 * X + 2);
    if constexpr ((MODE & 1) && N == 27) {
        if constexpr (MODE == 3) PH(6 * X + 3);
        if constexpr (MODE & 2) {
            if (X == 1 && c.drain) asm volatile("s_waitcnt vmcnt(0)" ::: "memory");
            asm volatile("s_waitcnt vmcnt(8) lgkmcnt(0)\n\ts_barrier" ::: "memory");
        } else asm volatile("s_waitcnt vmcnt(0) lgkmcnt(0)\n\ts_barrier" ::: "memory");
        if constexpr (MODE == 3) PH(6 * X + 4);
        SBAR();
    }
}
template <int X, int MODE, int... Ns> DI void phaseA(AT& c, iseq<Ns...>) { (gapA<X, MODE, Ns>(c), ...); }
template <int X, int MODE, int... Ns> DI void phaseB(AT& c, iseq<Ns...>) { (gapB<X, MODE, Ns>(c), ...); }
template <int X, int B, int... Es> DI void exps_from(AT& c, iseq<Es...>) { (exp_sum<X, B + Es>(c), ...); }
template <int X, int... Ws> DI void packs(AT& c, iseq<Ws...>) { (pack1<X, Ws>(c), ...); }
template <int... Is> DI void maxsteps1(AT& c, iseq<Is...>) { (maxstep<1, Is>(c), ...); }
DI void rescale_o(AT& c) {
    if (__builtin_expect(c.pend, 0)) {
        NOPS16();
        a_scales(c.alpha, make_iseq<128>{});
        NOPS2();
        c.l *= c.alpha; c.pend = false;
    }
}
DI float halfsum(float x) { auto rr = __builtin_amdgcn_permlane32_swap(__float_as_uint(x), __float_as_uint(x), false, false); return __uint_as_float(rr[0]) + __uint_as_float(rr[1]); }
DI void build_mask(AT& c, int mrel) {
    const float NEG = -__builtin_inff(), nm = -c.m;
#pragma unroll
    for (int r = 0; r < 16; ++r) { const int kk = (r & 3) + 8 * (r >> 2); c.cm[0][r] = kk > mrel ? NEG : nm; c.cm[1][r] = kk + 32 > mrel ? NEG : nm; }
    PIN(c.cm[0]); PIN(c.cm[1]); NOPS2();
}

struct ATW {
    const char *Kh, *Vh; ldsp lds; unsigned mk, mv; int n1, vs, lane, wid, r32, hi, qrel; bool isA;
    const char* part; f32x2* mlp; unsigned* flag;
};
DI int tile_of(const ATW& w, int g) { return w.isA ? w.n1 + g : (g < w.n1 ? g : g - w.n1); }
template <int X, bool DMA> DI void setup(AT& c, const ATW& w, int g) {
    c.vcur = w.lds + AT_V + w.vs * 32768 + w.lane * 16;
    if constexpr (DMA) {
        const int v2 = w.vs == 0 ? 2 : w.vs - 1, tn = tile_of(w, g + 2);
        c.gk = w.Kh + (size_t)tn * 32768; c.gv = w.Vh + (size_t)tn * 32768; c.m0k = w.mk + X * 32768; c.m0v = w.mv + v2 * 32768;
    }
}
template <bool LAST> DI void pair(AT& c, ATW& w, int g0) {
    setup<0, true>(c, w, g0);
    const bool seam = !w.isA && (g0 + 1 == w.n1);
    c.first = seam;
    const float m_old = c.m;
    if (seam) { a_qmovs(make_iseq<64>{}); NOPS2(); }
    if constexpr (LAST) build_mask(c, w.qrel);
    SBAR();
    if constexpr (!LAST) PH(0);
    phaseA<0, LAST ? 7 : 3>(c, make_iseq<32>{});
    if constexpr (!LAST) PH(1);
    phaseB<0, LAST ? 11 : 3>(c, make_iseq<32>{});
    if constexpr (!LAST) PH(5);
    rescale_o(c);
    if (seam) {
        NOPS16();
        o_stores((unsigned)(w.wid * 32768 + w.lane * 16), w.part, make_iseq<32>{});
        const float lt = halfsum(c.l);
        if (w.hi == 0) __hip_atomic_store((unsigned long long*)(w.mlp + w.wid * 32 + w.r32), ((unsigned long long)__float_as_uint(lt) << 32) | __float_as_uint(m_old), RLX_AGENT);
        NOPS2();
        a_zeros(make_iseq<128>{});
        NOPS2();
        c.l = 0.f;
    }
    c.drain = seam; c.first = false;
    w.vs = w.vs == 2 ? 0 : w.vs + 1;
    setup<1, !LAST>(c, w, g0 + 1);
    if constexpr (LAST) build_mask(c, w.qrel - 64);
    SBAR();
    if constexpr (!LAST) PH(6);
    phaseA<1, LAST ? 5 : 3>(c, make_iseq<32>{});
    if constexpr (!LAST) PH(7);
    phaseB<1, LAST ? 1 : 3>(c, make_iseq<32>{});
    if constexpr (!LAST) PH(11);
    rescale_o(c);
    if (seam) { if (threadIdx.x == 0) __hip_atomic_store(w.flag, 1u, RLX_AGENT); }
    w.vs = w.vs == 2 ? 0 : w.vs + 1;
}

__global__ __launch_bounds__(256, 1) void k2_attn(unsigned char* ws) {
    extern __shared__ __attribute__((aligned(16))) unsigned char shm[];
    ldsp lds = (ldsp)shm;
    const int tid = threadIdx.x, wid = rfl(tid >> 6), lane = tid & 63, r32 = lane & 31, hi = lane >> 5;
    const int b = blockIdx.x, xcd = b & 7, j = b >> 3, head = 2 * xcd + (j >> 4), w16 = j & 15;
    const bool isA = w16 >= 8; const int i = w16 & 7, bq = 15 - i;
    const int seg = head * 8 + i;
    ATW w;
    w.Kh = (const char*)ws + WS_K + (size_t)head * MB; w.Vh = (const char*)ws + WS_V + (size_t)head * MB;
    const char* Qh = (const char*)ws + WS_Q + (size_t)head * MB;
    w.lds = lds; w.n1 = 15 - 2 * i; w.vs = 0; w.lane = lane; w.wid = wid; w.r32 = r32; w.hi = hi; w.isA = isA; w.qrel = wid * 32 + r32 - 4 * hi;
    w.part = (const char*)ws + WS_PART + (size_t)seg * 131072;
    w.mlp = (f32x2*)(ws + WS_ML) + seg * 128;
    w.flag = (unsigned*)(ws + WS_FLAGS) + seg * FLAG_STRIDE;
    AT c;
    const unsigned lds0 = (unsigned)(uintptr_t)lds;
    c.goff = (unsigned)wid * 8192u + (unsigned)lane * 16u;
    c.kb = lds + AT_K + lane * 16;
    w.mk = lds0 + AT_K + wid * 8192; w.mv = lds0 + AT_V + wid * 8192;
    TS(ws, 1, 0);
    asm volatile("s_nop 4" ::: ACLOB);
    dma_tile(w.mk, c.goff, w.Kh + (size_t)tile_of(w, 0) * 32768);
    q_loads<0>((unsigned)lane * 16u, Qh + (size_t)(bq * 4 + wid) * 16384, make_iseq<16>{});
    dma_tile(w.mk + 32768, c.goff, w.Kh + (size_t)tile_of(w, 1) * 32768);
    dma_tile(w.mv, c.goff, w.Vh + (size_t)tile_of(w, 0) * 32768);
    dma_tile(w.mv + 32768, c.goff, w.Vh + (size_t)tile_of(w, 1) * 32768);
    q_loads<16>((unsigned)lane * 16u, Qh + (size_t)(i * 4 + wid) * 16384, make_iseq<16>{});
    a_zeros(make_iseq<128>{});
    c.cm[0] = f32x16{}; c.cm[1] = f32x16{}; c.m = 0.f; c.l = 0.f; c.rs0 = 0.f; c.rs1 = 0.f; c.alpha = 1.f; c.pend = false; c.drain = false;
    c.aone = hi ? 0.f : 1.f;
    PIN(c.cm[0]);
    asm volatile("s_waitcnt vmcnt(40)\n\ts_barrier" ::: "memory");
    SBAR();
    c.fr[0] = kfrag<0, 0>(c); c.fr[1] = kfrag<0, 1>(c); c.fr[2] = kfrag<0, 2>(c); c.fr[3] = kfrag<0, 3>(c); c.fr[4] = kfrag<0, 4>(c); c.fr[5] = kfrag<0, 5>(c);
    SBAR();
    phaseA<1, 0>(c, make_iseq<32>{});
    SBAR(); asm volatile("s_nop 7\n\ts_nop 7" : "+v"(c.s[0][0]), "+v"(c.s[0][1])); SBAR();
    c.first = true;
    maxsteps1(c, make_iseq<8>{});
    decide<1>(c);
    exps_from<0, 0>(c, make_iseq<NEB>{});
    asm volatile("s_waitcnt vmcnt(0) lgkmcnt(0)\n\ts_barrier" ::: "memory");
    SBAR();
    c.fr[0] = kfrag<1, 0>(c); c.fr[1] = kfrag<1, 1>(c); c.fr[2] = kfrag<1, 2>(c); c.fr[3] = kfrag<1, 3>(c); c.fr[4] = kfrag<1, 4>(c); c.fr[5] = kfrag<1, 5>(c);
    TS(ws, 1, 1);
    for (int g0 = 0; g0 < 14; g0 += 2) pair<false>(c, w, g0);
    TS(ws, 1, 2);
    pair<true>(c, w, 14);
    TS(ws, 1, 3);
    if (wid == 3) CODE_TOUCH2(K3_SYM, lds0 + AT_K, j & 7, lane);
    c.vcur = lds + AT_V + w.vs * 32768 + lane * 16;
    c.fr[0] = vfrag<0>(c.vcur); c.fr[1] = vfrag<1>(c.vcur); c.fr[2] = vfrag<2>(c.vcur); c.fr[3] = vfrag<3>(c.vcur); c.fr[4] = vfrag<4>(c.vcur); c.fr[5] = vfrag<5>(c.vcur);
    exps_from<0, NEB>(c, make_iseq<32 - NEB>{});
    packs<0>(c, make_iseq<16>{});
    c.l += c.rs0 + c.rs1;
    PIN(c.p[0]); PIN(c.p[1]); PIN(c.p[2]); PIN(c.p[3]);
    SBAR(); NOPS2(); SBAR();
    phaseB<0, 0>(c, make_iseq<32>{});
    SBAR(); NOPS16(); SBAR();
    const float lt = halfsum(c.l);
    TS(ws, 1, 4);
    float f1, f2 = 0.f;
    if (isA) {
        if (wid == 0) {
            unsigned spins = 0;
            while (rfl(__hip_atomic_load(w.flag, RLX_AGENT)) == 0u) { __builtin_amdgcn_s_sleep(2); if (++spins > (1u << 22)) break; }
            __builtin_amdgcn_fence(__ATOMIC_ACQUIRE, "agent");
            asm volatile("s_waitcnt vmcnt(0)" ::: "memory");
        }
        __syncthreads();
        const f32x2 ml2 = w.mlp[wid * 32 + r32];
        const float mn = __builtin_fmaxf(c.m, ml2[0]);
        const float w1 = ex2(c.m - mn), w2 = ex2(ml2[0] - mn);
        const float inv = 1.0f / (lt * w1 + ml2[1] * w2);
        f1 = w1 * inv; f2 = w2 * inv;
    } else {
        f1 = 1.0f / lt;
    }
    TS(ws, 1, 5);
    {
        const int row = (isA ? bq : i) * 128 + wid * 32 + r32;
        unsigned char* cp = ws + WS_CTX + ((size_t)(row >> 4) * 128 + head * 8) * 1024 + hi * 256 + (row & 15) * 16;
        const f32x4* pp = (const f32x4*)(w.part + wid * 32768 + lane * 16);
        f32x4 pv[32];
        if (isA) {
#pragma unroll
            for (int q = 0; q < 32; ++q) pv[q] = pp[q * 64];
        } else {
#pragma unroll
            for (int q = 0; q < 32; ++q) pv[q] = (f32x4){0.f, 0.f, 0.f, 0.f};
        }
        auto fin = [&](auto dtag) {
            constexpr int d = decltype(dtag)::value;
            float o[16]; a_reads<16 * d>(o, make_iseq<16>{});
#pragma unroll
            for (int s2 = 0; s2 < 2; ++s2) {
                u32x4 wv;
#pragma unroll
                for (int e = 0; e < 4; ++e) { const int r = 8 * s2 + 2 * e;
                    wv[e] = cvtpk(o[r] * f1 + pv[4 * d + (r >> 2)][r & 3] * f2, o[r + 1] * f1 + pv[4 * d + ((r + 1) >> 2)][(r + 1) & 3] * f2); }
                *(u32x4*)(cp + d * 1024 + s2 * 512) = wv;
            }
        };
        fin(std::integral_constant<int, 0>{}); fin(std::integral_constant<int, 1>{}); fin(std::integral_constant<int, 2>{}); fin(std::integral_constant<int, 3>{});
        fin(std::integral_constant<int, 4>{}); fin(std::integral_constant<int, 5>{}); fin(std::integral_constant<int, 6>{}); fin(std::integral_constant<int, 7>{});
    }
    TS(ws, 1, 7);
}

constexpr int OP_WST = 128 * 224, OP_NS = 5, OP_LDS = OP_NS * OP_WST;

template <int Q> DI void op_finish(const f32x4 (&acc)[14], const __amdgpu_buffer_rsrc_t srs, int loff, const u32x2 (&xw)[4], const u32x2 (&yw)[4], float g,
                                   bf16_t* outp) {
    constexpr int F0 = (Q >> 1) * 7 + (Q & 1) * 4, NF = 4 - (Q & 1);
    f32x4 pt[4][NF];
#pragma unroll
    for (int q = 0; q < 4; ++q)
#pragma unroll
        for (int f = 0; f < NF; ++f)
            if (q != Q) pt[q][f] = __builtin_bit_cast(f32x4, __builtin_amdgcn_raw_buffer_load_b128(srs, loff + q * 57344 + (F0 + f) * 1024, 0, 16));
    const float og = rbf(1.0f - g);
#pragma unroll
    for (int f = 0; f < NF; ++f) {
        pt[Q][f] = acc[F0 + f];
        const f32x4 sum = ((pt[0][f] + pt[1][f]) + pt[2][f]) + pt[3][f];
        const float xv[4] = {bf_lo(xw[f][0]), bf_hi(xw[f][0]), bf_lo(xw[f][1]), bf_hi(xw[f][1])};
        const float yv[4] = {bf_lo(yw[f][0]), bf_hi(yw[f][0]), bf_lo(yw[f][1]), bf_hi(yw[f][1])};
        float r[4];
#pragma unroll
        for (int e = 0; e < 4; ++e) r[e] = rbf(g * rbf(xv[e] * yv[e])) + rbf(og * rbf(sum[e]));
        u32x2 w = {cvtpk(r[0], r[1]), cvtpk(r[2], r[3])};
        *(u32x2*)(outp + f * 16) = w;
    }
}
template <int Q> DI void op_publish(const f32x4 (&acc)[14], const __amdgpu_buffer_rsrc_t srs, int off) {
    constexpr int F0 = (Q >> 1) * 7 + (Q & 1) * 4, NF = 4 - (Q & 1);
#pragma unroll
    for (int f = 0; f < 14; ++f)
        if (f < F0 || f >= F0 + NF) __builtin_amdgcn_raw_buffer_store_b128(__builtin_bit_cast(u32x4, acc[f]), srs, off + f * 1024, 0, 16);
}

__global__ __launch_bounds__(256, 1) void k3_oproj(unsigned char* ws, const bf16_t* x, bf16_t* out) {
    extern __shared__ __attribute__((aligned(16))) unsigned char shm[];
    ldsp lds = (ldsp)shm;
    const int tid = threadIdx.x, wid = rfl(tid >> 6), lane = tid & 63, fr = lane & 15, fq = lane >> 4;
    const int b = blockIdx.x, xcd = b & 7, j = b >> 3, sk = xcd & 3, rb = 8 * (xcd >> 2) + (j >> 2), cn = j & 3;
    const int grp = rb * 4 + cn;
    const char* wbase = (const char*)ws + WS_WI + (size_t)(sk * 4 + cn) * (8 * OP_WST) + wid * 7168;
    const char* cbase = (const char*)ws + WS_CTX + ((size_t)(rb * 8 + 2 * wid) * 128 + sk * 32) * 1024;
    const unsigned cv = (unsigned)lane * 16u;
    bf16x8 cf[8][2][4];
    const unsigned lds0 = (unsigned)(uintptr_t)lds + wid * 7168;
    auto issue = [&](const int s) {
        const char* wg = wbase + s * OP_WST;
        const unsigned lw = lds0 + (s % OP_NS) * OP_WST;
        dma_piece(lw, cv, wg, 0); dma_piece(lw, cv, wg, 1); dma_piece(lw, cv, wg, 2); dma_piece(lw, cv, wg, 3);
        dma_piece(lw + 4096, cv, wg + 4096, 0); dma_piece(lw + 4096, cv, wg + 4096, 1); dma_piece(lw + 4096, cv, wg + 4096, 2);
#pragma unroll
        for (int tb = 0; tb < 2; ++tb) {
            const char* cg = cbase + tb * 131072 + s * 4096;
            asm volatile("global_load_dwordx4 %0, %1, %2" : "=v"(cf[s][tb][0]) : "v"(cv), "s"(cg) : "memory");
            asm volatile("global_load_dwordx4 %0, %1, %2 offset:1024" : "=v"(cf[s][tb][1]) : "v"(cv), "s"(cg) : "memory");
            asm volatile("global_load_dwordx4 %0, %1, %2 offset:2048" : "=v"(cf[s][tb][2]) : "v"(cv), "s"(cg) : "memory");
            asm volatile("global_load_dwordx4 %0, %1, %2 offset:3072" : "=v"(cf[s][tb][3]) : "v"(cv), "s"(cg) : "memory");
        }
        SBAR();
    };
    f32x4 acc[14];
#pragma unroll
    for (int f = 0; f < 14; ++f) acc[f] = (f32x4){0.f, 0.f, 0.f, 0.f};
    const int trow = rb * 128 + wid * 32 + (sk >> 1) * 16 + fr, tcol = cn * 112 + (sk & 1) * 64 + 4 * fq;
    u32x2 xw[4], yw[4]; float gate;
    TS(ws, 2, 0);
    issue(0); issue(1); issue(2); issue(3);
#pragma unroll
    for (int s = 0; s < 8; ++s) {
#define CFT "+v"(cf[s][0][0]), "+v"(cf[s][0][1]), "+v"(cf[s][0][2]), "+v"(cf[s][0][3]), "+v"(cf[s][1][0]), "+v"(cf[s][1][1]), "+v"(cf[s][1][2]), "+v"(cf[s][1][3])
        switch (s) {
        case 0: case 1: case 2: case 3: case 4: asm volatile("s_waitcnt vmcnt(45)\n\ts_barrier" : CFT :: "memory"); break;
        case 5: asm volatile("s_waitcnt vmcnt(51)\n\ts_barrier" : CFT :: "memory"); break;
        case 6: asm volatile("s_waitcnt vmcnt(36)\n\ts_barrier" : CFT :: "memory"); break;
        default: asm volatile("s_waitcnt vmcnt(21)\n\ts_barrier" : CFT :: "memory"); break;
        }
#undef CFT
        SBAR();
        ldscp wp = lds + (s % OP_NS) * OP_WST + lane * 16;
        bf16x8 wf[8];
#pragma unroll
        for (int i = 0; i < 7; ++i) wf[i] = ldsrd16(wp + i * 1024);
        SBAR();
        if (s + 4 < 8) issue(s + 4);
        if (s == 4) {
            const bf16_t* y2p = (const bf16_t*)(ws + WS_Y2);
#pragma unroll
            for (int f = 0; f < 4; ++f) {
                const int c = tcol + ((f == 3 && (sk & 1)) ? 0 : f * 16);
                xw[f] = *(const u32x2*)(x + (size_t)trow * DM + c);
                const bf16_t* yp = y2p + (size_t)c * SEQ + trow;
                yw[f] = (u32x2){yp[0] | (unsigned)yp[SEQ] << 16, yp[2 * SEQ] | (unsigned)yp[3 * SEQ] << 16};
            }
            gate = ((const float*)(ws + WS_G))[trow];
            SBAR();
        }
#pragma unroll
        for (int i = 0; i < 28; ++i) {
            const int kk = i / 7, cb = i % 7;
            if (i + 7 < 28) wf[(i + 7) & 7] = ldsrd16(wp + (i + 7) * 1024);
            acc[cb] = MFMA16(wf[i & 7], cf[s][0][kk], acc[cb]);
            acc[7 + cb] = MFMA16(wf[i & 7], cf[s][1][kk], acc[7 + cb]);
            SBAR();
        }
    }
    TS(ws, 2, 1);
    float* slab = (float*)(ws + WS_OPART) + (size_t)grp * 4 * 14336;
    const __amdgpu_buffer_rsrc_t srs = __builtin_amdgcn_make_buffer_rsrc((void*)slab, (short)0, 4 * 57344, 0x00020000);
    const int soff = ((sk * 4 + wid) * 14 * 64 + lane) * 16;
    if (sk == 0) op_publish<0>(acc, srs, soff); else if (sk == 1) op_publish<1>(acc, srs, soff);
    else if (sk == 2) op_publish<2>(acc, srs, soff); else op_publish<3>(acc, srs, soff);
    asm volatile("s_waitcnt vmcnt(0)\n\ts_barrier" ::: "memory");
    TS(ws, 2, 2);
    unsigned* fl = (unsigned*)(ws + WS_FLAGS) + (N_AFLAGS + grp) * FLAG_STRIDE;
    if (wid == 0) {
        if (lane == 0) __hip_atomic_store(fl + sk, 1u, RLX_AGENT);
        unsigned spins = 0;
        while (true) {
            const unsigned v = __hip_atomic_load(fl + (lane & 3), RLX_AGENT);
            if (__all(v != 0u)) break;
            __builtin_amdgcn_s_sleep(1);
            if (++spins > (1u << 22)) break;
        }
    }
    asm volatile("s_barrier" ::: "memory");
    TS(ws, 2, 3);
    bf16_t* outp = out + (size_t)trow * DM + tcol;
    const int loff = (wid * 14 * 64 + lane) * 16;
    if (sk == 0) op_finish<0>(acc, srs, loff, xw, yw, gate, outp); else if (sk == 1) op_finish<1>(acc, srs, loff, xw, yw, gate, outp);
    else if (sk == 2) op_finish<2>(acc, srs, loff, xw, yw, gate, outp); else op_finish<3>(acc, srs, loff, xw, yw, gate, outp);
    TS(ws, 2, 7);
}

extern "C" __device__ __attribute__((used, noinline)) void text_end_mark() { asm volatile("s_nop 0"); }

extern "C" void kernel_launch(void* const* d_in, const int* in_sizes, int n_in, void* d_out, int out_size, void* d_ws, size_t ws_size,
                              hipStream_t stream) {
    (void)in_sizes; (void)n_in; (void)out_size; (void)ws_size;
    (void)hipFuncSetAttribute((const void*)k1_proj, hipFuncAttributeMaxDynamicSharedMemorySize, K1_LDS);
    (void)hipFuncSetAttribute((const void*)k2_attn, hipFuncAttributeMaxDynamicSharedMemorySize, AT_LDS);
    (void)hipFuncSetAttribute((const void*)k3_oproj, hipFuncAttributeMaxDynamicSharedMemorySize, OP_LDS);
    K1Args P;
    P.x = (const bf16_t*)d_in[0]; P.ak = (const bf16_t*)d_in[1]; P.av = (const bf16_t*)d_in[2]; P.shift_k = (const bf16_t*)d_in[3];
    P.ssm_k = (const bf16_t*)d_in[4]; P.ssm_d = (const bf16_t*)d_in[5]; P.ckv = (const bf16_t*)d_in[7]; P.wq = (const bf16_t*)d_in[8];
    P.wuk = (const bf16_t*)d_in[9]; P.wuv = (const bf16_t*)d_in[10]; P.gate_w = (const bf16_t*)d_in[12]; P.wo = (const bf16_t*)d_in[11]; P.ws = (unsigned char*)d_ws;
#define DEV_STAGES 7
    if (DEV_STAGES & 1) k1_proj<<<256, 512, K1_LDS, stream>>>(P);
    if (DEV_STAGES & 2) k2_attn<<<256, 256, AT_LDS, stream>>>((unsigned char*)d_ws);
    if (DEV_STAGES & 4) k3_oproj<<<256, 256, OP_LDS, stream>>>((unsigned char*)d_ws, (const bf16_t*)d_in[0], (bf16_t*)d_out);
}
```

```cpp
#include <hip/hip_runtime.h>
#include <hip/hip_bf16.h>
#include <stdint.h>
#include <utility>

#define LAS __attribute__((address_space(3)))
#define GAS __attribute__((address_space(1)))
#define CAS __attribute__((address_space(4)))
#define DI __device__ __forceinline__
typedef unsigned short bf16_t;
typedef short bf16x8 __attribute__((ext_vector_type(8)));
typedef short s16x4 __attribute__((ext_vector_type(4)));
typedef float f32x2 __attribute__((ext_vector_type(2)));
typedef float f32x4 __attribute__((ext_vector_type(4)));
typedef float f32x16 __attribute__((ext_vector_type(16)));
typedef unsigned u32x2 __attribute__((ext_vector_type(2)));
typedef unsigned u32x4 __attribute__((ext_vector_type(4)));
typedef __bf16 bfx2 __attribute__((ext_vector_type(2)));
typedef LAS unsigned char* ldsp;
typedef const LAS unsigned char* ldscp;

constexpr int SEQ = 2048, DM = 448, NH = 16, HD = 256, DC = 1024, NW_ = 4096;

constexpr size_t MB = 1024 * 1024;
constexpr size_t WS_Q = 0, WS_K = 16 * MB, WS_V = 32 * MB, WS_CTX = 48 * MB, WS_PART = 64 * MB, WS_ML = 80 * MB,
                 WS_Y2 = 81 * MB, WS_G = 84 * MB, WS_OPART = 85 * MB, WS_FLAGS = 100 * MB, WS_WI = 102 * MB;
constexpr int FLAG_STRIDE = 64;
constexpr int N_AFLAGS = 128, N_OFLAGS = 64, N_CFLAGS = 64;

#define TS(wsp, kern, slot) do { } while (0)
#define TSI(wsp, idx) do { } while (0)
#define SBAR() __builtin_amdgcn_sched_barrier(0)
#define PIN(x) asm volatile("" : "+v"(x))
#define RLX_AGENT __ATOMIC_RELAXED, __HIP_MEMORY_SCOPE_AGENT

DI unsigned cvtpk(float lo, float hi) { f32x2 v = {lo, hi}; bfx2 b = __builtin_convertvector(v, bfx2); return __builtin_bit_cast(unsigned, b); }
DI float bf_lo(unsigned w) { return __uint_as_float(w << 16); }
DI float bf_hi(unsigned w) { return __uint_as_float(w & 0xffff0000u); }
DI float rbf(float x) { return bf_lo(cvtpk(x, 0.f)); }
DI unsigned rfl(unsigned x) { return __builtin_amdgcn_readfirstlane(x); }
DI s16x4 trread(ldscp p) { return __builtin_amdgcn_ds_read_tr16_b64_v4i16((LAS s16x4*)p); }
DI bf16x8 cat8(s16x4 lo, s16x4 hi) { return __builtin_shufflevector(lo, hi, 0, 1, 2, 3, 4, 5, 6, 7); }
DI bf16x8 ldsrd16(ldscp p) { return *(const LAS bf16x8*)p; }
#define MFMA16(a, b, c) __builtin_amdgcn_mfma_f32_16x16x32_bf16((a), (b), (c), 0, 0, 0)
#define MFMA32(a, b, c) __builtin_amdgcn_mfma_f32_32x32x16_bf16((a), (b), (c), 0, 0, 0)

DI void touch(unsigned sink, unsigned voff, const char* g) {
    asm volatile("s_mov_b32 m0, %0\n\ts_nop 0\n\tglobal_load_lds_dword %1, %2" :: "s"(sink), "v"(voff), "s"(g) : "memory");
}
#define CODE_TOUCH(SYM, END, sink, chunk, lane) do { const char* _p; unsigned _n; \
        asm volatile("s_getpc_b64 s[90:91]\n\ts_add_u32 s90, s90, " SYM "@rel32@lo+4\n\ts_addc_u32 s91, s91, " SYM "@rel32@hi+12\n\ts_mov_b64 %0, s[90:91]\n\ts_mov_b32 %1, " END "-" SYM \
                     : "=s"(_p), "=s"(_n) :: "s90", "s91", "scc"); \
        const unsigned _o = ((unsigned)(chunk) * 64u + (unsigned)(lane)) * 128u; \
        if ((unsigned)(chunk) * 8192u < _n && _n < 262144u) touch((sink), _o < _n - 4u ? _o : _n - 4u, _p); } while (0)
#define K1_SYM "_Z7k1_proj6K1Args", "_Z7k2_attnPhPKtPt"
#define K2_SYM "_Z7k2_attnPhPKtPt", "text_end_mark"
#define CODE_TOUCH2(S, sink, chunk, lane) CODE_TOUCH(S, sink, chunk, lane)

constexpr int HTB = 128 * 64 * 2;
constexpr int K1_X = 8 * HTB;
constexpr int K1_LDS = K1_X + 16384 + 2048;

DI int lds_byte(int r, int c) { const int st = (r >> 4) * 2 + (c >> 5), rr = r & 15, cc = c & 31, ob = rr * 64 + cc * 2; return st * 1024 + (ob ^ (((ob >> 9) & 1) << 5)); }
DI void stage_rc(int b, int& R, int& C) { const int st = b / 1024, sb = b % 1024, swz = sb ^ (((sb >> 9) & 1) << 5); R = (st >> 1) * 16 + swz / 64; C = (st & 1) * 32 + (swz % 64) / 2; }

struct K1Args {
    const bf16_t *x, *ak, *av, *shift_k, *ssm_k, *ssm_d, *ckv, *wq, *wuk, *wuv, *gate_w, *wo;
    unsigned char* ws;
};

#define SA(b, h) (((b) * 2 + (h)) * HTB)
#define SB(b, h) ((4 + (b) * 2 + (h)) * HTB)
constexpr int ring_slot(int u) {
    switch (u & 7) { case 5: return SB(0, 0); case 6: return SA(0, 0); case 7: return SB(0, 1); case 0: return SA(0, 1);
                     case 1: return SB(1, 0); case 2: return SA(1, 0); case 3: return SB(1, 1); default: return SA(1, 1); }
}
struct G1 {
    ldsp lds; unsigned ldsw  , voffA_kv, voffA_q, voffB, sv; int aoff, boff0, boff1;
    const char *A_kv, *A_q, *B_kv, *B_q, *sbase; unsigned sstep, sdst, sdstep; int sadd;
};
DI int lane_id() { int l; asm volatile("v_mbcnt_lo_u32_b32 %0, -1, 0\n\tv_mbcnt_hi_u32_b32 %0, -1, %0" : "=v"(l)); return l; }
template <int OFF> DI void dma16(unsigned lbase, unsigned voff, const char* g) {
    asm volatile("s_add_i32 m0, %0, %c3\n\ts_nop 0\n\tglobal_load_lds_dwordx4 %1, %2" :: "s"(lbase), "v"(voff), "s"(g), "i"(OFF) : "memory", "scc");
}
template <int BUF, int STRIDE, int H> DI void stage_a(const G1& g, const char* base, unsigned voff) {
    dma16<BUF>(g.ldsw, voff, base + H * 128 * STRIDE); dma16<BUF + 8192>(g.ldsw, voff, base + (H * 128 + 64) * STRIDE);
}
template <int BUF, int H> DI void stage_b(const G1& g, const char* base) {
    dma16<BUF>(g.ldsw, g.voffB, base + H * 256); dma16<BUF + 8192>(g.ldsw, g.voffB, base + H * 256 + 32 * NW_ * 2);
}
template <int U> DI void stage_unit(const G1& g) {
    if constexpr (U < 21) {
        constexpr int t = U / 3, r = U % 3;
        if constexpr (r == 0) stage_b<ring_slot(U), 0>(g, g.B_q + (size_t)t * (64 * NW_ * 2));
        else stage_a<ring_slot(U), DM * 2, r - 1>(g, g.A_q + t * 128, g.voffA_q);
    } else {
        constexpr int t = (U - 21) >> 2, r = (U - 21) & 3;
        if constexpr ((r & 1) == 0) stage_b<ring_slot(U), (r >> 1)>(g, g.B_kv + (size_t)t * (64 * NW_ * 2));
        else stage_a<ring_slot(U), DC * 2, (r >> 1)>(g, g.A_kv + t * 128, g.voffA_kv);
    }
}
DI void side(const G1& g, int t) {
    const int tt = t + g.sadd > 15 ? 15 : t + g.sadd;
    asm volatile("s_mov_b32 m0, %0\n\ts_nop 0\n\tglobal_load_lds_dword %1, %2" :: "s"(g.sdst + (unsigned)t * g.sdstep), "v"(g.sv), "s"(g.sbase + rfl((unsigned)tt * g.sstep)) : "memory");
}
DI void lda(bf16x8 (&dst)[4][2], const G1& g, int buf) {
#pragma unroll
    for (int m = 0; m < 4; ++m)
#pragma unroll
        for (int k = 0; k < 2; ++k) dst[m][k] = ldsrd16(g.lds + buf + g.aoff + m * 2048 + k * 1024);
}
DI void ldb(bf16x8 (&dst)[2][2], const G1& g, int buf) {
#pragma unroll
    for (int n = 0; n < 2; ++n)
#pragma unroll
        for (int k = 0; k < 2; ++k) { ldscp p = g.lds + buf + (n ? g.boff1 : g.boff0) + k * 8192; dst[n][k] = cat8(trread(p), trread(p + 256)); }
}
template <bool VT> DI void mma(f32x4 (&acc)[4][2], const bf16x8 (&At)[4][2], const bf16x8 (&Bt)[2][2]) {
    __builtin_amdgcn_s_setprio(1);
#pragma unroll
    for (int m = 0; m < 4; ++m)
#pragma unroll
        for (int n = 0; n < 2; ++n)
#pragma unroll
            for (int k = 0; k < 2; ++k) acc[m][n] = VT ? MFMA16(At[m][k], Bt[n][k], acc[m][n]) : MFMA16(Bt[n][k], At[m][k], acc[m][n]);
    __builtin_amdgcn_s_setprio(0);
}
#define WAIT_V(n) asm volatile("s_waitcnt vmcnt(" #n ")" ::: "memory")
#define WAIT_L(n) asm volatile("s_waitcnt lgkmcnt(" #n ")" ::: "memory")
#define BAR __builtin_amdgcn_s_barrier()
DI void zero8(f32x4 (&a)[4][2]) {
#pragma unroll
    for (int m = 0; m < 4; ++m) { a[m][0] = (f32x4){0.f, 0.f, 0.f, 0.f}; a[m][1] = (f32x4){0.f, 0.f, 0.f, 0.f}; }
}

DI void pack_kq(const f32x4 (&acc)[4][2], u32x4* pk, float scale) {
#pragma unroll
    for (int m = 0; m < 4; ++m) {
        const f32x4 a0 = acc[m][0] * scale, a1 = acc[m][1] * scale;
        const unsigned w0 = cvtpk(a0[0], a0[1]), w1 = cvtpk(a0[2], a0[3]), x0 = cvtpk(a1[0], a1[1]), x1 = cvtpk(a1[2], a1[3]);
        auto r0 = __builtin_amdgcn_permlane16_swap(w0, x0, false, false);
        auto r1 = __builtin_amdgcn_permlane16_swap(w1, x1, false, false);
        pk[m] = (u32x4){r0[0], r1[0], r0[1], r1[1]};
    }
}
DI unsigned kq_off(int pm, int wr, int wc, int fr, int fq) {
    return (unsigned)((pm * 8 + wr * 2) * 16384 + fr * 16 + (2 * wc + (fq & 1)) * 1024 + (fq >> 1) * 512);
}
DI void pack_v(const f32x4 (&acc)[4][2], u32x4* pk) {
#pragma unroll
    for (int n = 0; n < 2; ++n)
#pragma unroll
        for (int mm = 0; mm < 2; ++mm) {
            const f32x4 a0 = acc[2 * mm][n], a1 = acc[2 * mm + 1][n];
            const unsigned w0 = cvtpk(a0[0], a0[1]), w1 = cvtpk(a0[2], a0[3]), x0 = cvtpk(a1[0], a1[1]), x1 = cvtpk(a1[2], a1[3]);
            auto r0 = __builtin_amdgcn_permlane32_swap(w0, x0, false, false);
            auto r1 = __builtin_amdgcn_permlane32_swap(w1, x1, false, false);
            pk[2 * mm + n] = (u32x4){r0[0], r1[0], r0[1], r1[1]};
        }
}
#define ST_AUX 16
struct Out1 { u32x4 pk[16]; __amdgpu_buffer_rsrc_t rs; unsigned voff; int sbj, shi; };
template <int I> DI void st_piece(const Out1& o) {
    __builtin_amdgcn_raw_buffer_store_b128(o.pk[I], o.rs, o.voff + (I & 1) * 256, (I >> 3) * 65536 + ((I >> 2) & 1) * o.sbj + ((I >> 1) & 1) * o.shi, ST_AUX);
}
template <int A, int B> DI void st_range(const Out1& o) { if constexpr (A < B) { st_piece<A>(o); st_range<A + 1, B>(o); } }

template <int N, class W> DI void qphase(const G1& g, f32x4 (&a0)[4][2], f32x4 (&a1)[4][2], bf16x8 (&At)[4][2], bf16x8 (&B0)[2][2], W& warm) {
    constexpr int t = N >> 1;
    if constexpr ((N & 1) == 0) {
        ldb(B0, g, ring_slot(3 * t)); SBAR(); lda(At, g, ring_slot(3 * t + 1));
        if constexpr (t < 5) warm(t);
        if constexpr (N >= 2) stage_unit<3 * t + 6>(g);
        if constexpr (N == 0) WAIT_V(10); else WAIT_V(8);
        WAIT_L(8); BAR; WAIT_L(0); mma<false>(a0, At, B0); BAR; SBAR();
    } else {
        lda(At, g, ring_slot(3 * t + 2));
        if constexpr (t >= 1) stage_unit<3 * t + 7>(g);
        stage_unit<3 * t + 8>(g);
        WAIT_V(8);
        if constexpr (N == 13) WAIT_L(0);
        BAR; WAIT_L(0); mma<false>(a1, At, B0); BAR; SBAR();
    }
}
template <class W, int... Ns> DI void qphases(const G1& g, f32x4 (&a0)[4][2], f32x4 (&a1)[4][2], bf16x8 (&At)[4][2], bf16x8 (&B0)[2][2], W& warm, std::integer_sequence<int, Ns...>) {
    (qphase<Ns>(g, a0, a1, At, B0, warm), ...);
}
template <int... Us> DI void stage_units(const G1& g, std::integer_sequence<int, Us...>) { (stage_unit<Us>(g), ...); }

template <bool VT, int POS, class W>
DI void mainpair(const G1& g, int t, f32x4 (&acc)[2][2][4][2], bf16x8 (&At)[4][2], bf16x8 (&B0)[2][2], bf16x8 (&B1)[2][2], W& lastside) {
    const char* a1 = g.A_kv + (t + 1) * 128; const char* a2 = a1 + 128; const char* a3 = a1 + 256;
    const char* b2 = g.B_kv + (size_t)(t + 2) * (64 * NW_ * 2); const char* b3 = b2 + 64 * NW_ * 2;
    ldb(B0, g, SB(0, 0)); SBAR(); lda(At, g, SA(0, 0));
    if constexpr (POS != 0) stage_a<SA(1, 1), DC * 2, 1>(g, a1, g.voffA_kv); else WAIT_V(18);
    WAIT_L(8); BAR; WAIT_L(0); mma<VT>(acc[0][0], At, B0); BAR; SBAR();
    ldb(B1, g, SB(0, 1));
    if constexpr (POS == 2) lastside(0); else side(g, t);
    if constexpr (POS != 2) stage_b<SB(0, 0), 0>(g, b2);
    if constexpr (POS == 0) WAIT_V(19);
    BAR; WAIT_L(0); mma<VT>(acc[0][1], At, B1); BAR;
    lda(At, g, SA(0, 1));
    if constexpr (POS != 2) stage_a<SA(0, 0), DC * 2, 0>(g, a2, g.voffA_kv);
    BAR; WAIT_L(0); mma<VT>(acc[1][0], At, B0); BAR; SBAR();
    if constexpr (POS != 2) stage_b<SB(0, 1), 1>(g, b2);
    if constexpr (POS == 0) WAIT_V(15); else if constexpr (POS == 1) WAIT_V(7); else WAIT_V(1);
    BAR; mma<VT>(acc[1][1], At, B1); BAR;
    ldb(B0, g, SB(1, 0)); SBAR(); lda(At, g, SA(1, 0));
    if constexpr (POS != 2) stage_a<SA(0, 1), DC * 2, 1>(g, a2, g.voffA_kv);
    WAIT_L(8); BAR; WAIT_L(0); mma<VT>(acc[0][0], At, B0); BAR; SBAR();
    ldb(B1, g, SB(1, 1));
    if constexpr (POS == 2) lastside(1); else side(g, t + 1);
    if constexpr (POS != 2) stage_b<SB(1, 0), 0>(g, b3);
    BAR; WAIT_L(0); mma<VT>(acc[0][1], At, B1); BAR;
    lda(At, g, SA(1, 1));
    if constexpr (POS != 2) stage_a<SA(1, 0), DC * 2, 0>(g, a3, g.voffA_kv);
    BAR; WAIT_L(0); mma<VT>(acc[1][0], At, B0); BAR; SBAR();
    if constexpr (POS != 2) { stage_b<SB(1, 1), 1>(g, b3); WAIT_V(7); }
    BAR; mma<VT>(acc[1][1], At, B1); BAR;
}

constexpr int CV_RAW = SA(0, 0);
constexpr int K1_WO = SA(0, 1);
constexpr int CV_HSTR = 4256;
constexpr int CV_H = 2 * HTB;
constexpr int CV_USTR = 4608;
constexpr int CV_U = CV_H + 8 * CV_HSTR;
constexpr int CV_R = CV_U + 2 * CV_USTR;
static_assert(CV_R + 65536 <= K1_LDS && CV_R <= K1_X, "lds");

DI void tail_stage(const G1& g, const K1Args& P, int c0, int e, int wid, int lane) {
    dma16<CV_RAW>(g.ldsw, (unsigned)lane * 16u, (const char*)P.ssm_k + (size_t)c0 * (SEQ * 2) + wid * 1024);
    if (wid < 7) {
        const char* wg = (const char*)P.wo + ((size_t)((e >> 6) * 1024 + (e & 15) * 64) * DM + ((e >> 4) & 3) * 112) * 2;
        { const int ci = wid * 64 + lane, k = ci / 14, cc = ci - k * 14; dma16<K1_WO>(g.ldsw, (unsigned)(k * DM + cc * 8) * 2u, wg); }
        { const int ci = (wid + 7) * 64 + lane, k = ci / 14, cc = ci - k * 14; dma16<K1_WO + 7168>(g.ldsw, (unsigned)(k * DM + cc * 8) * 2u, wg); }
    }
}
DI void wo_image(ldsp lds, const K1Args& P, int e, int wid, int lane) {
    const int fr = lane & 15, fq = lane >> 4;
    if (wid < 7) {
        ldscp wp = lds + K1_WO + (16 * (fq >> 1) + 4 * (fq & 1) + (fr >> 2)) * 224 + (fr & 3) * 8 + wid * 32;
        unsigned char* op = P.ws + WS_WI + (size_t)e * 14336 + wid * 1024 + lane * 16;
#pragma unroll
        for (int kk = 0; kk < 2; ++kk) *(bf16x8*)(op + kk * 7168) = cat8(trread(wp + kk * 7168), trread(wp + kk * 7168 + 1792));
    }
}
DI void conv_copies(ldsp q, const u32x4 d, const u32x2 e) {
    const unsigned a0 = __builtin_amdgcn_alignbit(d[1], d[0], 16), a1 = __builtin_amdgcn_alignbit(d[2], d[1], 16), a2 = __builtin_amdgcn_alignbit(d[3], d[2], 16),
                   a3 = __builtin_amdgcn_alignbit(e[0], d[3], 16), a4 = __builtin_amdgcn_alignbit(e[1], e[0], 16);
    *(LAS u32x4*)(q) = (u32x4){a0, a1, a2, a3};
    *(LAS u32x4*)(q + CV_HSTR) = (u32x4){d[1], d[2], d[3], e[0]};
    *(LAS u32x4*)(q + 2 * CV_HSTR) = (u32x4){a1, a2, a3, a4};
    *(LAS u32x4*)(q + 3 * CV_HSTR) = (u32x4){d[2], d[3], e[0], e[1]};
}
DI void conv_frags(bf16x8 (&A)[8], bf16x8 (&B)[8], ldscp hp, ldscp up) {
#pragma unroll
    for (int v = 0; v < 8; ++v) {
        B[v] = ldsrd16(up + (7 - v) * 512);
        const s16x4 lo = *(const LAS s16x4*)(hp + v * 512), hi = *(const LAS s16x4*)(hp + v * 512 + 8);
        A[v] = cat8(lo, hi);
    }
}
DI void conv_mma(f32x4 (&acc)[8], const bf16x8 (&A)[8], const bf16x8 (&B)[8]) {
#pragma unroll
    for (int J = 0; J < 8; ++J)
#pragma unroll
        for (int v = 0; v <= J; ++v) acc[J] = MFMA16(A[v], B[J - v], acc[J]);
}
DI void conv_part(ldsp lds, const K1Args& P, int c0, const u32x4 sw, const unsigned dw, int wid, int lane, const Out1& o) {
    const int tid = wid * 64 + lane, fr = lane & 15, fq = lane >> 4;
    {
        const int cq = tid & 255;
        const u32x4 d = *(const LAS u32x4*)(lds + CV_RAW + tid * 16);
        u32x2 e = *(const LAS u32x2*)(lds + CV_RAW + tid * 16 + 16);
        const u32x4 z4 = {0u, 0u, 0u, 0u}; const u32x2 z2 = {0u, 0u};
        if (cq == 255) e = z2;
        ldsp q = lds + CV_H + (tid >> 8) * 4 * CV_HSTR + (cq + 2) * 16;
        conv_copies(q, d, e);
        if (cq == 0) { conv_copies(q - 16, z4, (u32x2){d[0], d[1]}); conv_copies(q - 32, z4, z2); }
        if (cq == 255) { conv_copies(q + 16, z4, z2); conv_copies(q + 32, z4, z2); }
    }
    SBAR(); st_range<4, 6>(o); SBAR();
    {
        ldscp kp = lds + K1_X + tid * 16;
        u32x4 a = *(const LAS u32x4*)(kp - 16);
        const u32x4 b = *(const LAS u32x4*)(kp), vr = *(const LAS u32x4*)(kp + 8192);
        if (tid == 0) a = (u32x4){0u, 0u, 0u, 0u};
        const unsigned kr[7] = {a[1], a[2], a[3], b[0], b[1], b[2], b[3]};
#pragma unroll
        for (int c = 0; c < 2; ++c) {
            const float w0 = bf_lo(sw[2 * c]), w1 = bf_hi(sw[2 * c]), w2 = bf_lo(sw[2 * c + 1]), w3 = bf_hi(sw[2 * c + 1]);
            float kc[7], kv[4];
#pragma unroll
            for (int i = 0; i < 7; ++i) kc[i] = c ? bf_hi(kr[i]) : bf_lo(kr[i]);
#pragma unroll
            for (int i = 0; i < 4; ++i) {
                float s = rbf(kc[i] * w3);
                s = rbf(s + rbf(kc[i + 1] * w2));
                s = rbf(s + rbf(kc[i + 2] * w1));
                s = rbf(s + rbf(kc[i + 3] * w0));
                kv[i] = s * (c ? bf_hi(vr[i]) : bf_lo(vr[i]));
            }
            u32x2 wv = {cvtpk(kv[3], kv[2]), cvtpk(kv[1], kv[0])};
            *(LAS u32x2*)(lds + CV_U + c * CV_USTR + (2044 - 4 * tid) * 2) = wv;
            SBAR(); if (c == 0) st_range<6, 8>(o); else st_range<8, 10>(o); SBAR();
        }
        if (tid < 120) { const int c = tid / 60, j = tid - c * 60; *(LAS u32x2*)(lds + CV_U + c * CV_USTR + 4096 + j * 8) = (u32x2){0u, 0u}; }
    }
    __syncthreads();
    TS(P.ws, 0, 5);
    const int ch = wid >> 2, qw = wid & 3;
    {
        const int y1 = 24 + fr - 8 * fq;
        ldscp hb = lds + CV_H + ch * 4 * CV_HSTR + (y1 & 3) * CV_HSTR + (y1 & ~3) * 2 + qw * 128;
        ldscp ub = lds + CV_U + ch * CV_USTR + (2056 - 256 * 7 - 16 * fr - 8 * fq) * 2 + qw * 128;
        f32x4 acc[8];
#pragma unroll
        for (int i = 0; i < 8; ++i) acc[i] = (f32x4){0.f, 0.f, 0.f, 0.f};
        bf16x8 A0[8], B0[8], A1[8], B1[8];
        conv_frags(A0, B0, hb, ub); SBAR();
        conv_frags(A1, B1, hb + 64, ub + 64); SBAR();
        st_range<10, 12>(o); SBAR();
        conv_mma(acc, A0, B0); SBAR();
        st_range<12, 14>(o); SBAR();
        conv_mma(acc, A1, B1); SBAR();
        st_range<14, 16>(o); SBAR();
#pragma unroll
        for (int i = 0; i < 8; ++i) *(LAS f32x4*)(lds + CV_R + wid * 8192 + i * 1024 + lane * 16) = acc[i];
    }
    __syncthreads();
    TS(P.ws, 0, 6);
    {
        const float dd = ch ? bf_hi(dw) : bf_lo(dw);
#pragma unroll
        for (int i = 0; i < 2; ++i) {
            ldscp rp = lds + CV_R + ch * 32768 + (2 * qw + i) * 1024 + lane * 16;
            const f32x4 y = ((*(const LAS f32x4*)rp + *(const LAS f32x4*)(rp + 8192)) + *(const LAS f32x4*)(rp + 16384)) + *(const LAS f32x4*)(rp + 24576);
            const int t = 256 * (2 * qw + i) + 16 * fr + 4 * fq;
            const u32x2 uw = *(const LAS u32x2*)(lds + CV_U + ch * CV_USTR + (2044 - t) * 2);
            const float kv[4] = {bf_hi(uw[1]), bf_lo(uw[1]), bf_hi(uw[0]), bf_lo(uw[0])};
            float r[4];
#pragma unroll
            for (int e = 0; e < 4; ++e) r[e] = rbf(y[e]) + rbf(kv[e] * dd);
            u32x2 w = {cvtpk(r[0], r[1]), cvtpk(r[2], r[3])};
            *(u32x2*)(P.ws + WS_Y2 + ((size_t)(c0 + ch) * SEQ + t) * 2) = w;
        }
    }
}

DI void gate_part(const K1Args& P, int row0, int wid, int lane) {
    const int lc = lane < 56 ? lane : 0;
    u32x4 gw = *(const u32x4*)(P.gate_w + lc * 8);
    float* gp = (float*)(P.ws + WS_G);
    u32x4 xv[8];
#pragma unroll
    for (int r = 0; r < 8; ++r) xv[r] = *(const u32x4*)(P.x + (size_t)(row0 + wid * 8 + r) * DM + lc * 8);
    float mine = 0.f;
#pragma unroll
    for (int r = 0; r < 8; ++r) {
        float s = 0.f;
#pragma unroll
        for (int j = 0; j < 4; ++j) { s += rbf(bf_lo(xv[r][j]) * bf_lo(gw[j])); s += rbf(bf_hi(xv[r][j]) * bf_hi(gw[j])); }
        if (lane >= 56) s = 0.f;
#pragma unroll
        for (int o = 32; o >= 1; o >>= 1) s += __shfl_xor(s, o);
        if (lane == r) mine = s;
    }
    const float z = rbf(mine);
    const float g = rbf(1.0f / (1.0f + __expf(-z)));
    if (lane < 8) gp[row0 + wid * 8 + lane] = g;
}

template <bool VT>
DI void big_tile(const G1& g, const K1Args& P, int pm, int head, int c0, int e, int wid, bf16x8 (&At)[4][2], bf16x8 (&B0)[2][2], Out1& o) {
    f32x4 acc[2][2][4][2];
    zero8(acc[0][0]); zero8(acc[0][1]); zero8(acc[1][0]); zero8(acc[1][1]);
    bf16x8 B1[2][2];
    auto lastside = [&](int s) {
        if (wid < 4) side(g, 14 + s);
        else if (s == 0) {
            const unsigned l = (unsigned)lane_id();
            if (wid == 4) touch(g.sdst, l * 128u, (const char*)P.ssm_k + (size_t)c0 * (SEQ * 2));
            else touch(g.sdst, l * (DM * 2), (const char*)P.wo + ((size_t)((e >> 6) * 1024 + (e & 15) * 64) * DM + ((e >> 4) & 3) * 112) * 2 + (wid - 5) * 110);
        }
    };
    mainpair<VT, 0>(g, 0, acc, At, B0, B1, lastside);
    for (int t = 2; t < 14; t += 2) mainpair<VT, 1>(g, t, acc, At, B0, B1, lastside);
    mainpair<VT, 2>(g, 14, acc, At, B0, B1, lastside);
    TS(P.ws, 0, 3);
    const int lane = lane_id();
    tail_stage(g, P, c0, e, wid, lane);
    if (wid == 7) CODE_TOUCH2(K2_SYM, g.sdst, e & 7, lane);
    SBAR();
    const int wr = wid >> 2, wc = wid & 3, fr = lane & 15, fq = lane >> 4;
    if (VT) {
        o.voff = (unsigned)((pm * 4 + wr) * 32768 + wc * 4096 + (fq >> 1) * 1024 + (fq & 1) * 512 + fr * 16);
        pack_v(acc[0][0], o.pk); pack_v(acc[0][1], o.pk + 4); pack_v(acc[1][0], o.pk + 8); pack_v(acc[1][1], o.pk + 12);
    } else {
        o.voff = kq_off(pm, wr, wc, fr, fq);
        pack_kq(acc[0][0], o.pk, 1.0f); pack_kq(acc[0][1], o.pk + 4, 1.0f); pack_kq(acc[1][0], o.pk + 8, 1.0f); pack_kq(acc[1][1], o.pk + 12, 1.0f);
    }
}

__global__ __launch_bounds__(512, 2) void k1_proj(K1Args P) {
    extern __shared__ __attribute__((aligned(16))) unsigned char shm[];
    ldsp lds = (ldsp)shm;
    const int b = blockIdx.x, xcd = b & 7, j = b >> 3, li = j & 15, pm = li & 7, head = 2 * xcd + (li >> 3), vt = j >> 4;
    const int tid = threadIdx.x, wid = rfl(tid >> 6), lane = tid & 63, wr = wid >> 2, wc = wid & 3, fr = lane & 15, fq = lane >> 4;
    const int c0 = (xcd * 28 + (j < 28 ? j : 0)) * 2, e = xcd * 32 + j;
    TS(P.ws, 0, 0);
    if (wid == 7) CODE_TOUCH2(K1_SYM, (unsigned)(uintptr_t)lds + K1_X + 16384 + 7 * 256, j, lane);
    u32x4 sw = *(const CAS u32x4*)(uintptr_t)(P.shift_k + c0 * 4); unsigned dw = *(const CAS unsigned*)(uintptr_t)(P.ssm_d + c0);
    G1 g;
    const unsigned lds0 = (unsigned)(uintptr_t)lds;
    g.lds = lds; g.ldsw = lds0 + (unsigned)wid * 1024u;
    { int R, C; stage_rc(tid * 16, R, C); g.voffA_kv = (unsigned)(R * DC + C) * 2u; g.voffA_q = (unsigned)(R * DM + C) * 2u; }
    { const int kg = wid >> 1, row = (lane >> 2) & 7, ch = (lane & 3) ^ (2 * (kg & 1)), n = 32 * (2 * (wid & 1) + (lane >> 5)) + 8 * ch;
      g.voffB = (unsigned)((8 * kg + row) * NW_ + n) * 2u; }
    g.aoff = lds_byte(wr * 64 + fr, fq * 8);
    g.boff0 = fq * 2048 + wc * 512 + (fr >> 2) * 64 + (32 * (fq & 1)) + 8 * (fr & 3);
    g.boff1 = g.boff0 ^ 32;
    g.A_kv = (const char*)P.ckv + (size_t)pm * 256 * DC * 2;
    g.A_q = (const char*)P.x + (size_t)pm * 256 * DM * 2;
    g.B_kv = (const char*)(vt ? P.wuv : P.wuk) + head * 512;
    g.B_q = (const char*)P.wq + head * 512 + vt * 256;
    if (wid < 4) {
        g.sbase = (const char*)(wid < 2 ? P.ak : P.av) + (wid & 1) * (64 * DM * 2) + c0 * 2; g.sstep = 128 * DM * 2; g.sv = (unsigned)lane * (DM * 2);
        g.sadd = 0; g.sdst = lds0 + K1_X + (wid >> 1) * 8192 + (wid & 1) * 256; g.sdstep = 512;
    } else {
        if (wid == 4) { g.sbase = g.A_kv + (size_t)((head & 1) * 2 + vt) * 64 * (DC * 2); g.sstep = 128; g.sv = (unsigned)lane * (DC * 2); }
        else if (wid == 5) { g.sbase = g.B_kv + (size_t)pm * 8 * (NW_ * 2); g.sstep = 64 * NW_ * 2; g.sv = (unsigned)((lane >> 2) & 7) * (NW_ * 2) + (lane & 3) * 128; }
        else { g.sbase = (const char*)(wid == 6 ? P.ak : P.av) + (size_t)j * 4 * (DM * 2) + xcd * 112; g.sstep = 128 * DM * 2; g.sv = (unsigned)(lane & 3) * (DM * 2) + ((lane >> 2) & 1) * 108; }
        g.sadd = 4; g.sdst = lds0 + K1_X + 16384 + wid * 256; g.sdstep = 0;
    }
    PIN(g.voffA_kv); PIN(g.voffB); PIN(g.sv); PIN(g.aoff); PIN(g.boff0); PIN(g.boff1);
    stage_units(g, std::make_integer_sequence<int, 8>{});
    WAIT_V(12);
    auto warm = [&](int slot) {
        const unsigned sink = lds0 + K1_X + 16384 + wid * 256;
        if (slot > 0) { if (wid >= 4) side(g, slot - 5); }
        else if (wid < 3) touch(sink, (unsigned)lane * (DM * 2), g.A_q + (size_t)((head & 1) * 2 + vt) * 64 * (DM * 2) + (4 + wid) * 128);
        else if (wid == 3) touch(sink, (unsigned)((lane < 48 ? lane : 0) >> 1) * (NW_ * 2) + (lane & 1) * 128, g.B_q + (size_t)(256 + pm * 24) * (NW_ * 2));
    };
    asm volatile("" : "+s"(sw), "+s"(dw));
    if (b == 0 && tid < N_AFLAGS + N_OFLAGS + N_CFLAGS) *(u32x4*)((unsigned*)(P.ws + WS_FLAGS) + tid * FLAG_STRIDE) = (u32x4){0u, 0u, 0u, 0u};
    bf16x8 At[4][2], B0[2][2];
    BAR;
    if (wr == 1) BAR;
    TS(P.ws, 0, 1);
    {
        f32x4 q0[4][2], q1[4][2];
        zero8(q0); zero8(q1);
        qphases(g, q0, q1, At, B0, warm, std::make_integer_sequence<int, 14>{});
        stage_unit<27>(g); stage_unit<28>(g); SBAR();
        Out1 o;
        o.rs = __builtin_amdgcn_make_buffer_rsrc(P.ws + WS_Q + (size_t)head * MB, (short)0, (int)MB, 0x00020000);
        o.voff = kq_off(pm, wr, wc, fr, fq) + vt * 8192; o.sbj = 65536; o.shi = 16384;
        pack_kq(q0, o.pk, 0.0625f * 1.4426950408889634f); pack_kq(q1, o.pk + 4, 0.0625f * 1.4426950408889634f);
        st_range<0, 8>(o);
        SBAR();
    }
    TS(P.ws, 0, 2);
    Out1 o;
    if (vt) big_tile<true>(g, P, pm, head, c0, e, wid, At, B0, o); else big_tile<false>(g, P, pm, head, c0, e, wid, At, B0, o);
    o.rs = __builtin_amdgcn_make_buffer_rsrc(P.ws + (vt ? WS_V : WS_K) + (size_t)head * MB, (short)0, (int)MB, 0x00020000);
    o.sbj = vt ? 16384 : 8192; o.shi = vt ? 2048 : 16384;
    SBAR(); st_range<0, 4>(o); SBAR();
    WAIT_V(4);
    if (wr == 0) BAR;
    BAR;
    TS(P.ws, 0, 4);
    const int lane2 = lane_id();
    wo_image(lds, P, e, wid, lane2);
    if (j < 28) conv_part(lds, P, c0, sw, dw, wid, lane2, o);
    else { st_range<4, 16>(o); gate_part(P, (xcd * 4 + (j - 28)) * 64, wid, lane2); }
    TS(P.ws, 0, 7);
}
#undef SA
#undef SB

constexpr int AT_K = 0, AT_V = 65536, AT_LDS = 163840;
constexpr float THR = 8.0f;
constexpr int PD = 6;
constexpr int NEB = 24;
template <int... Is> using iseq = std::integer_sequence<int, Is...>;
template <int N> using make_iseq = std::make_integer_sequence<int, N>;
#define ACLOB "a0","a1","a2","a3","a4","a5","a6","a7","a8","a9","a10","a11","a12","a13","a14","a15","a16","a17","a18","a19","a20","a21","a22","a23","a24","a25","a26","a27","a28","a29","a30","a31","a32","a33","a34","a35","a36","a37","a38","a39","a40","a41","a42","a43","a44","a45","a46","a47","a48","a49","a50","a51","a52","a53","a54","a55","a56","a57","a58","a59","a60","a61","a62","a63","a64","a65","a66","a67","a68","a69","a70","a71","a72","a73","a74","a75","a76","a77","a78","a79","a80","a81","a82","a83","a84","a85","a86","a87","a88","a89","a90","a91","a92","a93","a94","a95","a96","a97","a98","a99","a100","a101","a102","a103","a104","a105","a106","a107","a108","a109","a110","a111","a112","a113","a114","a115","a116","a117","a118","a119","a120","a121","a122","a123","a124","a125","a126","a127","a128","a129","a130","a131","a132","a133","a134","a135","a136","a137","a138","a139","a140","a141","a142","a143","a144","a145","a146","a147","a148","a149","a150","a151","a152","a153","a154","a155","a156","a157","a158","a159","a160","a161","a162","a163","a164","a165","a166","a167","a168","a169","a170","a171","a172","a173","a174","a175","a176","a177","a178","a179","a180","a181","a182","a183","a184","a185","a186","a187","a188","a189","a190","a191","a192","a193","a194","a195","a196","a197","a198","a199","a200","a201","a202","a203","a204","a205","a206","a207","a208","a209","a210","a211","a212","a213","a214","a215","a216","a217","a218","a219","a220","a221","a222","a223","a224","a225","a226","a227","a228","a229","a230","a231","a232","a233","a234","a235","a236","a237","a238","a239","a240","a241","a242","a243","a244","a245","a246","a247","a248","a249","a250","a251","a252","a253","a254","a255"

#define PH(i) do { } while (0)
constexpr int OP_WST = 128 * 224;
constexpr int op_slot(int s) { switch (s % 5) { case 0: return 0; case 1: return OP_WST; case 2: return 2 * OP_WST; case 3: return 131072; default: return 3 * OP_WST; } }
static_assert(3 * OP_WST <= AT_V + 32768 && 131072 + OP_WST <= AT_LDS && AT_V + 65536 == 131072, "lds");
struct OPJ { const char *wbase, *cbase; unsigned cv, lds0; ldscp wp; unsigned char* ws; };
struct AT {
    OPJ o;
    u32x2 pk;
    f32x2 ml2;
    const char *part, *mlp;
    bool isA, early;
    f32x16 s[2][2];
    f32x16 cm[2];
    u32x4 p[4];
    bf16x8 fr[8];
    float m, l, rs0, rs1, mx0, mx1, alpha, aone;
    bool pend, first, drain;
    ldscp kb;
    ldscp vcur;
    unsigned goff;
    unsigned m0k, m0v;
    const char *gk, *gv;
};

DI void dma_piece(unsigned m0v, unsigned goff, const char* g, const int off) {
    switch (off) {
    case 0: asm volatile("s_mov_b32 m0, %0\n\ts_nop 0\n\tglobal_load_lds_dwordx4 %1, %2" :: "s"(m0v), "v"(goff), "s"(g) : "memory"); break;
    case 1: asm volatile("s_mov_b32 m0, %0\n\ts_nop 0\n\tglobal_load_lds_dwordx4 %1, %2 offset:1024" :: "s"(m0v), "v"(goff), "s"(g) : "memory"); break;
    case 2: asm volatile("s_mov_b32 m0, %0\n\ts_nop 0\n\tglobal_load_lds_dwordx4 %1, %2 offset:2048" :: "s"(m0v), "v"(goff), "s"(g) : "memory"); break;
    default: asm volatile("s_mov_b32 m0, %0\n\ts_nop 0\n\tglobal_load_lds_dwordx4 %1, %2 offset:3072" :: "s"(m0v), "v"(goff), "s"(g) : "memory"); break;
    }
}
template <int I> DI void dma8(unsigned m0base, unsigned goff, const char* g) { dma_piece(m0base + (I >> 2) * 4096, goff, g + (I >> 2) * 4096, I & 3); }
DI void dma_tile(unsigned m0base, unsigned goff, const char* g) {
    dma8<0>(m0base, goff, g); dma8<1>(m0base, goff, g); dma8<2>(m0base, goff, g); dma8<3>(m0base, goff, g);
    dma8<4>(m0base, goff, g); dma8<5>(m0base, goff, g); dma8<6>(m0base, goff, g); dma8<7>(m0base, goff, g);
}
DI float ex2(float x) { return __builtin_amdgcn_exp2f(x); }
DI float MX3(float a, float b, float c) { float r; asm("v_max3_f32 %0, %1, %2, %3" : "=v"(r) : "v"(a), "v"(b), "v"(c)); return r; }
DI float MX2(float a, float b) { float r; asm("v_max_f32_e32 %0, %1, %2" : "=v"(r) : "v"(a), "v"(b)); return r; }
template <int D0> DI void qk_init(f32x16& s, const bf16x8& k, const f32x16& c) {
    asm volatile("v_mfma_f32_32x32x16_bf16 %0, %1, a[%c3:%c4], %2" : "=&v"(s) : "v"(k), "v"(c), "i"(128 + 4 * D0), "i"(131 + 4 * D0)); }
template <int D0> DI void qk_acc(f32x16& s, const bf16x8& k) {
    asm volatile("v_mfma_f32_32x32x16_bf16 %0, %1, a[%c2:%c3], %0" : "+v"(s) : "v"(k), "i"(128 + 4 * D0), "i"(131 + 4 * D0)); }
template <int DB> DI void pv_acc(const bf16x8& v, const u32x4& p) {
    asm volatile("v_mfma_f32_32x32x16_bf16 a[%c2:%c3], %0, %1, a[%c2:%c3]" :: "v"(v), "v"(p), "i"(16 * DB), "i"(16 * DB + 15)); }
DI void add_bcast(f32x16& x, float aone, float b) { asm volatile("s_nop 1\n\tv_mfma_f32_32x32x2_f32 %0, %1, %2, %0" : "+v"(x) : "v"(aone), "v"(b)); }
#define NOPS16() asm volatile("s_nop 7\n\ts_nop 7" ::: "memory")
#define NOPS2() asm volatile("s_nop 1" ::: "memory")
template <int I> DI void a_zero() { asm volatile("v_accvgpr_write_b32 a[%c0], 0" :: "i"(I)); }
template <int I> DI void a_scale(float f) { float t; asm volatile("v_accvgpr_read_b32 %0, a[%c2]\n\tv_mul_f32 %0, %1, %0\n\tv_accvgpr_write_b32 a[%c2], %0" : "=&v"(t) : "v"(f), "i"(I)); }
template <int I> DI void a_qmov() { asm volatile("v_accvgpr_mov_b32 a[%c0], a[%c1]" :: "i"(128 + I), "i"(192 + I)); }
template <int I> DI float a_read() { float t; asm volatile("v_accvgpr_read_b32 %0, a[%c1]" : "=v"(t) : "i"(I)); return t; }
template <int F> DI void q_load(unsigned voff, const char* g) {
    asm volatile("global_load_dwordx4 a[%c2:%c3], %0, %1 offset:%c4" :: "v"(voff), "s"(g + ((F & 15) >> 2) * 4096), "i"(128 + 4 * F), "i"(131 + 4 * F), "i"((F & 3) * 1024) : "memory"); }
template <int K> DI void pk_store(const AT& c, const u32x4 pk) {
    asm volatile("global_store_dwordx4 %0, %1, %2 offset:%c3 sc1\n\ts_nop 1" :: "v"(c.o.cv), "v"(pk), "s"(c.part + (K >> 2) * 4096), "i"((K & 3) * 1024) : "memory"); }
template <int K> DI void p_load(const AT& c) {
    asm volatile("global_load_dwordx4 a[%c2:%c3], %0, %1 offset:%c4 sc1" :: "v"(c.o.cv), "s"(c.part + (K >> 2) * 4096), "i"(192 + 4 * K), "i"(195 + 4 * K), "i"((K & 3) * 1024) : "memory"); }
DI void ml_load(AT& c) { asm volatile("global_load_dwordx2 %0, %1, %2 sc1" : "+v"(c.ml2) : "v"((c.o.cv & 496u) >> 1), "s"(c.mlp) : "memory"); }
template <int I> DI void op_wpiece(const OPJ& o) {
    constexpr int S = I / 7, P = I % 7, H = P >= 4 ? 4096 : 0;
    dma_piece(o.lds0 + op_slot(S) + H, o.cv, o.wbase + S * OP_WST + H, P & 3);
}
template <int DB> DI void pv_new(const bf16x8& v, const u32x4& p) {
    asm volatile("v_mfma_f32_32x32x16_bf16 a[%c2:%c3], %0, %1, 0" :: "v"(v), "v"(p), "i"(16 * DB), "i"(16 * DB + 15)); }
template <int... Is> DI void a_zeros(iseq<Is...>) { (a_zero<Is>(), ...); }
template <int... Is> DI void a_scales(float f, iseq<Is...>) { (a_scale<Is>(f), ...); }
template <int... Is> DI void a_qmovs(iseq<Is...>) { (a_qmov<Is>(), ...); }
template <int B, int... Is> DI void q_loads(unsigned voff, const char* g, iseq<Is...>) { (q_load<B + Is>(voff, g), ...); }
template <int... Is> DI void p_loads(const AT& c, iseq<Is...>) { (p_load<Is>(c), ...); }
template <int B, int... Is> DI void a_reads(float (&o)[16], iseq<Is...>) { ((o[Is] = a_read<B + Is>()), ...); }

template <int KS, int R> DI bf16x8 kfrag(const AT& c) { return ldsrd16(c.kb + KS * 32768 + ((R & 1) * 16 + (R >> 1)) * 1024); }
template <int R, bool DBM = false> DI bf16x8 vfrag(ldscp v) {
    return ldsrd16(v + (DBM ? (R >> 2) * 4096 + (R & 3) * 1024 : (R & 7) * 4096 + (R >> 3) * 1024)); }
template <int Q> DI void pk_quad(AT& c) {
    const float t0 = a_read<4 * Q>(), t1 = a_read<4 * Q + 1>(), t2 = a_read<4 * Q + 2>(), t3 = a_read<4 * Q + 3>();
    if constexpr (Q & 1) pk_store<(Q >> 1)>(c, (u32x4){c.pk[0], c.pk[1], cvtpk(t0, t1), cvtpk(t2, t3)});
    else { c.pk = (u32x2){cvtpk(t0, t1), cvtpk(t2, t3)}; PIN(c.pk); }
}
template <int X, int E> DI void exp_sum(AT& c) {
    const float e = ex2(c.s[X][E >> 4][E & 15]); c.s[X][E >> 4][E & 15] = e;
    if constexpr (E & 1) c.rs1 += e; else c.rs0 += e;
}
template <int X, int W> DI void pack1(AT& c) {
    c.p[W >> 2][W & 3] = cvtpk(c.s[X][W >> 3][(2 * W) & 15], c.s[X][W >> 3][(2 * W + 1) & 15]);
}
template <int X, int MODE, int N> DI void gapA(AT& c) {
    constexpr int d0 = N >> 1, kb = N & 1, Y = X ^ 1;
    if constexpr (d0 == 0) qk_init<0>(c.s[Y][kb], c.fr[N & 7], c.cm[(MODE & 4) ? kb : 0]);
    else qk_acc<d0>(c.s[Y][kb], c.fr[N & 7]);
    constexpr int R = N + PD;
    if constexpr (R < 32) c.fr[R & 7] = kfrag<Y, R>(c); else if constexpr (MODE & 1) c.fr[R & 7] = vfrag<R - 32, (MODE & 18) == 18>(c.vcur);
    if constexpr (MODE & 1) {
        if constexpr (N < 32 - NEB) { exp_sum<X, NEB + N>(c); PIN(c.rs0); PIN(c.rs1); }
        if constexpr (N >= 10 && N < 26) { pack1<X, N - 10>(c); PIN(c.p[(N - 10) >> 2]); }
    }
    if constexpr ((MODE & 4) && N == 2) asm volatile("" :: "v"(c.cm[0]), "v"(c.cm[1]));
    if constexpr ((MODE & 18) == 16) pk_quad<N>(c);
    if constexpr ((MODE & 18) == 18 && N >= 24) pk_quad<N - 24>(c);
    if constexpr ((MODE & 2) && (N & 1)) {
        if constexpr (N < 16) dma8<(N >> 1)>(c.m0k, c.goff, c.gk); else dma8<((N - 16) >> 1)>(c.m0v, c.goff, c.gv);
    }
    SBAR();
}
template <int X> DI void decide(AT& c) {
    constexpr int Y = X ^ 1;
    float mm = MX2(c.mx0, c.mx1);
    auto rr = __builtin_amdgcn_permlane32_swap(__float_as_uint(mm), __float_as_uint(mm), false, false);
    const float rm = MX2(__uint_as_float(rr[0]), __uint_as_float(rr[1]));
    if (__builtin_expect(c.first || __any(rm > THR), 0)) {
        const float dl = c.first ? rm : MX2(rm, 0.f);
        c.m += dl; c.alpha = ex2(-dl); c.pend = !c.first;
        const float nd = -dl;
        add_bcast(c.s[Y][0], c.aone, nd); add_bcast(c.s[Y][1], c.aone, nd); add_bcast(c.cm[0], c.aone, nd);
        asm volatile("s_nop 7\n\ts_nop 7\n\ts_nop 3" : "+v"(c.s[Y][0]), "+v"(c.s[Y][1]), "+v"(c.cm[0]));
    }
}
template <int X, int I> DI void maxstep(AT& c) {
    constexpr int Y = X ^ 1, h = I >> 2, r = 4 * (I & 3);
    if constexpr (I == 0) { c.mx0 = MX2(c.s[Y][0][0], c.s[Y][0][1]); c.mx1 = MX2(c.s[Y][0][2], c.s[Y][0][3]); }
    else { c.mx0 = MX3(c.mx0, c.s[Y][h][r], c.s[Y][h][r + 1]); c.mx1 = MX3(c.mx1, c.s[Y][h][r + 2], c.s[Y][h][r + 3]); }
}
template <int X, int MODE, int N> DI void gapB(AT& c) {
    constexpr bool DBM = (MODE & 18) == 18;
    constexpr int ks = DBM ? N & 3 : N >> 3, db = DBM ? N >> 2 : N & 7, Y = X ^ 1;
    if constexpr ((MODE & 16) && ks == 0) pv_new<db>(c.fr[N & 7], c.p[ks]); else pv_acc<db>(c.fr[N & 7], c.p[ks]);
    if constexpr (DBM && N < 24) pk_quad<N + 8>(c);
    if constexpr ((MODE & 32) && N == 10) {
        if (c.isA) {
            asm volatile("s_waitcnt vmcnt(0)" ::: "memory");
            c.early = rfl(__float_as_uint(a_read<192>())) != 0u;
            __builtin_amdgcn_fence(__ATOMIC_ACQUIRE, "wavefront");
            if (c.early) ml_load(c);
        }
    }
    if constexpr ((MODE & 32) && N > 10 && N < 27) { if (c.early) p_load<N - 11>(c); }
    if constexpr ((MODE & 64) && N < 28) op_wpiece<N>(c.o);
    constexpr int R = N + PD;
    if constexpr (R < 32) c.fr[R & 7] = vfrag<R, DBM>(c.vcur);
    if constexpr (MODE & 1) {
        if constexpr (MODE & 2) {
            if constexpr (N == 28) { c.fr[0] = kfrag<X, 0>(c); c.fr[1] = kfrag<X, 1>(c); }
            if constexpr (N == 29) { c.fr[2] = kfrag<X, 2>(c); c.fr[3] = kfrag<X, 3>(c); }
            if constexpr (N == 30) { c.fr[4] = kfrag<X, 4>(c); }
            if constexpr (N == 31) { c.fr[5] = kfrag<X, 5>(c); }
        }
        if constexpr (N == 1) { asm volatile("s_nop 7" : "+v"(c.s[Y][0]), "+v"(c.s[Y][1])); }
        if constexpr (N >= 2 && N < 6) { maxstep<X, 2 * (N - 2)>(c); maxstep<X, 2 * (N - 2) + 1>(c); PIN(c.mx0); PIN(c.mx1); }
        if constexpr (N == 6) { c.l += c.rs0 + c.rs1; c.rs0 = 0.f; c.rs1 = 0.f; PIN(c.l); }
        if constexpr (N == 7) decide<X>(c);
        if constexpr (N >= 8) { exp_sum<Y, N - 8>(c); PIN(c.rs0); PIN(c.rs1); }
    }
    SBAR();
    if constexpr (N == 8 && MODE == 3) PH(6 * X + 2);
    if constexpr ((MODE & 1) && N == 27) {
        if constexpr (MODE == 3) PH(6 * X + 3);
        if constexpr (MODE & 2) {
            if (X == 0 && c.drain) asm volatile("s_waitcnt vmcnt(0)" ::: "memory");
            if constexpr (MODE & 16) asm volatile("s_waitcnt vmcnt(24) lgkmcnt(0)\n\ts_barrier" ::: "memory");
            else asm volatile("s_waitcnt vmcnt(8) lgkmcnt(0)\n\ts_barrier" ::: "memory");
        } else {
            if (!c.isA) asm volatile("s_waitcnt vmcnt(0)" ::: "memory");
            asm volatile("s_waitcnt lgkmcnt(0)\n\ts_barrier" ::: "memory");
        }
        if constexpr (MODE == 3) PH(6 * X + 4);
        SBAR();
    }
}
template <int X, int MODE, int... Ns> DI void phaseA(AT& c, iseq<Ns...>) { (gapA<X, MODE, Ns>(c), ...); }
template <int X, int MODE, int... Ns> DI void phaseB(AT& c, iseq<Ns...>) { (gapB<X, MODE, Ns>(c), ...); }
template <int X, int B, int... Es> DI void exps_from(AT& c, iseq<Es...>) { (exp_sum<X, B + Es>(c), ...); }
template <int X, int... Ws> DI void packs(AT& c, iseq<Ws...>) { (pack1<X, Ws>(c), ...); }
template <int... Is> DI void maxsteps1(AT& c, iseq<Is...>) { (maxstep<1, Is>(c), ...); }
DI void rescale_o(AT& c) {
    if (__builtin_expect(c.pend, 0)) {
        NOPS16();
        a_scales(c.alpha, make_iseq<128>{});
        NOPS2();
        c.l *= c.alpha; c.pend = false;
    }
}
DI float halfsum(float x) { auto rr = __builtin_amdgcn_permlane32_swap(__float_as_uint(x), __float_as_uint(x), false, false); return __uint_as_float(rr[0]) + __uint_as_float(rr[1]); }
DI void build_mask(AT& c, int mrel) {
    const float NEG = -__builtin_inff(), nm = -c.m;
#pragma unroll
    for (int r = 0; r < 16; ++r) { const int kk = (r & 3) + 8 * (r >> 2); c.cm[0][r] = kk > mrel ? NEG : nm; c.cm[1][r] = kk + 32 > mrel ? NEG : nm; }
    PIN(c.cm[0]); PIN(c.cm[1]); NOPS2();
}

struct ATW {
    const char *Kh, *Vh; ldsp lds; unsigned mk, mv; int n1, vs, lane, wid, r32, hi, qrel; bool isA;
    unsigned* flag;
};
DI int tile_of(const ATW& w, int g) { return w.isA ? w.n1 + g : (g < w.n1 ? g : g - w.n1); }
template <int X, bool DMA> DI void setup(AT& c, const ATW& w, int g) {
    c.vcur = w.lds + AT_V + w.vs * 32768 + w.lane * 16;
    if constexpr (DMA) {
        const int v2 = w.vs == 0 ? 2 : w.vs - 1, tn = tile_of(w, g + 2);
        c.gk = w.Kh + (size_t)tn * 32768; c.gv = w.Vh + (size_t)tn * 32768; c.m0k = w.mk + X * 32768; c.m0v = w.mv + v2 * 32768;
    }
}
template <bool LAST, bool seam> DI void pair(AT& c, ATW& w, int g0) {
    setup<0, true>(c, w, g0);
    c.first = seam;
    const float m_old = c.m;
    if constexpr (seam) { a_qmovs(make_iseq<64>{}); NOPS2(); }
    int qr = w.qrel;
    if constexpr (LAST) {
        if constexpr (seam) asm volatile("; last pair, two segments" : "+v"(qr), "+v"(c.s[0][0]), "+v"(c.s[0][1]), "+v"(c.m));
        else asm volatile("; last pair" : "+v"(qr), "+v"(c.s[0][0]), "+v"(c.s[0][1]), "+v"(c.m));
        build_mask(c, qr);
    }
    SBAR();
    if constexpr (!LAST && !seam) PH(0);
    phaseA<0, LAST ? 7 : 3>(c, make_iseq<32>{});
    if constexpr (!LAST && !seam) PH(1);
    phaseB<0, LAST || seam ? 11 : 3>(c, make_iseq<32>{});
    if constexpr (!LAST && !seam) PH(5);
    rescale_o(c);
    if (c.drain) { if (w.wid == 0) __hip_atomic_store(w.flag, 1u, RLX_AGENT); c.drain = false; }
    c.first = false;
    w.vs = w.vs == 2 ? 0 : w.vs + 1;
    setup<1, !LAST>(c, w, g0 + 1);
    if constexpr (LAST || seam) asm volatile("" : "+s"(c.part));
    if constexpr (LAST) {
        asm volatile("global_load_dword a192, %0, %1 sc1" :: "v"(0u), "s"(w.flag) : "memory");
        PIN(qr); build_mask(c, qr - 64);
    }
    if constexpr (seam) {
        const float lt = halfsum(c.l);
        __hip_atomic_store((unsigned long long*)c.mlp + w.r32, ((unsigned long long)__float_as_uint(lt) << 32) | __float_as_uint(m_old), RLX_AGENT);
        c.l = 0.f;
        asm volatile("; second segment" : "+v"(c.s[1][0]), "+v"(c.s[1][1]), "+v"(c.rs0), "+v"(c.rs1), "+v"(c.l));
        SBAR();
        phaseA<1, (LAST ? 5 : 3) | 16>(c, make_iseq<32>{});
        phaseB<1, (LAST ? 33 : 11) | 16>(c, make_iseq<32>{});
        if constexpr (LAST) { if (w.wid == 0) __hip_atomic_store(w.flag, 1u, RLX_AGENT); } else c.drain = true;
    } else {
        SBAR();
        if constexpr (!LAST) PH(6);
        phaseA<1, LAST ? 5 : 3>(c, make_iseq<32>{});
        if constexpr (!LAST) PH(7);
        phaseB<1, LAST ? 33 : 3>(c, make_iseq<32>{});
        if constexpr (!LAST) PH(11);
    }
    rescale_o(c);
    w.vs = w.vs == 2 ? 0 : w.vs + 1;
}

template <int S> DI void op_wstage(const OPJ& o) {
    op_wpiece<7 * S>(o); op_wpiece<7 * S + 1>(o); op_wpiece<7 * S + 2>(o); op_wpiece<7 * S + 3>(o); op_wpiece<7 * S + 4>(o); op_wpiece<7 * S + 5>(o); op_wpiece<7 * S + 6>(o);
}
template <int F> DI void op_cload(const OPJ& o) {
    asm volatile("global_load_dwordx4 a[%c2:%c3], %0, %1 offset:%c4 sc1" :: "v"(o.cv), "s"(o.cbase + ((F >> 1) & 3) * 131072 + (F >> 3) * 4096),
                 "i"(4 * F), "i"(4 * F + 3), "i"((F & 1) * 2048) : "memory"); }
template <int B, int... Is> DI void op_cloads(const OPJ& o, iseq<Is...>) { (op_cload<B + Is>(o), ...); }
template <int F> DI void op_mfma(f32x4& acc, const bf16x8& wf) {
    asm volatile("v_mfma_f32_16x16x32_bf16 %0, %1, a[%c2:%c3], %0" : "+v"(acc) : "v"(wf), "i"(4 * F), "i"(4 * F + 3)); }
constexpr int OP_PD = 4;
template <int I> DI bf16x8 op_wfrag(const OPJ& o) { return ldsrd16(o.wp + op_slot(I / 14) + ((I % 14) / 7) * 14336 + (I % 7) * 1024); }
template <int I> DI void op_step(const OPJ& o, f32x4 (&acc)[28], bf16x8 (&wf)[8]) {
    constexpr int S = I / 14, jj = (I % 14) / 7, cb = I % 7;
    if constexpr (I % 14 == 0) {
        if constexpr (S < 2) asm volatile("s_waitcnt vmcnt(48)\n\ts_barrier" ::: "memory");
        else if constexpr (S == 2) asm volatile("s_waitcnt vmcnt(47)\n\ts_barrier" ::: "memory");
        else if constexpr (S == 3) asm volatile("s_waitcnt vmcnt(46)\n\ts_barrier" ::: "memory");
        else if constexpr (S == 4) asm volatile("s_waitcnt vmcnt(14)\n\ts_barrier" ::: "memory");
        else if constexpr (S == 5) asm volatile("s_waitcnt vmcnt(7)\n\ts_barrier" ::: "memory");
        else if constexpr (S == 6) asm volatile("s_waitcnt vmcnt(0)\n\ts_barrier" ::: "memory");
        SBAR();
    }
    op_mfma<S * 8 + jj>(acc[cb], wf[I & 7]);
    if constexpr (I + OP_PD < 112) wf[(I + OP_PD) & 7] = op_wfrag<I + OP_PD>(o);
    op_mfma<S * 8 + 2 + jj>(acc[7 + cb], wf[I & 7]);
    if constexpr (S == 0 && I < 8) op_cload<56 + I>(o);
    if constexpr (S >= 1 && S <= 3 && I % 14 < 7) op_wpiece<7 * (S + 4) + I % 14>(o);
    op_mfma<S * 8 + 4 + jj>(acc[14 + cb], wf[I & 7]);
    op_mfma<S * 8 + 6 + jj>(acc[21 + cb], wf[I & 7]);
    SBAR();
}
template <int... Is> DI void op_steps(const OPJ& o, f32x4 (&acc)[28], bf16x8 (&wf)[8], iseq<Is...>) { (op_step<Is>(o, acc, wf), ...); }

template <int Q> DI void op_finish(const f32x4 (&acc)[14], const __amdgpu_buffer_rsrc_t srs, int loff, const u32x2 (&xw)[4], const u32x2 (&yw)[4], float g,
                                   bf16_t* outp) {
    constexpr int F0 = (Q >> 1) * 7 + (Q & 1) * 4, NF = 4 - (Q & 1);
    f32x4 pt[4][NF];
#pragma unroll
    for (int q = 0; q < 4; ++q)
#pragma unroll
        for (int f = 0; f < NF; ++f)
            if (q != Q) pt[q][f] = __builtin_bit_cast(f32x4, __builtin_amdgcn_raw_buffer_load_b128(srs, loff + q * 57344 + (F0 + f) * 1024, 0, 16));
    const float og = rbf(1.0f - g);
#pragma unroll
    for (int f = 0; f < NF; ++f) {
        pt[Q][f] = acc[F0 + f];
        const f32x4 sum = ((pt[0][f] + pt[1][f]) + pt[2][f]) + pt[3][f];
        const float xv[4] = {bf_lo(xw[f][0]), bf_hi(xw[f][0]), bf_lo(xw[f][1]), bf_hi(xw[f][1])};
        const float yv[4] = {bf_lo(yw[f][0]), bf_hi(yw[f][0]), bf_lo(yw[f][1]), bf_hi(yw[f][1])};
        float r[4];
#pragma unroll
        for (int e = 0; e < 4; ++e) r[e] = rbf(g * rbf(xv[e] * yv[e])) + rbf(og * rbf(sum[e]));
        u32x2 w = {cvtpk(r[0], r[1]), cvtpk(r[2], r[3])};
        *(u32x2*)(outp + f * 16) = w;
    }
}
template <int Q> DI void op_publish(const f32x4 (&acc)[14], const __amdgpu_buffer_rsrc_t srs, int off) {
    constexpr int F0 = (Q >> 1) * 7 + (Q & 1) * 4, NF = 4 - (Q & 1);
#pragma unroll
    for (int f = 0; f < 14; ++f)
        if (f < F0 || f >= F0 + NF) __builtin_amdgcn_raw_buffer_store_b128(__builtin_bit_cast(u32x4, acc[f]), srs, off + f * 1024, 0, 16);
}
template <bool MERGE, int D> DI void ctx_out(float f1, float f2, const __amdgpu_buffer_rsrc_t crs, int voff) {
    float o[16], p[16];
    a_reads<16 * D>(o, make_iseq<16>{});
    if constexpr (MERGE) a_reads<192 + 8 * D>(p, make_iseq<8>{});
#pragma unroll
    for (int s2 = 0; s2 < 2; ++s2) {
        u32x4 wv;
#pragma unroll
        for (int e = 0; e < 4; ++e) { const int r = 8 * s2 + 2 * e;
            if constexpr (MERGE) { const unsigned pw = __float_as_uint(p[r >> 1]); wv[e] = cvtpk(o[r] * f1 + bf_lo(pw) * f2, o[r + 1] * f1 + bf_hi(pw) * f2); }
            else wv[e] = cvtpk(o[r] * f1, o[r + 1] * f1); }
        __builtin_amdgcn_raw_buffer_store_b128(wv, crs, voff + D * 1024 + s2 * 512, 0, 16);
    }
}
template <bool MERGE, int... Ds> DI void ctx_outs(float f1, float f2, const __amdgpu_buffer_rsrc_t crs, int voff, iseq<Ds...>) { (ctx_out<MERGE, Ds>(f1, f2, crs, voff), ...); }
DI void poll4(const unsigned* fl, int lane) {
    unsigned spins = 0;
    while (true) {
        const unsigned v = __hip_atomic_load(fl + (lane & 3), RLX_AGENT);
        if (__all(v != 0u)) break;
        __builtin_amdgcn_s_sleep(1);
        if (++spins > (1u << 22)) break;
    }
    __builtin_amdgcn_fence(__ATOMIC_ACQUIRE, "wavefront");
}

__global__ __launch_bounds__(256, 1) void k2_attn(unsigned char* ws, const bf16_t* x, bf16_t* out) {
    extern __shared__ __attribute__((aligned(16))) unsigned char shm[];
    ldsp lds = (ldsp)shm;
    const int tid = threadIdx.x, wid = rfl(tid >> 6), lane = tid & 63, r32 = lane & 31, hi = lane >> 5;
    const int b = blockIdx.x, xcd = b & 7, j = b >> 3, head = 2 * xcd + (j >> 4), w16 = j & 15;
    const bool isA = w16 >= 8; const int i = w16 & 7, bq = 15 - i;
    const int seg = head * 8 + i;
    ATW w;
    w.Kh = (const char*)ws + WS_K + (size_t)head * MB; w.Vh = (const char*)ws + WS_V + (size_t)head * MB;
    const char* Qh = (const char*)ws + WS_Q + (size_t)head * MB;
    w.lds = lds; w.n1 = 15 - 2 * i; w.vs = 0; w.lane = lane; w.wid = wid; w.r32 = r32; w.hi = hi; w.isA = isA; w.qrel = wid * 32 + r32 - 4 * hi;
    w.flag = (unsigned*)(ws + WS_FLAGS) + seg * FLAG_STRIDE;
    AT c;
    const unsigned lds0 = (unsigned)(uintptr_t)lds;
    c.part = (const char*)ws + WS_PART + (size_t)(seg * 4 + wid) * 16384; c.mlp = (const char*)ws + WS_ML + (seg * 4 + wid) * 256;
    c.isA = isA; c.early = false; c.ml2 = (f32x2){0.f, 0.f};
    OPJ& o = c.o;
    o.cv = (unsigned)lane * 16u;
    c.goff = (unsigned)wid * 8192u + (unsigned)lane * 16u;
    c.kb = lds + AT_K + lane * 16;
    w.mk = lds0 + AT_K + wid * 8192; w.mv = lds0 + AT_V + wid * 8192;
    TS(ws, 1, 0);
    asm volatile("s_nop 4" ::: ACLOB);
    dma_tile(w.mk, c.goff, w.Kh + (size_t)tile_of(w, 0) * 32768);
    q_loads<0>((unsigned)lane * 16u, Qh + (size_t)(bq * 4 + wid) * 16384, make_iseq<16>{});
    dma_tile(w.mk + 32768, c.goff, w.Kh + (size_t)tile_of(w, 1) * 32768);
    dma_tile(w.mv, c.goff, w.Vh + (size_t)tile_of(w, 0) * 32768);
    if (wid == 3) CODE_TOUCH2(K2_SYM, lds0 + AT_V + 65536, j & 7, lane);
    const bool q2now = !isA && w.n1 == 1;
    if (q2now) q_loads<16>((unsigned)lane * 16u, Qh + (size_t)(i * 4 + wid) * 16384, make_iseq<16>{});
    a_zeros(make_iseq<128>{});
    c.cm[0] = f32x16{}; c.cm[1] = f32x16{}; c.m = 0.f; c.l = 0.f; c.rs0 = 0.f; c.rs1 = 0.f; c.alpha = 1.f; c.pend = false; c.drain = false;
    c.aone = hi ? 0.f : 1.f;
    PIN(c.cm[0]);
    if (q2now) asm volatile("s_waitcnt vmcnt(32)" ::: "memory"); else asm volatile("s_waitcnt vmcnt(16)" ::: "memory");
    asm volatile("s_barrier" ::: "memory");
    SBAR();
    c.fr[0] = kfrag<0, 0>(c); c.fr[1] = kfrag<0, 1>(c); c.fr[2] = kfrag<0, 2>(c); c.fr[3] = kfrag<0, 3>(c); c.fr[4] = kfrag<0, 4>(c); c.fr[5] = kfrag<0, 5>(c);
    SBAR();
    phaseA<1, 0>(c, make_iseq<32>{});
    dma_tile(w.mv + 32768, c.goff, w.Vh + (size_t)tile_of(w, 1) * 32768);
    if (!isA && !q2now) q_loads<16>((unsigned)lane * 16u, Qh + (size_t)(i * 4 + wid) * 16384, make_iseq<16>{});
    SBAR(); asm volatile("s_nop 7\n\ts_nop 7" : "+v"(c.s[0][0]), "+v"(c.s[0][1])); SBAR();
    c.first = true;
    maxsteps1(c, make_iseq<8>{});
    decide<1>(c);
    exps_from<0, 0>(c, make_iseq<NEB>{});
    if (isA || q2now) asm volatile("s_waitcnt vmcnt(8)" ::: "memory");
    else asm volatile("s_waitcnt vmcnt(24)" ::: "memory");
    asm volatile("s_waitcnt lgkmcnt(0)\n\ts_barrier" ::: "memory");
    SBAR();
    c.fr[0] = kfrag<1, 0>(c); c.fr[1] = kfrag<1, 1>(c); c.fr[2] = kfrag<1, 2>(c); c.fr[3] = kfrag<1, 3>(c); c.fr[4] = kfrag<1, 4>(c); c.fr[5] = kfrag<1, 5>(c);
    TS(ws, 1, 1);
    for (int g0 = 0, gstop = isA ? 14 : w.n1 - 1; ; g0 += 2, gstop = 14) {
        for (; g0 < gstop; g0 += 2) pair<false, false>(c, w, g0);
        if (g0 >= 14) break;
        pair<false, true>(c, w, g0);
    }
    TS(ws, 1, 2);
    if (!isA && w.n1 == 15) pair<true, true>(c, w, 14); else pair<true, false>(c, w, 14);
    TS(ws, 1, 3);
    int b2 = b; asm volatile("" : "+s"(b2), "+s"(ws));
    const int sk = b2 & 3, rb = 8 * ((b2 >> 2) & 1) + (b2 >> 5), cn = (b2 >> 3) & 3, grp = rb * 4 + cn, fr = lane & 15, fq = lane >> 4;
    o.wbase = (const char*)ws + WS_WI + (size_t)(sk * 4 + cn) * (8 * OP_WST) + wid * 7168;
    o.cbase = (const char*)ws + WS_CTX + ((size_t)(rb * 8 + 4 * (wid >> 1)) * 128 + sk * 32 + (wid & 1)) * 1024;
    o.lds0 = lds0 + wid * 7168; o.wp = lds + lane * 16 + (wid & 1) * 7168; o.ws = ws;
    c.vcur = lds + AT_V + 32768 + lane * 16;
    c.fr[0] = vfrag<0>(c.vcur); c.fr[1] = vfrag<1>(c.vcur); c.fr[2] = vfrag<2>(c.vcur); c.fr[3] = vfrag<3>(c.vcur); c.fr[4] = vfrag<4>(c.vcur); c.fr[5] = vfrag<5>(c.vcur);
    if (isA && !c.early) {
        unsigned spins = 0;
        while (rfl(__hip_atomic_load(w.flag, RLX_AGENT)) == 0u) { __builtin_amdgcn_s_sleep(1); if (++spins > (1u << 22)) break; }
        __builtin_amdgcn_fence(__ATOMIC_ACQUIRE, "wavefront");
        ml_load(c);
        p_loads(c, make_iseq<16>{});
    }
    SBAR();
    exps_from<0, NEB>(c, make_iseq<32 - NEB>{});
    packs<0>(c, make_iseq<16>{});
    c.l += c.rs0 + c.rs1;
    PIN(c.p[0]); PIN(c.p[1]); PIN(c.p[2]); PIN(c.p[3]);
    SBAR(); NOPS2(); SBAR();
    phaseB<0, 64>(c, make_iseq<32>{});
    SBAR();
    const float lt = halfsum(c.l);
    TS(ws, 1, 4);
    {
        const __amdgpu_buffer_rsrc_t crs = __builtin_amdgcn_make_buffer_rsrc(ws + WS_CTX, (short)0, (int)(16 * MB), 0x00020000);
        const int row = (isA ? bq : i) * 128 + wid * 32 + r32;
        const int voff = ((row >> 4) * 128 + head * 8) * 1024 + hi * 256 + (row & 15) * 16;
        if (isA) {
            asm volatile("s_waitcnt vmcnt(28)" : "+v"(c.ml2) :: "memory");
            const float mn = __builtin_fmaxf(c.m, c.ml2[0]);
            const float w1 = ex2(c.m - mn), w2 = ex2(c.ml2[0] - mn);
            const float inv = 1.0f / (lt * w1 + c.ml2[1] * w2);
            SBAR(); NOPS16(); SBAR();
            ctx_outs<true>(w1 * inv, w2 * inv, crs, voff, make_iseq<8>{});
        } else {
            const float inv = 1.0f / lt;
            SBAR(); NOPS16(); SBAR();
            ctx_outs<false>(inv, 0.f, crs, voff, make_iseq<8>{});
        }
    }
    TS(ws, 1, 5);
    unsigned* flags = (unsigned*)(ws + WS_FLAGS);
    f32x4 ac2[28];
#pragma unroll
    for (int f = 0; f < 28; ++f) { ac2[f] = (f32x4){0.f, 0.f, 0.f, 0.f}; PIN(ac2[f]); }
    asm volatile("s_waitcnt vmcnt(0)\n\ts_barrier" ::: "memory");
    if (tid == 0) __hip_atomic_store(flags + (N_AFLAGS + N_OFLAGS + (head >> 2) * 16 + (isA ? bq : i)) * FLAG_STRIDE + (head & 3), 1u, RLX_AGENT);
    TS(ws, 1, 7);
    TS(ws, 2, 0);
    op_wstage<4>(o);
    const int trow = rb * 128 + wid * 32 + (sk >> 1) * 16 + fr, tcol = cn * 112 + (sk & 1) * 64 + 4 * fq;
    u32x2 xw[4], yw[4];
    {
        const bf16_t* y2p = (const bf16_t*)(ws + WS_Y2);
#pragma unroll
        for (int f = 0; f < 4; ++f) {
            const int cc = tcol + ((f == 3 && (sk & 1)) ? 0 : f * 16);
            xw[f] = *(const u32x2*)(x + (size_t)trow * DM + cc);
            const bf16_t* yp = y2p + (size_t)cc * SEQ + trow;
            yw[f] = (u32x2){yp[0] | (unsigned)yp[SEQ] << 16, yp[2 * SEQ] | (unsigned)yp[3 * SEQ] << 16};
        }
    }
    const float gate = ((const float*)(ws + WS_G))[trow];
    bf16x8 wf[8];
    wf[0] = op_wfrag<0>(o); wf[1] = op_wfrag<1>(o); wf[2] = op_wfrag<2>(o); wf[3] = op_wfrag<3>(o);
    SBAR();
    poll4(flags + (N_AFLAGS + N_OFLAGS + sk * 16 + rb) * FLAG_STRIDE, lane);
    asm volatile("s_waitcnt vmcnt(0)" ::: "memory");
    TS(ws, 2, 4);
    op_cloads<0>(o, make_iseq<56>{});
    SBAR();
    op_steps(o, ac2, wf, make_iseq<112>{});
#pragma unroll
    for (int f = 0; f < 28; ++f) PIN(ac2[f]);
    NOPS16();
#pragma unroll
    for (int f = 0; f < 28; ++f) PIN(ac2[f]);
    f32x4 acc[14];
    {
        ldsp xw_ = lds + (wid >> 1 ? op_slot(3) : op_slot(4)) + lane * 16;
        if (wid & 1) {
#pragma unroll
            for (int f = 0; f < 14; ++f) { *(LAS f32x4*)(xw_ + 14336 + f * 1024) = ac2[f]; acc[f] = ac2[14 + f]; }
        } else {
#pragma unroll
            for (int f = 0; f < 14; ++f) { *(LAS f32x4*)(xw_ + f * 1024) = ac2[14 + f]; acc[f] = ac2[f]; }
        }
        asm volatile("s_waitcnt lgkmcnt(0)\n\ts_barrier" ::: "memory");
        ldscp xr = xw_ + (wid & 1 ? 0 : 14336);
#pragma unroll
        for (int f = 0; f < 14; ++f) acc[f] += *(const LAS f32x4*)(xr + f * 1024);
    }
    TS(ws, 2, 1);
    float* slab = (float*)(ws + WS_OPART) + (size_t)grp * 4 * 14336;
    const __amdgpu_buffer_rsrc_t srs = __builtin_amdgcn_make_buffer_rsrc((void*)slab, (short)0, 4 * 57344, 0x00020000);
    const int soff = ((sk * 4 + wid) * 14 * 64 + lane) * 16;
    if (sk == 0) op_publish<0>(acc, srs, soff); else if (sk == 1) op_publish<1>(acc, srs, soff);
    else if (sk == 2) op_publish<2>(acc, srs, soff); else op_publish<3>(acc, srs, soff);
    asm volatile("s_waitcnt vmcnt(0)\n\ts_barrier" ::: "memory");
    TS(ws, 2, 2);
    unsigned* fl = flags + (N_AFLAGS + grp) * FLAG_STRIDE;
    if (tid == 0) __hip_atomic_store(fl + sk, 1u, RLX_AGENT);
    poll4(fl, lane);
    TS(ws, 2, 3);
    bf16_t* outp = out + (size_t)trow * DM + tcol;
    const int loff = (wid * 14 * 64 + lane) * 16;
    if (sk == 0) op_finish<0>(acc, srs, loff, xw, yw, gate, outp); else if (sk == 1) op_finish<1>(acc, srs, loff, xw, yw, gate, outp);
    else if (sk == 2) op_finish<2>(acc, srs, loff, xw, yw, gate, outp); else op_finish<3>(acc, srs, loff, xw, yw, gate, outp);
    TS(ws, 2, 7);
}

extern "C" __device__ __attribute__((used, noinline)) void text_end_mark() { asm volatile("s_nop 0"); }

extern "C" void kernel_launch(void* const* d_in, const int* in_sizes, int n_in, void* d_out, int out_size, void* d_ws, size_t ws_size,
                              hipStream_t stream) {
    (void)in_sizes; (void)n_in; (void)out_size; (void)ws_size;
    (void)hipFuncSetAttribute((const void*)k1_proj, hipFuncAttributeMaxDynamicSharedMemorySize, K1_LDS);
    (void)hipFuncSetAttribute((const void*)k2_attn, hipFuncAttributeMaxDynamicSharedMemorySize, AT_LDS);
    K1Args P;
    P.x = (const bf16_t*)d_in[0]; P.ak = (const bf16_t*)d_in[1]; P.av = (const bf16_t*)d_in[2]; P.shift_k = (const bf16_t*)d_in[3];
    P.ssm_k = (const bf16_t*)d_in[4]; P.ssm_d = (const bf16_t*)d_in[5]; P.ckv = (const bf16_t*)d_in[7]; P.wq = (const bf16_t*)d_in[8];
    P.wuk = (const bf16_t*)d_in[9]; P.wuv = (const bf16_t*)d_in[10]; P.gate_w = (const bf16_t*)d_in[12]; P.wo = (const bf16_t*)d_in[11]; P.ws = (unsigned char*)d_ws;
#define DEV_STAGES 7
    if (DEV_STAGES & 1) k1_proj<<<256, 512, K1_LDS, stream>>>(P);
    if (DEV_STAGES & 2) k2_attn<<<256, 256, AT_LDS, stream>>>((unsigned char*)d_ws, (const bf16_t*)d_in[0], (bf16_t*)d_out);
}
```

```cpp
#include <hip/hip_runtime.h>
#include <hip/hip_bf16.h>
#include <stdint.h>
#include <utility>

#define LAS __attribute__((address_space(3)))
#define GAS __attribute__((address_space(1)))
#define CAS __attribute__((address_space(4)))
#define DI __device__ __forceinline__
typedef unsigned short bf16_t;
typedef short bf16x8 __attribute__((ext_vector_type(8)));
typedef short s16x4 __attribute__((ext_vector_type(4)));
typedef float f32x2 __attribute__((ext_vector_type(2)));
typedef float f32x4 __attribute__((ext_vector_type(4)));
typedef float f32x16 __attribute__((ext_vector_type(16)));
typedef unsigned u32x2 __attribute__((ext_vector_type(2)));
typedef unsigned u32x4 __attribute__((ext_vector_type(4)));
typedef __bf16 bfx2 __attribute__((ext_vector_type(2)));
typedef LAS unsigned char* ldsp;
typedef const LAS unsigned char* ldscp;

constexpr int SEQ = 2048, DM = 448, NH = 16, HD = 256, DC = 1024, NW_ = 4096;

constexpr size_t MB = 1024 * 1024;
constexpr size_t WS_Q = 0, WS_K = 16 * MB, WS_V = 32 * MB, WS_CTX = 48 * MB, WS_PART = 64 * MB, WS_ML = 80 * MB,
                 WS_Y2 = 81 * MB, WS_G = 84 * MB, WS_OPART = 85 * MB, WS_FLAGS = 100 * MB, WS_WI = 102 * MB;
constexpr int FLAG_STRIDE = 64;
constexpr int N_AFLAGS = 128, N_OFLAGS = 64, N_CFLAGS = 64;

#define TS(wsp, kern, slot) do { } while (0)
#define TSI(wsp, idx) do { } while (0)
#define SBAR() __builtin_amdgcn_sched_barrier(0)
#define PIN(x) asm volatile("" : "+v"(x))
#define RLX_AGENT __ATOMIC_RELAXED, __HIP_MEMORY_SCOPE_AGENT

DI unsigned cvtpk(float lo, float hi) { f32x2 v = {lo, hi}; bfx2 b = __builtin_convertvector(v, bfx2); return __builtin_bit_cast(unsigned, b); }
DI float bf_lo(unsigned w) { return __uint_as_float(w << 16); }
DI float bf_hi(unsigned w) { return __uint_as_float(w & 0xffff0000u); }
DI float rbf(float x) { return bf_lo(cvtpk(x, 0.f)); }
DI unsigned rfl(unsigned x) { return __builtin_amdgcn_readfirstlane(x); }
DI s16x4 trread(ldscp p) { return __builtin_amdgcn_ds_read_tr16_b64_v4i16((LAS s16x4*)p); }
DI bf16x8 cat8(s16x4 lo, s16x4 hi) { return __builtin_shufflevector(lo, hi, 0, 1, 2, 3, 4, 5, 6, 7); }
DI bf16x8 ldsrd16(ldscp p) { return *(const LAS bf16x8*)p; }
#define MFMA16(a, b, c) __builtin_amdgcn_mfma_f32_16x16x32_bf16((a), (b), (c), 0, 0, 0)
#define MFMA32(a, b, c) __builtin_amdgcn_mfma_f32_32x32x16_bf16((a), (b), (c), 0, 0, 0)

DI void touch(unsigned sink, unsigned voff, const char* g) {
    asm volatile("s_mov_b32 m0, %0\n\ts_nop 0\n\tglobal_load_lds_dword %1, %2" :: "s"(sink), "v"(voff), "s"(g) : "memory");
}
#define CODE_TOUCH(SYM, END, sink, chunk, lane) do { const char* _p; unsigned _n; \
        asm volatile("s_getpc_b64 s[90:91]\n\ts_add_u32 s90, s90, " SYM "@rel32@lo+4\n\ts_addc_u32 s91, s91, " SYM "@rel32@hi+12\n\ts_mov_b64 %0, s[90:91]\n\ts_mov_b32 %1, " END "-" SYM \
                     : "=s"(_p), "=s"(_n) :: "s90", "s91", "scc"); \
        const unsigned _o = ((unsigned)(chunk) * 64u + (unsigned)(lane)) * 128u; \
        if ((unsigned)(chunk) * 8192u < _n && _n < 262144u) touch((sink), _o < _n - 4u ? _o : _n - 4u, _p); } while (0)
#define K1_SYM "_Z7k1_proj6K1Args", "_Z7k2_attnPhPKtPt"
#define K2_SYM "_Z7k2_attnPhPKtPt", "text_end_mark"
#define CODE_TOUCH2(S, sink, chunk, lane) CODE_TOUCH(S, sink, chunk, lane)

constexpr int HTB = 128 * 64 * 2;
constexpr int K1_X = 8 * HTB;
constexpr int K1_LDS = K1_X + 16384 + 2048;

DI int lds_byte(int r, int c) { const int st = (r >> 4) * 2 + (c >> 5), rr = r & 15, cc = c & 31, ob = rr * 64 + cc * 2; return st * 1024 + (ob ^ (((ob >> 9) & 1) << 5)); }
DI void stage_rc(int b, int& R, int& C) { const int st = b / 1024, sb = b % 1024, swz = sb ^ (((sb >> 9) & 1) << 5); R = (st >> 1) * 16 + swz / 64; C = (st & 1) * 32 + (swz % 64) / 2; }

struct K1Args {
    const bf16_t *x, *ak, *av, *shift_k, *ssm_k, *ssm_d, *ckv, *wq, *wuk, *wuv, *gate_w, *wo;
    unsigned char* ws;
};

#define SA(b, h) (((b) * 2 + (h)) * HTB)
#define SB(b, h) ((4 + (b) * 2 + (h)) * HTB)
constexpr int ring_slot(int u) {
    switch (u & 7) { case 5: return SB(0, 0); case 6: return SA(0, 0); case 7: return SB(0, 1); case 0: return SA(0, 1);
                     case 1: return SB(1, 0); case 2: return SA(1, 0); case 3: return SB(1, 1); default: return SA(1, 1); }
}
struct G1 {
    ldsp lds; unsigned ldsw  , voffA_kv, voffA_q, voffB, sv; int aoff, boff0, boff1;
    const char *A_kv, *A_q, *B_kv, *B_q, *sbase; unsigned sstep, sdst, sdstep; int sadd;
};
DI int lane_id() { int l; asm volatile("v_mbcnt_lo_u32_b32 %0, -1, 0\n\tv_mbcnt_hi_u32_b32 %0, -1, %0" : "=v"(l)); return l; }
template <int OFF> DI void dma16(unsigned lbase, unsigned voff, const char* g) {
    asm volatile("s_add_i32 m0, %0, %c3\n\ts_nop 0\n\tglobal_load_lds_dwordx4 %1, %2" :: "s"(lbase), "v"(voff), "s"(g), "i"(OFF) : "memory", "scc");
}
template <int BUF, int STRIDE, int H> DI void stage_a(const G1& g, const char* base, unsigned voff) {
    dma16<BUF>(g.ldsw, voff, base + H * 128 * STRIDE); dma16<BUF + 8192>(g.ldsw, voff, base + (H * 128 + 64) * STRIDE);
}
template <int BUF, int H> DI void stage_b(const G1& g, const char* base) {
    dma16<BUF>(g.ldsw, g.voffB, base + H * 256); dma16<BUF + 8192>(g.ldsw, g.voffB, base + H * 256 + 32 * NW_ * 2);
}
template <int U> DI void stage_unit(const G1& g) {
    if constexpr (U < 21) {
        constexpr int t = U / 3, r = U % 3;
        if constexpr (r == 0) stage_b<ring_slot(U), 0>(g, g.B_q + (size_t)t * (64 * NW_ * 2));
        else stage_a<ring_slot(U), DM * 2, r - 1>(g, g.A_q + t * 128, g.voffA_q);
    } else {
        constexpr int t = (U - 21) >> 2, r = (U - 21) & 3;
        if constexpr ((r & 1) == 0) stage_b<ring_slot(U), (r >> 1)>(g, g.B_kv + (size_t)t * (64 * NW_ * 2));
        else stage_a<ring_slot(U), DC * 2, (r >> 1)>(g, g.A_kv + t * 128, g.voffA_kv);
    }
}
DI void side(const G1& g, int t) {
    const int tt = t + g.sadd > 15 ? 15 : t + g.sadd;
    asm volatile("s_mov_b32 m0, %0\n\ts_nop 0\n\tglobal_load_lds_dword %1, %2" :: "s"(g.sdst + (unsigned)t * g.sdstep), "v"(g.sv), "s"(g.sbase + rfl((unsigned)tt * g.sstep)) : "memory");
}
DI void lda(bf16x8 (&dst)[4][2], const G1& g, int buf) {
#pragma unroll
    for (int m = 0; m < 4; ++m)
#pragma unroll
        for (int k = 0; k < 2; ++k) dst[m][k] = ldsrd16(g.lds + buf + g.aoff + m * 2048 + k * 1024);
}
DI void ldb(bf16x8 (&dst)[2][2], const G1& g, int buf) {
#pragma unroll
    for (int n = 0; n < 2; ++n)
#pragma unroll
        for (int k = 0; k < 2; ++k) { ldscp p = g.lds + buf + (n ? g.boff1 : g.boff0) + k * 8192; dst[n][k] = cat8(trread(p), trread(p + 256)); }
}
template <bool VT> DI void mma(f32x4 (&acc)[4][2], const bf16x8 (&At)[4][2], const bf16x8 (&Bt)[2][2]) {
    __builtin_amdgcn_s_setprio(1);
#pragma unroll
    for (int m = 0; m < 4; ++m)
#pragma unroll
        for (int n = 0; n < 2; ++n)
#pragma unroll
            for (int k = 0; k < 2; ++k) acc[m][n] = VT ? MFMA16(At[m][k], Bt[n][k], acc[m][n]) : MFMA16(Bt[n][k], At[m][k], acc[m][n]);
    __builtin_amdgcn_s_setprio(0);
}
#define WAIT_V(n) asm volatile("s_waitcnt vmcnt(" #n ")" ::: "memory")
#define WAIT_L(n) asm volatile("s_waitcnt lgkmcnt(" #n ")" ::: "memory")
#define BAR __builtin_amdgcn_s_barrier()
DI void zero8(f32x4 (&a)[4][2]) {
#pragma unroll
    for (int m = 0; m < 4; ++m) { a[m][0] = (f32x4){0.f, 0.f, 0.f, 0.f}; a[m][1] = (f32x4){0.f, 0.f, 0.f, 0.f}; }
}

DI void pack_kq(const f32x4 (&acc)[4][2], u32x4* pk, float scale) {
#pragma unroll
    for (int m = 0; m < 4; ++m) {
        const f32x4 a0 = acc[m][0] * scale, a1 = acc[m][1] * scale;
        const unsigned w0 = cvtpk(a0[0], a0[1]), w1 = cvtpk(a0[2], a0[3]), x0 = cvtpk(a1[0], a1[1]), x1 = cvtpk(a1[2], a1[3]);
        auto r0 = __builtin_amdgcn_permlane16_swap(w0, x0, false, false);
        auto r1 = __builtin_amdgcn_permlane16_swap(w1, x1, false, false);
        pk[m] = (u32x4){r0[0], r1[0], r0[1], r1[1]};
    }
}
DI unsigned kq_off(int pm, int wr, int wc, int fr, int fq) {
    return (unsigned)((pm * 8 + wr * 2) * 16384 + fr * 16 + (2 * wc + (fq & 1)) * 1024 + (fq >> 1) * 512);
}
DI void pack_v(const f32x4 (&acc)[4][2], u32x4* pk) {
#pragma unroll
    for (int n = 0; n < 2; ++n)
#pragma unroll
        for (int mm = 0; mm < 2; ++mm) {
            const f32x4 a0 = acc[2 * mm][n], a1 = acc[2 * mm + 1][n];
            const unsigned w0 = cvtpk(a0[0], a0[1]), w1 = cvtpk(a0[2], a0[3]), x0 = cvtpk(a1[0], a1[1]), x1 = cvtpk(a1[2], a1[3]);
            auto r0 = __builtin_amdgcn_permlane32_swap(w0, x0, false, false);
            auto r1 = __builtin_amdgcn_permlane32_swap(w1, x1, false, false);
            pk[2 * mm + n] = (u32x4){r0[0], r1[0], r0[1], r1[1]};
        }
}
#define ST_AUX 16
struct Out1 { u32x4 pk[16]; __amdgpu_buffer_rsrc_t rs; unsigned voff; int sbj, shi; };
template <int I> DI void st_piece(const Out1& o) {
    __builtin_amdgcn_raw_buffer_store_b128(o.pk[I], o.rs, o.voff + (I & 1) * 256, (I >> 3) * 65536 + ((I >> 2) & 1) * o.sbj + ((I >> 1) & 1) * o.shi, ST_AUX);
}
template <int A, int B> DI void st_range(const Out1& o) { if constexpr (A < B) { st_piece<A>(o); st_range<A + 1, B>(o); } }

template <int N, class W> DI void qphase(const G1& g, f32x4 (&a0)[4][2], f32x4 (&a1)[4][2], bf16x8 (&At)[4][2], bf16x8 (&B0)[2][2], W& warm) {
    constexpr int t = N >> 1;
    if constexpr ((N & 1) == 0) {
        ldb(B0, g, ring_slot(3 * t)); SBAR(); lda(At, g, ring_slot(3 * t + 1));
        if constexpr (t < 5) warm(t);
        if constexpr (N >= 2) stage_unit<3 * t + 6>(g);
        if constexpr (N == 0) WAIT_V(10); else WAIT_V(8);
        WAIT_L(8); BAR; WAIT_L(0); mma<false>(a0, At, B0); BAR; SBAR();
    } else {
        lda(At, g, ring_slot(3 * t + 2));
        if constexpr (t >= 1) stage_unit<3 * t + 7>(g);
        stage_unit<3 * t + 8>(g);
        WAIT_V(8);
        if constexpr (N == 13) WAIT_L(0);
        BAR; WAIT_L(0); mma<false>(a1, At, B0); BAR; SBAR();
    }
}
template <class W, int... Ns> DI void qphases(const G1& g, f32x4 (&a0)[4][2], f32x4 (&a1)[4][2], bf16x8 (&At)[4][2], bf16x8 (&B0)[2][2], W& warm, std::integer_sequence<int, Ns...>) {
    (qphase<Ns>(g, a0, a1, At, B0, warm), ...);
}
template <int... Us> DI void stage_units(const G1& g, std::integer_sequence<int, Us...>) { (stage_unit<Us>(g), ...); }

template <bool VT, int POS, class W>
DI void mainpair(const G1& g, int t, f32x4 (&acc)[2][2][4][2], bf16x8 (&At)[4][2], bf16x8 (&B0)[2][2], bf16x8 (&B1)[2][2], W& lastside) {
    const char* a1 = g.A_kv + (t + 1) * 128; const char* a2 = a1 + 128; const char* a3 = a1 + 256;
    const char* b2 = g.B_kv + (size_t)(t + 2) * (64 * NW_ * 2); const char* b3 = b2 + 64 * NW_ * 2;
    ldb(B0, g, SB(0, 0)); SBAR(); lda(At, g, SA(0, 0));
    if constexpr (POS != 0) stage_a<SA(1, 1), DC * 2, 1>(g, a1, g.voffA_kv); else WAIT_V(18);
    WAIT_L(8); BAR; WAIT_L(0); mma<VT>(acc[0][0], At, B0); BAR; SBAR();
    ldb(B1, g, SB(0, 1));
    if constexpr (POS == 2) lastside(0); else side(g, t);
    if constexpr (POS != 2) stage_b<SB(0, 0), 0>(g, b2);
    if constexpr (POS == 0) WAIT_V(19);
    BAR; WAIT_L(0); mma<VT>(acc[0][1], At, B1); BAR;
    lda(At, g, SA(0, 1));
    if constexpr (POS != 2) stage_a<SA(0, 0), DC * 2, 0>(g, a2, g.voffA_kv);
    BAR; WAIT_L(0); mma<VT>(acc[1][0], At, B0); BAR; SBAR();
    if constexpr (POS != 2) stage_b<SB(0, 1), 1>(g, b2);
    if constexpr (POS == 0) WAIT_V(15); else if constexpr (POS == 1) WAIT_V(7); else WAIT_V(1);
    BAR; mma<VT>(acc[1][1], At, B1); BAR;
    ldb(B0, g, SB(1, 0)); SBAR(); lda(At, g, SA(1, 0));
    if constexpr (POS != 2) stage_a<SA(0, 1), DC * 2, 1>(g, a2, g.voffA_kv);
    WAIT_L(8); BAR; WAIT_L(0); mma<VT>(acc[0][0], At, B0); BAR; SBAR();
    ldb(B1, g, SB(1, 1));
    if constexpr (POS == 2) lastside(1); else side(g, t + 1);
    if constexpr (POS != 2) stage_b<SB(1, 0), 0>(g, b3);
    BAR; WAIT_L(0); mma<VT>(acc[0][1], At, B1); BAR;
    lda(At, g, SA(1, 1));
    if constexpr (POS != 2) stage_a<SA(1, 0), DC * 2, 0>(g, a3, g.voffA_kv);
    BAR; WAIT_L(0); mma<VT>(acc[1][0], At, B0); BAR; SBAR();
    if constexpr (POS != 2) { stage_b<SB(1, 1), 1>(g, b3); WAIT_V(7); }
    BAR; mma<VT>(acc[1][1], At, B1); BAR;
}

constexpr int CV_RAW = SA(0, 0);
constexpr int K1_WO = SA(0, 1);
constexpr int CV_HSTR = 4256;
constexpr int CV_H = 2 * HTB;
constexpr int CV_USTR = 4608;
constexpr int CV_U = CV_H + 8 * CV_HSTR;
constexpr int CV_R = CV_U + 2 * CV_USTR;
static_assert(CV_R + 65536 <= K1_LDS && CV_R <= K1_X, "lds");

DI void tail_stage(const G1& g, const K1Args& P, int c0, int e, int wid, int lane) {
    dma16<CV_RAW>(g.ldsw, (unsigned)lane * 16u, (const char*)P.ssm_k + (size_t)c0 * (SEQ * 2) + wid * 1024);
    if (wid < 7) {
        const char* wg = (const char*)P.wo + ((size_t)((e >> 6) * 1024 + (e & 15) * 64) * DM + ((e >> 4) & 3) * 112) * 2;
        { const int ci = wid * 64 + lane, k = ci / 14, cc = ci - k * 14; dma16<K1_WO>(g.ldsw, (unsigned)(k * DM + cc * 8) * 2u, wg); }
        { const int ci = (wid + 7) * 64 + lane, k = ci / 14, cc = ci - k * 14; dma16<K1_WO + 7168>(g.ldsw, (unsigned)(k * DM + cc * 8) * 2u, wg); }
    }
}
DI void wo_image(ldsp lds, const K1Args& P, int e, int wid, int lane) {
    const int fr = lane & 15, fq = lane >> 4;
    if (wid < 7) {
        ldscp wp = lds + K1_WO + (16 * (fq >> 1) + 4 * (fq & 1) + (fr >> 2)) * 224 + (fr & 3) * 8 + wid * 32;
        unsigned char* op = P.ws + WS_WI + (size_t)e * 14336 + wid * 1024 + lane * 16;
#pragma unroll
        for (int kk = 0; kk < 2; ++kk) *(bf16x8*)(op + kk * 7168) = cat8(trread(wp + kk * 7168), trread(wp + kk * 7168 + 1792));
    }
}
DI void conv_copies(ldsp q, const u32x4 d, const u32x2 e) {
    const unsigned a0 = __builtin_amdgcn_alignbit(d[1], d[0], 16), a1 = __builtin_amdgcn_alignbit(d[2], d[1], 16), a2 = __builtin_amdgcn_alignbit(d[3], d[2], 16),
                   a3 = __builtin_amdgcn_alignbit(e[0], d[3], 16), a4 = __builtin_amdgcn_alignbit(e[1], e[0], 16);
    *(LAS u32x4*)(q) = (u32x4){a0, a1, a2, a3};
    *(LAS u32x4*)(q + CV_HSTR) = (u32x4){d[1], d[2], d[3], e[0]};
    *(LAS u32x4*)(q + 2 * CV_HSTR) = (u32x4){a1, a2, a3, a4};
    *(LAS u32x4*)(q + 3 * CV_HSTR) = (u32x4){d[2], d[3], e[0], e[1]};
}
DI void conv_frags(bf16x8 (&A)[8], bf16x8 (&B)[8], ldscp hp, ldscp up) {
#pragma unroll
    for (int v = 0; v < 8; ++v) {
        B[v] = ldsrd16(up + (7 - v) * 512);
        const s16x4 lo = *(const LAS s16x4*)(hp + v * 512), hi = *(const LAS s16x4*)(hp + v * 512 + 8);
        A[v] = cat8(lo, hi);
    }
}
DI void conv_mma(f32x4 (&acc)[8], const bf16x8 (&A)[8], const bf16x8 (&B)[8]) {
#pragma unroll
    for (int J = 0; J < 8; ++J)
#pragma unroll
        for (int v = 0; v <= J; ++v) acc[J] = MFMA16(A[v], B[J - v], acc[J]);
}
DI void conv_part(ldsp lds, const K1Args& P, int c0, const u32x4 sw, const unsigned dw, int wid, int lane, const Out1& o) {
    const int tid = wid * 64 + lane, fr = lane & 15, fq = lane >> 4;
    {
        const int cq = tid & 255;
        const u32x4 d = *(const LAS u32x4*)(lds + CV_RAW + tid * 16);
        u32x2 e = *(const LAS u32x2*)(lds + CV_RAW + tid * 16 + 16);
        const u32x4 z4 = {0u, 0u, 0u, 0u}; const u32x2 z2 = {0u, 0u};
        if (cq == 255) e = z2;
        ldsp q = lds + CV_H + (tid >> 8) * 4 * CV_HSTR + (cq + 2) * 16;
        conv_copies(q, d, e);
        if (cq == 0) { conv_copies(q - 16, z4, (u32x2){d[0], d[1]}); conv_copies(q - 32, z4, z2); }
        if (cq == 255) { conv_copies(q + 16, z4, z2); conv_copies(q + 32, z4, z2); }
    }
    SBAR(); st_range<4, 6>(o); SBAR();
    {
        ldscp kp = lds + K1_X + tid * 16;
        u32x4 a = *(const LAS u32x4*)(kp - 16);
        const u32x4 b = *(const LAS u32x4*)(kp), vr = *(const LAS u32x4*)(kp + 8192);
        if (tid == 0) a = (u32x4){0u, 0u, 0u, 0u};
        const unsigned kr[7] = {a[1], a[2], a[3], b[0], b[1], b[2], b[3]};
#pragma unroll
        for (int c = 0; c < 2; ++c) {
            const float w0 = bf_lo(sw[2 * c]), w1 = bf_hi(sw[2 * c]), w2 = bf_lo(sw[2 * c + 1]), w3 = bf_hi(sw[2 * c + 1]);
            float kc[7], kv[4];
#pragma unroll
            for (int i = 0; i < 7; ++i) kc[i] = c ? bf_hi(kr[i]) : bf_lo(kr[i]);
#pragma unroll
            for (int i = 0; i < 4; ++i) {
                float s = rbf(kc[i] * w3);
                s = rbf(s + rbf(kc[i + 1] * w2));
                s = rbf(s + rbf(kc[i + 2] * w1));
                s = rbf(s + rbf(kc[i + 3] * w0));
                kv[i] = s * (c ? bf_hi(vr[i]) : bf_lo(vr[i]));
            }
            u32x2 wv = {cvtpk(kv[3], kv[2]), cvtpk(kv[1], kv[0])};
            *(LAS u32x2*)(lds + CV_U + c * CV_USTR + (2044 - 4 * tid) * 2) = wv;
            SBAR(); if (c == 0) st_range<6, 8>(o); else st_range<8, 10>(o); SBAR();
        }
        if (tid < 120) { const int c = tid / 60, j = tid - c * 60; *(LAS u32x2*)(lds + CV_U + c * CV_USTR + 4096 + j * 8) = (u32x2){0u, 0u}; }
    }
    __syncthreads();
    TS(P.ws, 0, 5);
    const int ch = wid >> 2, qw = wid & 3;
    {
        const int y1 = 24 + fr - 8 * fq;
        ldscp hb = lds + CV_H + ch * 4 * CV_HSTR + (y1 & 3) * CV_HSTR + (y1 & ~3) * 2 + qw * 128;
        ldscp ub = lds + CV_U + ch * CV_USTR + (2056 - 256 * 7 - 16 * fr - 8 * fq) * 2 + qw * 128;
        f32x4 acc[8];
#pragma unroll
        for (int i = 0; i < 8; ++i) acc[i] = (f32x4){0.f, 0.f, 0.f, 0.f};
        bf16x8 A0[8], B0[8], A1[8], B1[8];
        conv_frags(A0, B0, hb, ub); SBAR();
        conv_frags(A1, B1, hb + 64, ub + 64); SBAR();
        st_range<10, 12>(o); SBAR();
        conv_mma(acc, A0, B0); SBAR();
        st_range<12, 14>(o); SBAR();
        conv_mma(acc, A1, B1); SBAR();
        st_range<14, 16>(o); SBAR();
#pragma unroll
        for (int i = 0; i < 8; ++i) *(LAS f32x4*)(lds + CV_R + wid * 8192 + i * 1024 + lane * 16) = acc[i];
    }
    __syncthreads();
    TS(P.ws, 0, 6);
    {
        const float dd = ch ? bf_hi(dw) : bf_lo(dw);
#pragma unroll
        for (int i = 0; i < 2; ++i) {
            ldscp rp = lds + CV_R + ch * 32768 + (2 * qw + i) * 1024 + lane * 16;
            const f32x4 y = ((*(const LAS f32x4*)rp + *(const LAS f32x4*)(rp + 8192)) + *(const LAS f32x4*)(rp + 16384)) + *(const LAS f32x4*)(rp + 24576);
            const int t = 256 * (2 * qw + i) + 16 * fr + 4 * fq;
            const u32x2 uw = *(const LAS u32x2*)(lds + CV_U + ch * CV_USTR + (2044 - t) * 2);
            const float kv[4] = {bf_hi(uw[1]), bf_lo(uw[1]), bf_hi(uw[0]), bf_lo(uw[0])};
            float r[4];
#pragma unroll
            for (int e = 0; e < 4; ++e) r[e] = rbf(y[e]) + rbf(kv[e] * dd);
            u32x2 w = {cvtpk(r[0], r[1]), cvtpk(r[2], r[3])};
            *(u32x2*)(P.ws + WS_Y2 + ((size_t)(c0 + ch) * SEQ + t) * 2) = w;
        }
    }
}

DI void gate_part(const K1Args& P, int row0, int wid, int lane) {
    const int lc = lane < 56 ? lane : 0;
    u32x4 gw = *(const u32x4*)(P.gate_w + lc * 8);
    float* gp = (float*)(P.ws + WS_G);
    u32x4 xv[8];
#pragma unroll
    for (int r = 0; r < 8; ++r) xv[r] = *(const u32x4*)(P.x + (size_t)(row0 + wid * 8 + r) * DM + lc * 8);
    float mine = 0.f;
#pragma unroll
    for (int r = 0; r < 8; ++r) {
        float s = 0.f;
#pragma unroll
        for (int j = 0; j < 4; ++j) { s += rbf(bf_lo(xv[r][j]) * bf_lo(gw[j])); s += rbf(bf_hi(xv[r][j]) * bf_hi(gw[j])); }
        if (lane >= 56) s = 0.f;
#pragma unroll
        for (int o = 32; o >= 1; o >>= 1) s += __shfl_xor(s, o);
        if (lane == r) mine = s;
    }
    const float z = rbf(mine);
    const float g = rbf(1.0f / (1.0f + __expf(-z)));
    if (lane < 8) gp[row0 + wid * 8 + lane] = g;
}

template <bool VT>
DI void big_tile(const G1& g, const K1Args& P, int pm, int head, int c0, int e, int wid, bf16x8 (&At)[4][2], bf16x8 (&B0)[2][2], Out1& o) {
    f32x4 acc[2][2][4][2];
    zero8(acc[0][0]); zero8(acc[0][1]); zero8(acc[1][0]); zero8(acc[1][1]);
    bf16x8 B1[2][2];
    auto lastside = [&](int s) {
        if (wid < 4) side(g, 14 + s);
        else if (s == 0) {
            const unsigned l = (unsigned)lane_id();
            if (wid == 4) touch(g.sdst, l * 128u, (const char*)P.ssm_k + (size_t)c0 * (SEQ * 2));
            else touch(g.sdst, l * (DM * 2), (const char*)P.wo + ((size_t)((e >> 6) * 1024 + (e & 15) * 64) * DM + ((e >> 4) & 3) * 112) * 2 + (wid - 5) * 110);
        }
    };
    mainpair<VT, 0>(g, 0, acc, At, B0, B1, lastside);
    for (int t = 2; t < 14; t += 2) mainpair<VT, 1>(g, t, acc, At, B0, B1, lastside);
    mainpair<VT, 2>(g, 14, acc, At, B0, B1, lastside);
    TS(P.ws, 0, 3);
    const int lane = lane_id();
    tail_stage(g, P, c0, e, wid, lane);
    if (wid == 7) CODE_TOUCH2(K2_SYM, g.sdst, e & 7, lane);
    SBAR();
    const int wr = wid >> 2, wc = wid & 3, fr = lane & 15, fq = lane >> 4;
    if (VT) {
        o.voff = (unsigned)((pm * 4 + wr) * 32768 + wc * 4096 + (fq >> 1) * 1024 + (fq & 1) * 512 + fr * 16);
        pack_v(acc[0][0], o.pk); pack_v(acc[0][1], o.pk + 4); pack_v(acc[1][0], o.pk + 8); pack_v(acc[1][1], o.pk + 12);
    } else {
        o.voff = kq_off(pm, wr, wc, fr, fq);
        pack_kq(acc[0][0], o.pk, 1.0f); pack_kq(acc[0][1], o.pk + 4, 1.0f); pack_kq(acc[1][0], o.pk + 8, 1.0f); pack_kq(acc[1][1], o.pk + 12, 1.0f);
    }
}

__global__ __launch_bounds__(512, 2) void k1_proj(K1Args P) {
    extern __shared__ __attribute__((aligned(16))) unsigned char shm[];
    ldsp lds = (ldsp)shm;
    const int b = blockIdx.x, xcd = b & 7, j = b >> 3, li = j & 15, pm = li & 7, head = 2 * xcd + (li >> 3), vt = j >> 4;
    const int tid = threadIdx.x, wid = rfl(tid >> 6), lane = tid & 63, wr = wid >> 2, wc = wid & 3, fr = lane & 15, fq = lane >> 4;
    const int c0 = (xcd * 28 + (j < 28 ? j : 0)) * 2, e = xcd * 32 + j;
    TS(P.ws, 0, 0);
    if (wid == 7) CODE_TOUCH2(K1_SYM, (unsigned)(uintptr_t)lds + K1_X + 16384 + 7 * 256, j, lane);
    u32x4 sw = *(const CAS u32x4*)(uintptr_t)(P.shift_k + c0 * 4); unsigned dw = *(const CAS unsigned*)(uintptr_t)(P.ssm_d + c0);
    G1 g;
    const unsigned lds0 = (unsigned)(uintptr_t)lds;
    g.lds = lds; g.ldsw = lds0 + (unsigned)wid * 1024u;
    { int R, C; stage_rc(tid * 16, R, C); g.voffA_kv = (unsigned)(R * DC + C) * 2u; g.voffA_q = (unsigned)(R * DM + C) * 2u; }
    { const int kg = wid >> 1, row = (lane >> 2) & 7, ch = (lane & 3) ^ (2 * (kg & 1)), n = 32 * (2 * (wid & 1) + (lane >> 5)) + 8 * ch;
      g.voffB = (unsigned)((8 * kg + row) * NW_ + n) * 2u; }
    g.aoff = lds_byte(wr * 64 + fr, fq * 8);
    g.boff0 = fq * 2048 + wc * 512 + (fr >> 2) * 64 + (32 * (fq & 1)) + 8 * (fr & 3);
    g.boff1 = g.boff0 ^ 32;
    g.A_kv = (const char*)P.ckv + (size_t)pm * 256 * DC * 2;
    g.A_q = (const char*)P.x + (size_t)pm * 256 * DM * 2;
    g.B_kv = (const char*)(vt ? P.wuv : P.wuk) + head * 512;
    g.B_q = (const char*)P.wq + head * 512 + vt * 256;
    if (wid < 4) {
        g.sbase = (const char*)(wid < 2 ? P.ak : P.av) + (wid & 1) * (64 * DM * 2) + c0 * 2; g.sstep = 128 * DM * 2; g.sv = (unsigned)lane * (DM * 2);
        g.sadd = 0; g.sdst = lds0 + K1_X + (wid >> 1) * 8192 + (wid & 1) * 256; g.sdstep = 512;
    } else {
        if (wid == 4) { g.sbase = g.A_kv + (size_t)((head & 1) * 2 + vt) * 64 * (DC * 2); g.sstep = 128; g.sv = (unsigned)lane * (DC * 2); }
        else if (wid == 5) { g.sbase = g.B_kv + (size_t)pm * 8 * (NW_ * 2); g.sstep = 64 * NW_ * 2; g.sv = (unsigned)((lane >> 2) & 7) * (NW_ * 2) + (lane & 3) * 128; }
        else { g.sbase = (const char*)(wid == 6 ? P.ak : P.av) + (size_t)j * 4 * (DM * 2) + xcd * 112; g.sstep = 128 * DM * 2; g.sv = (unsigned)(lane & 3) * (DM * 2) + ((lane >> 2) & 1) * 108; }
        g.sadd = 4; g.sdst = lds0 + K1_X + 16384 + wid * 256; g.sdstep = 0;
    }
    PIN(g.voffA_kv); PIN(g.voffB); PIN(g.sv); PIN(g.aoff); PIN(g.boff0); PIN(g.boff1);
    stage_units(g, std::make_integer_sequence<int, 8>{});
    WAIT_V(12);
    auto warm = [&](int slot) {
        const unsigned sink = lds0 + K1_X + 16384 + wid * 256;
        if (slot > 0) { if (wid >= 4) side(g, slot - 5); }
        else if (wid < 3) touch(sink, (unsigned)lane * (DM * 2), g.A_q + (size_t)((head & 1) * 2 + vt) * 64 * (DM * 2) + (4 + wid) * 128);
        else if (wid == 3) touch(sink, (unsigned)((lane < 48 ? lane : 0) >> 1) * (NW_ * 2) + (lane & 1) * 128, g.B_q + (size_t)(256 + pm * 24) * (NW_ * 2));
    };
    asm volatile("" : "+s"(sw), "+s"(dw));
    if (b == 0 && tid < N_AFLAGS + N_OFLAGS + N_CFLAGS) {
#pragma unroll
        for (int q = 0; q < 4; ++q) *(u32x4*)((unsigned*)(P.ws + WS_FLAGS) + tid * FLAG_STRIDE + 4 * q) = (u32x4){0u, 0u, 0u, 0u};
    }
    bf16x8 At[4][2], B0[2][2];
    BAR;
    if (wr == 1) BAR;
    TS(P.ws, 0, 1);
    {
        f32x4 q0[4][2], q1[4][2];
        zero8(q0); zero8(q1);
        qphases(g, q0, q1, At, B0, warm, std::make_integer_sequence<int, 14>{});
        stage_unit<27>(g); stage_unit<28>(g); SBAR();
        Out1 o;
        o.rs = __builtin_amdgcn_make_buffer_rsrc(P.ws + WS_Q + (size_t)head * MB, (short)0, (int)MB, 0x00020000);
        o.voff = kq_off(pm, wr, wc, fr, fq) + vt * 8192; o.sbj = 65536; o.shi = 16384;
        pack_kq(q0, o.pk, 0.0625f * 1.4426950408889634f); pack_kq(q1, o.pk + 4, 0.0625f * 1.4426950408889634f);
        st_range<0, 8>(o);
        SBAR();
    }
    TS(P.ws, 0, 2);
    Out1 o;
    if (vt) big_tile<true>(g, P, pm, head, c0, e, wid, At, B0, o); else big_tile<false>(g, P, pm, head, c0, e, wid, At, B0, o);
    o.rs = __builtin_amdgcn_make_buffer_rsrc(P.ws + (vt ? WS_V : WS_K) + (size_t)head * MB, (short)0, (int)MB, 0x00020000);
    o.sbj = vt ? 16384 : 8192; o.shi = vt ? 2048 : 16384;
    SBAR(); st_range<0, 4>(o); SBAR();
    WAIT_V(4);
    if (wr == 0) BAR;
    BAR;
    TS(P.ws, 0, 4);
    const int lane2 = lane_id();
    wo_image(lds, P, e, wid, lane2);
    if (j < 28) conv_part(lds, P, c0, sw, dw, wid, lane2, o);
    else { st_range<4, 16>(o); gate_part(P, (xcd * 4 + (j - 28)) * 64, wid, lane2); }
    TS(P.ws, 0, 7);
}
#undef SA
#undef SB

constexpr int AT_K = 0, AT_V = 65536, AT_LDS = 163840;
constexpr float THR = 8.0f;
constexpr int PD = 6;
constexpr int NEB = 24;
template <int... Is> using iseq = std::integer_sequence<int, Is...>;
template <int N> using make_iseq = std::make_integer_sequence<int, N>;
#define ACLOB "a0","a1","a2","a3","a4","a5","a6","a7","a8","a9","a10","a11","a12","a13","a14","a15","a16","a17","a18","a19","a20","a21","a22","a23","a24","a25","a26","a27","a28","a29","a30","a31","a32","a33","a34","a35","a36","a37","a38","a39","a40","a41","a42","a43","a44","a45","a46","a47","a48","a49","a50","a51","a52","a53","a54","a55","a56","a57","a58","a59","a60","a61","a62","a63","a64","a65","a66","a67","a68","a69","a70","a71","a72","a73","a74","a75","a76","a77","a78","a79","a80","a81","a82","a83","a84","a85","a86","a87","a88","a89","a90","a91","a92","a93","a94","a95","a96","a97","a98","a99","a100","a101","a102","a103","a104","a105","a106","a107","a108","a109","a110","a111","a112","a113","a114","a115","a116","a117","a118","a119","a120","a121","a122","a123","a124","a125","a126","a127","a128","a129","a130","a131","a132","a133","a134","a135","a136","a137","a138","a139","a140","a141","a142","a143","a144","a145","a146","a147","a148","a149","a150","a151","a152","a153","a154","a155","a156","a157","a158","a159","a160","a161","a162","a163","a164","a165","a166","a167","a168","a169","a170","a171","a172","a173","a174","a175","a176","a177","a178","a179","a180","a181","a182","a183","a184","a185","a186","a187","a188","a189","a190","a191","a192","a193","a194","a195","a196","a197","a198","a199","a200","a201","a202","a203","a204","a205","a206","a207","a208","a209","a210","a211","a212","a213","a214","a215","a216","a217","a218","a219","a220","a221","a222","a223","a224","a225","a226","a227","a228","a229","a230","a231","a232","a233","a234","a235","a236","a237","a238","a239","a240","a241","a242","a243","a244","a245","a246","a247","a248","a249","a250","a251","a252","a253","a254","a255"

#define PH(i) do { } while (0)
#define OPMAP 0
constexpr int OP_WST = 128 * 224;
constexpr int op_slot(int s) { switch (s % 5) { case 0: return 0; case 1: return OP_WST; case 2: return 2 * OP_WST; case 3: return 131072; default: return 3 * OP_WST; } }
static_assert(3 * OP_WST <= AT_V + 32768 && 131072 + OP_WST <= AT_LDS && AT_V + 65536 == 131072, "lds");
struct OPJ { const char *wbase[4], *cbase[4]; unsigned cv, lds0; ldscp wp; unsigned char* ws; };
struct AT {
    OPJ o;
    u32x2 pk;
    f32x2 ml2;
    const char *part, *mlp;
    bool isA, early;
    unsigned* flag;
    f32x16 s[2][2];
    f32x16 cm[2];
    u32x4 p[4];
    bf16x8 fr[8];
    float m, l, rs0, rs1, mx0, mx1, alpha, aone;
    bool pend, first, drain;
    ldscp kb;
    ldscp vcur;
    unsigned goff;
    unsigned m0k, m0v;
    const char *gk, *gv;
};

DI void dma_piece(unsigned m0v, unsigned goff, const char* g, const int off) {
    switch (off) {
    case 0: asm volatile("s_mov_b32 m0, %0\n\ts_nop 0\n\tglobal_load_lds_dwordx4 %1, %2" :: "s"(m0v), "v"(goff), "s"(g) : "memory"); break;
    case 1: asm volatile("s_mov_b32 m0, %0\n\ts_nop 0\n\tglobal_load_lds_dwordx4 %1, %2 offset:1024" :: "s"(m0v), "v"(goff), "s"(g) : "memory"); break;
    case 2: asm volatile("s_mov_b32 m0, %0\n\ts_nop 0\n\tglobal_load_lds_dwordx4 %1, %2 offset:2048" :: "s"(m0v), "v"(goff), "s"(g) : "memory"); break;
    default: asm volatile("s_mov_b32 m0, %0\n\ts_nop 0\n\tglobal_load_lds_dwordx4 %1, %2 offset:3072" :: "s"(m0v), "v"(goff), "s"(g) : "memory"); break;
    }
}
template <int I> DI void dma8(unsigned m0base, unsigned goff, const char* g) { dma_piece(m0base + (I >> 2) * 4096, goff, g + (I >> 2) * 4096, I & 3); }
DI void dma_tile(unsigned m0base, unsigned goff, const char* g) {
    dma8<0>(m0base, goff, g); dma8<1>(m0base, goff, g); dma8<2>(m0base, goff, g); dma8<3>(m0base, goff, g);
    dma8<4>(m0base, goff, g); dma8<5>(m0base, goff, g); dma8<6>(m0base, goff, g); dma8<7>(m0base, goff, g);
}
DI float ex2(float x) { return __builtin_amdgcn_exp2f(x); }
DI float MX3(float a, float b, float c) { float r; asm("v_max3_f32 %0, %1, %2, %3" : "=v"(r) : "v"(a), "v"(b), "v"(c)); return r; }
DI float MX2(float a, float b) { float r; asm("v_max_f32_e32 %0, %1, %2" : "=v"(r) : "v"(a), "v"(b)); return r; }
template <int D0> DI void qk_init(f32x16& s, const bf16x8& k, const f32x16& c) {
    asm volatile("v_mfma_f32_32x32x16_bf16 %0, %1, a[%c3:%c4], %2" : "=&v"(s) : "v"(k), "v"(c), "i"(128 + 4 * D0), "i"(131 + 4 * D0)); }
template <int D0> DI void qk_acc(f32x16& s, const bf16x8& k) {
    asm volatile("v_mfma_f32_32x32x16_bf16 %0, %1, a[%c2:%c3], %0" : "+v"(s) : "v"(k), "i"(128 + 4 * D0), "i"(131 + 4 * D0)); }
template <int DB> DI void pv_acc(const bf16x8& v, const u32x4& p) {
    asm volatile("v_mfma_f32_32x32x16_bf16 a[%c2:%c3], %0, %1, a[%c2:%c3]" :: "v"(v), "v"(p), "i"(16 * DB), "i"(16 * DB + 15)); }
DI void add_bcast(f32x16& x, float aone, float b) { asm volatile("s_nop 1\n\tv_mfma_f32_32x32x2_f32 %0, %1, %2, %0" : "+v"(x) : "v"(aone), "v"(b)); }
#define NOPS16() asm volatile("s_nop 7\n\ts_nop 7" ::: "memory")
#define NOPS2() asm volatile("s_nop 1" ::: "memory")
template <int I> DI void a_zero() { asm volatile("v_accvgpr_write_b32 a[%c0], 0" :: "i"(I)); }
template <int I> DI void a_scale(float f) { float t; asm volatile("v_accvgpr_read_b32 %0, a[%c2]\n\tv_mul_f32 %0, %1, %0\n\tv_accvgpr_write_b32 a[%c2], %0" : "=&v"(t) : "v"(f), "i"(I)); }
template <int I> DI void a_qmov() { asm volatile("v_accvgpr_mov_b32 a[%c0], a[%c1]" :: "i"(128 + I), "i"(192 + I)); }
template <int I> DI float a_read() { float t; asm volatile("v_accvgpr_read_b32 %0, a[%c1]" : "=v"(t) : "i"(I)); return t; }
template <int F> DI void q_load(unsigned voff, const char* g) {
    asm volatile("global_load_dwordx4 a[%c2:%c3], %0, %1 offset:%c4" :: "v"(voff), "s"(g + ((F & 15) >> 2) * 4096), "i"(128 + 4 * F), "i"(131 + 4 * F), "i"((F & 3) * 1024) : "memory"); }
template <int K> DI void pk_store(const AT& c, const u32x4 pk) {
    asm volatile("global_store_dwordx4 %0, %1, %2 offset:%c3 sc1\n\ts_nop 1" :: "v"(c.o.cv), "v"(pk), "s"(c.part + (K >> 2) * 4096), "i"((K & 3) * 1024) : "memory"); }
template <int K> DI void p_load(const AT& c) {
    asm volatile("global_load_dwordx4 a[%c2:%c3], %0, %1 offset:%c4 sc1" :: "v"(c.o.cv), "s"(c.part + (K >> 2) * 4096), "i"(192 + 4 * K), "i"(195 + 4 * K), "i"((K & 3) * 1024) : "memory"); }
DI void ml_load(AT& c) { asm volatile("global_load_dwordx2 %0, %1, %2 sc1" : "+v"(c.ml2) : "v"((c.o.cv & 496u) >> 1), "s"(c.mlp) : "memory"); }
template <int I> DI void op_wpiece(const OPJ& o) {
    constexpr int S = I / 7, P = I % 7, H = P >= 4 ? 4096 : 0;
    dma_piece(o.lds0 + op_slot(S) + H, o.cv, o.wbase[S >> 1] + (S & 1) * OP_WST + H, P & 3);
}
template <int DB> DI void pv_new(const bf16x8& v, const u32x4& p) {
    asm volatile("v_mfma_f32_32x32x16_bf16 a[%c2:%c3], %0, %1, 0" :: "v"(v), "v"(p), "i"(16 * DB), "i"(16 * DB + 15)); }
template <int... Is> DI void a_zeros(iseq<Is...>) { (a_zero<Is>(), ...); }
template <int... Is> DI void a_scales(float f, iseq<Is...>) { (a_scale<Is>(f), ...); }
template <int... Is> DI void a_qmovs(iseq<Is...>) { (a_qmov<Is>(), ...); }
template <int B, int... Is> DI void q_loads(unsigned voff, const char* g, iseq<Is...>) { (q_load<B + Is>(voff, g), ...); }
template <int... Is> DI void p_loads(const AT& c, iseq<Is...>) { (p_load<Is>(c), ...); }
template <int B, int... Is> DI void a_reads(float (&o)[16], iseq<Is...>) { ((o[Is] = a_read<B + Is>()), ...); }

template <int KS, int R> DI bf16x8 kfrag(const AT& c) { return ldsrd16(c.kb + KS * 32768 + ((R & 1) * 16 + (R >> 1)) * 1024); }
template <int R, bool DBM = false> DI bf16x8 vfrag(ldscp v) {
    return ldsrd16(v + (DBM ? (R >> 2) * 4096 + (R & 3) * 1024 : (R & 7) * 4096 + (R >> 3) * 1024)); }
template <int Q> DI void pk_quad(AT& c) {
    const float t0 = a_read<4 * Q>(), t1 = a_read<4 * Q + 1>(), t2 = a_read<4 * Q + 2>(), t3 = a_read<4 * Q + 3>();
    if constexpr (Q & 1) pk_store<(Q >> 1)>(c, (u32x4){c.pk[0], c.pk[1], cvtpk(t0, t1), cvtpk(t2, t3)});
    else { c.pk = (u32x2){cvtpk(t0, t1), cvtpk(t2, t3)}; PIN(c.pk); }
}
template <int X, int E> DI void exp_sum(AT& c) {
    const float e = ex2(c.s[X][E >> 4][E & 15]); c.s[X][E >> 4][E & 15] = e;
    if constexpr (E & 1) c.rs1 += e; else c.rs0 += e;
}
template <int X, int W> DI void pack1(AT& c) {
    c.p[W >> 2][W & 3] = cvtpk(c.s[X][W >> 3][(2 * W) & 15], c.s[X][W >> 3][(2 * W + 1) & 15]);
}
template <int X, int MODE, int N> DI void gapA(AT& c) {
    constexpr int d0 = N >> 1, kb = N & 1, Y = X ^ 1;
    if constexpr (d0 == 0) qk_init<0>(c.s[Y][kb], c.fr[N & 7], c.cm[(MODE & 4) ? kb : 0]);
    else qk_acc<d0>(c.s[Y][kb], c.fr[N & 7]);
    constexpr int R = N + PD;
    if constexpr (R < 32) c.fr[R & 7] = kfrag<Y, R>(c); else if constexpr (MODE & 1) c.fr[R & 7] = vfrag<R - 32, (MODE & 18) == 18>(c.vcur);
    if constexpr (MODE & 1) {
        if constexpr (N < 32 - NEB) { exp_sum<X, NEB + N>(c); PIN(c.rs0); PIN(c.rs1); }
        if constexpr (N >= 10 && N < 26) { pack1<X, N - 10>(c); PIN(c.p[(N - 10) >> 2]); }
    }
    if constexpr ((MODE & 4) && N == 2) asm volatile("" :: "v"(c.cm[0]), "v"(c.cm[1]));
    if constexpr ((MODE & 18) == 16) pk_quad<N>(c);
    if constexpr ((MODE & 18) == 18 && N >= 24) pk_quad<N - 24>(c);
    if constexpr ((MODE & 2) && (N & 1)) {
        if constexpr (N < 16) dma8<(N >> 1)>(c.m0k, c.goff, c.gk); else dma8<((N - 16) >> 1)>(c.m0v, c.goff, c.gv);
    }
    SBAR();
}
template <int X> DI void decide(AT& c) {
    constexpr int Y = X ^ 1;
    float mm = MX2(c.mx0, c.mx1);
    auto rr = __builtin_amdgcn_permlane32_swap(__float_as_uint(mm), __float_as_uint(mm), false, false);
    const float rm = MX2(__uint_as_float(rr[0]), __uint_as_float(rr[1]));
    if (__builtin_expect(c.first || __any(rm > THR), 0)) {
        const float dl = c.first ? rm : MX2(rm, 0.f);
        c.m += dl; c.alpha = ex2(-dl); c.pend = !c.first;
        const float nd = -dl;
        add_bcast(c.s[Y][0], c.aone, nd); add_bcast(c.s[Y][1], c.aone, nd); add_bcast(c.cm[0], c.aone, nd);
        asm volatile("s_nop 7\n\ts_nop 7\n\ts_nop 3" : "+v"(c.s[Y][0]), "+v"(c.s[Y][1]), "+v"(c.cm[0]));
    }
}
template <int X, int I> DI void maxstep(AT& c) {
    constexpr int Y = X ^ 1, h = I >> 2, r = 4 * (I & 3);
    if constexpr (I == 0) { c.mx0 = MX2(c.s[Y][0][0], c.s[Y][0][1]); c.mx1 = MX2(c.s[Y][0][2], c.s[Y][0][3]); }
    else { c.mx0 = MX3(c.mx0, c.s[Y][h][r], c.s[Y][h][r + 1]); c.mx1 = MX3(c.mx1, c.s[Y][h][r + 2], c.s[Y][h][r + 3]); }
}
template <int X, int MODE, int N> DI void gapB(AT& c) {
    constexpr bool DBM = (MODE & 18) == 18;
    constexpr int ks = DBM ? N & 3 : N >> 3, db = DBM ? N >> 2 : N & 7, Y = X ^ 1;
    if constexpr ((MODE & 16) && ks == 0) pv_new<db>(c.fr[N & 7], c.p[ks]); else pv_acc<db>(c.fr[N & 7], c.p[ks]);
    if constexpr (DBM && N < 24) pk_quad<N + 8>(c);
    if constexpr ((MODE & 32) && N == 10) {
        if (c.isA) {
            asm volatile("s_waitcnt vmcnt(0)" ::: "memory");
            c.early = rfl(__float_as_uint(a_read<192>())) != 0u;
            __builtin_amdgcn_fence(__ATOMIC_ACQUIRE, "wavefront");
            if (c.early) ml_load(c);
        }
    }
    if constexpr ((MODE & 32) && N > 10 && N < 27) { if (c.early) p_load<N - 11>(c); }
    if constexpr ((MODE & 48) == 48 && N == 16) { asm volatile("s_waitcnt vmcnt(0)" ::: "memory"); __hip_atomic_store(c.flag, 1u, RLX_AGENT); }
    if constexpr ((MODE & 48) == 32 && N == 28) asm volatile("global_load_dword a128, %0, %1 sc1" :: "v"(0u), "s"(c.flag) : "memory");
    if constexpr ((MODE & 64) && N < 28) op_wpiece<N>(c.o);
    constexpr int R = N + PD;
    if constexpr (R < 32) c.fr[R & 7] = vfrag<R, DBM>(c.vcur);
    if constexpr (MODE & 1) {
        if constexpr (MODE & 2) {
            if constexpr (N == 28) { c.fr[0] = kfrag<X, 0>(c); c.fr[1] = kfrag<X, 1>(c); }
            if constexpr (N == 29) { c.fr[2] = kfrag<X, 2>(c); c.fr[3] = kfrag<X, 3>(c); }
            if constexpr (N == 30) { c.fr[4] = kfrag<X, 4>(c); }
            if constexpr (N == 31) { c.fr[5] = kfrag<X, 5>(c); }
        }
        if constexpr (N == 1) { asm volatile("s_nop 7" : "+v"(c.s[Y][0]), "+v"(c.s[Y][1])); }
        if constexpr (N >= 2 && N < 6) { maxstep<X, 2 * (N - 2)>(c); maxstep<X, 2 * (N - 2) + 1>(c); PIN(c.mx0); PIN(c.mx1); }
        if constexpr (N == 6) { c.l += c.rs0 + c.rs1; c.rs0 = 0.f; c.rs1 = 0.f; PIN(c.l); }
        if constexpr (N == 7) decide<X>(c);
        if constexpr (N >= 8) { exp_sum<Y, N - 8>(c); PIN(c.rs0); PIN(c.rs1); }
    }
    SBAR();
    if constexpr (N == 8 && MODE == 3) PH(6 * X + 2);
    if constexpr ((MODE & 1) && N == 27) {
        if constexpr (MODE == 3) PH(6 * X + 3);
        if constexpr (MODE & 2) {
            if (X == 0 && c.drain) asm volatile("s_waitcnt vmcnt(0)" ::: "memory");
            if constexpr (MODE & 16) asm volatile("s_waitcnt vmcnt(24) lgkmcnt(0)\n\ts_barrier" ::: "memory");
            else asm volatile("s_waitcnt vmcnt(8) lgkmcnt(0)\n\ts_barrier" ::: "memory");
        } else {
            if (!c.isA) asm volatile("s_waitcnt vmcnt(0)" ::: "memory");
            asm volatile("s_waitcnt lgkmcnt(0)\n\ts_barrier" ::: "memory");
        }
        if constexpr (MODE == 3) PH(6 * X + 4);
        SBAR();
    }
}
template <int X, int MODE, int... Ns> DI void phaseA(AT& c, iseq<Ns...>) { (gapA<X, MODE, Ns>(c), ...); }
template <int X, int MODE, int... Ns> DI void phaseB(AT& c, iseq<Ns...>) { (gapB<X, MODE, Ns>(c), ...); }
template <int X, int B, int... Es> DI void exps_from(AT& c, iseq<Es...>) { (exp_sum<X, B + Es>(c), ...); }
template <int X, int... Ws> DI void packs(AT& c, iseq<Ws...>) { (pack1<X, Ws>(c), ...); }
template <int... Is> DI void maxsteps1(AT& c, iseq<Is...>) { (maxstep<1, Is>(c), ...); }
DI void rescale_o(AT& c) {
    if (__builtin_expect(c.pend, 0)) {
        NOPS16();
        a_scales(c.alpha, make_iseq<128>{});
        NOPS2();
        c.l *= c.alpha; c.pend = false;
    }
}
DI float halfsum(float x) { auto rr = __builtin_amdgcn_permlane32_swap(__float_as_uint(x), __float_as_uint(x), false, false); return __uint_as_float(rr[0]) + __uint_as_float(rr[1]); }
DI void build_mask(AT& c, int mrel) {
    const float NEG = -__builtin_inff(), nm = -c.m;
#pragma unroll
    for (int r = 0; r < 16; ++r) { const int kk = (r & 3) + 8 * (r >> 2); c.cm[0][r] = kk > mrel ? NEG : nm; c.cm[1][r] = kk + 32 > mrel ? NEG : nm; }
    PIN(c.cm[0]); PIN(c.cm[1]); NOPS2();
}

struct ATW {
    const char *Kh, *Vh; ldsp lds; unsigned mk, mv; int n1, vs, lane, wid, r32, hi, qrel; bool isA;
};
DI int tile_of(const ATW& w, int g) { return w.isA ? w.n1 + g : (g < w.n1 ? g : g - w.n1); }
template <int X, bool DMA> DI void setup(AT& c, const ATW& w, int g) {
    c.vcur = w.lds + AT_V + w.vs * 32768 + w.lane * 16;
    if constexpr (DMA) {
        const int v2 = w.vs == 0 ? 2 : w.vs - 1, tn = tile_of(w, g + 2);
        c.gk = w.Kh + (size_t)tn * 32768; c.gv = w.Vh + (size_t)tn * 32768; c.m0k = w.mk + X * 32768; c.m0v = w.mv + v2 * 32768;
    }
}
template <bool LAST, bool seam> DI void pair(AT& c, ATW& w, int g0) {
    setup<0, true>(c, w, g0);
    c.first = seam;
    const float m_old = c.m;
    if constexpr (seam) { a_qmovs(make_iseq<64>{}); NOPS2(); }
    int qr = w.qrel;
    if constexpr (LAST) {
        if constexpr (seam) asm volatile("; last pair, two segments" : "+v"(qr), "+v"(c.s[0][0]), "+v"(c.s[0][1]), "+v"(c.m));
        else asm volatile("; last pair" : "+v"(qr), "+v"(c.s[0][0]), "+v"(c.s[0][1]), "+v"(c.m));
        build_mask(c, qr);
    }
    SBAR();
    if constexpr (!LAST && !seam) PH(0);
    phaseA<0, LAST ? 7 : 3>(c, make_iseq<32>{});
    if constexpr (!LAST && !seam) PH(1);
    phaseB<0, LAST || seam ? 11 : 3>(c, make_iseq<32>{});
    if constexpr (!LAST && !seam) PH(5);
    rescale_o(c);
    if (c.drain) { __hip_atomic_store(c.flag, 1u, RLX_AGENT); c.drain = false; }
    c.first = false;
    w.vs = w.vs == 2 ? 0 : w.vs + 1;
    setup<1, !LAST>(c, w, g0 + 1);
    if constexpr (LAST || seam) asm volatile("" : "+s"(c.part));
    if constexpr (LAST) {
        asm volatile("global_load_dword a192, %0, %1 sc1" :: "v"(0u), "s"(c.flag) : "memory");
        PIN(qr); build_mask(c, qr - 64);
    }
    if constexpr (seam) {
        const float lt = halfsum(c.l);
        __hip_atomic_store((unsigned long long*)c.mlp + w.r32, ((unsigned long long)__float_as_uint(lt) << 32) | __float_as_uint(m_old), RLX_AGENT);
        c.l = 0.f;
        asm volatile("; second segment" : "+v"(c.s[1][0]), "+v"(c.s[1][1]), "+v"(c.rs0), "+v"(c.rs1), "+v"(c.l));
        SBAR();
        phaseA<1, (LAST ? 5 : 3) | 16>(c, make_iseq<32>{});
        phaseB<1, (LAST ? 33 : 11) | 16>(c, make_iseq<32>{});
        if constexpr (!LAST) c.drain = true;
    } else {
        SBAR();
        if constexpr (!LAST) PH(6);
        phaseA<1, LAST ? 5 : 3>(c, make_iseq<32>{});
        if constexpr (!LAST) PH(7);
        phaseB<1, LAST ? 33 : 3>(c, make_iseq<32>{});
        if constexpr (!LAST) PH(11);
    }
    rescale_o(c);
    w.vs = w.vs == 2 ? 0 : w.vs + 1;
}

template <int S> DI void op_wstage(const OPJ& o) {
    op_wpiece<7 * S>(o); op_wpiece<7 * S + 1>(o); op_wpiece<7 * S + 2>(o); op_wpiece<7 * S + 3>(o); op_wpiece<7 * S + 4>(o); op_wpiece<7 * S + 5>(o); op_wpiece<7 * S + 6>(o);
}
template <int F> DI void op_cload(const OPJ& o) {
    asm volatile("global_load_dwordx4 a[%c2:%c3], %0, %1 offset:%c4 sc1" :: "v"(o.cv), "s"(o.cbase[F >> 4] + ((F >> 1) & 3) * 131072 + ((F >> 3) & 1) * 4096),
                 "i"(4 * F), "i"(4 * F + 3), "i"((F & 1) * 2048) : "memory"); }
template <int B, int... Is> DI void op_cloads(const OPJ& o, iseq<Is...>) { (op_cload<B + Is>(o), ...); }
template <int F> DI void op_mfma(f32x4& acc, const bf16x8& wf) {
    asm volatile("v_mfma_f32_16x16x32_bf16 %0, %1, a[%c2:%c3], %0" : "+v"(acc) : "v"(wf), "i"(4 * F), "i"(4 * F + 3)); }
constexpr int OP_PD = 4;
template <int I> DI bf16x8 op_wfrag(const OPJ& o) { return ldsrd16(o.wp + op_slot(I / 14) + ((I % 14) / 7) * 14336 + (I % 7) * 1024); }
template <int I, class E> DI void op_step(const OPJ& o, f32x4 (&acc)[28], bf16x8 (&wf)[8], E& epi) {
    constexpr int S = I / 14, jj = (I % 14) / 7, cb = I % 7;
    if constexpr (I % 14 == 0) {
        if constexpr (S == 0) asm volatile("s_waitcnt vmcnt(16)\n\ts_barrier" ::: "memory");
        else if constexpr (S == 1) asm volatile("s_waitcnt vmcnt(23)\n\ts_barrier" ::: "memory");
        else if constexpr (S < 5) asm volatile("s_waitcnt vmcnt(30)\n\ts_barrier" ::: "memory");
        else if constexpr (S == 5) asm volatile("s_waitcnt vmcnt(23)\n\ts_barrier" ::: "memory");
        else if constexpr (S == 6) asm volatile("s_waitcnt vmcnt(8)\n\ts_barrier" ::: "memory");
        else { asm volatile("s_waitcnt vmcnt(0)" ::: "memory"); epi(); }
        SBAR();
    }
    op_mfma<S * 8 + jj>(acc[cb], wf[I & 7]);
    if constexpr (I + OP_PD < 112) wf[(I + OP_PD) & 7] = op_wfrag<I + OP_PD>(o);
    op_mfma<S * 8 + 2 + jj>(acc[7 + cb], wf[I & 7]);
    if constexpr (S <= 4 && I % 14 < 8) op_cload<8 * (S + 3) + I % 14>(o);
    op_mfma<S * 8 + 4 + jj>(acc[14 + cb], wf[I & 7]);
    if constexpr (S <= 3 && I % 14 >= 7) op_wpiece<7 * (S + 4) + I % 14 - 7>(o);
    op_mfma<S * 8 + 6 + jj>(acc[21 + cb], wf[I & 7]);
    SBAR();
}
template <class E, int... Is> DI void op_steps(const OPJ& o, f32x4 (&acc)[28], bf16x8 (&wf)[8], E& epi, iseq<Is...>) { (op_step<Is>(o, acc, wf, epi), ...); }

template <int Q> DI void op_finish(const f32x4 (&acc)[14], const __amdgpu_buffer_rsrc_t srs, int loff, const u32x2 (&xw)[4], const u32x2 (&yw)[4], float g,
                                   bf16_t* outp) {
    constexpr int F0 = (Q >> 1) * 7 + (Q & 1) * 4, NF = 4 - (Q & 1);
    f32x4 pt[4][NF];
#pragma unroll
    for (int q = 0; q < 4; ++q)
#pragma unroll
        for (int f = 0; f < NF; ++f)
            if (q != Q) pt[q][f] = __builtin_bit_cast(f32x4, __builtin_amdgcn_raw_buffer_load_b128(srs, loff + q * 57344 + (F0 + f) * 1024, 0, 16));
    const float og = rbf(1.0f - g);
#pragma unroll
    for (int f = 0; f < NF; ++f) {
        pt[Q][f] = acc[F0 + f];
        const f32x4 sum = ((pt[0][f] + pt[1][f]) + pt[2][f]) + pt[3][f];
        const float xv[4] = {bf_lo(xw[f][0]), bf_hi(xw[f][0]), bf_lo(xw[f][1]), bf_hi(xw[f][1])};
        const float yv[4] = {bf_lo(yw[f][0]), bf_hi(yw[f][0]), bf_lo(yw[f][1]), bf_hi(yw[f][1])};
        float r[4];
#pragma unroll
        for (int e = 0; e < 4; ++e) r[e] = rbf(g * rbf(xv[e] * yv[e])) + rbf(og * rbf(sum[e]));
        u32x2 w = {cvtpk(r[0], r[1]), cvtpk(r[2], r[3])};
        *(u32x2*)(outp + f * 16) = w;
    }
}
template <int Q> DI void op_publish(const f32x4 (&acc)[14], const __amdgpu_buffer_rsrc_t srs, int off) {
    constexpr int F0 = (Q >> 1) * 7 + (Q & 1) * 4, NF = 4 - (Q & 1);
#pragma unroll
    for (int f = 0; f < 14; ++f)
        if (f < F0 || f >= F0 + NF) __builtin_amdgcn_raw_buffer_store_b128(__builtin_bit_cast(u32x4, acc[f]), srs, off + f * 1024, 0, 16);
}
template <bool MERGE, int D> DI void ctx_out(float f1, float f2, const __amdgpu_buffer_rsrc_t crs, int voff) {
    float o[16], p[16];
    a_reads<16 * D>(o, make_iseq<16>{});
    if constexpr (MERGE) a_reads<192 + 8 * D>(p, make_iseq<8>{});
#pragma unroll
    for (int s2 = 0; s2 < 2; ++s2) {
        u32x4 wv;
#pragma unroll
        for (int e = 0; e < 4; ++e) { const int r = 8 * s2 + 2 * e;
            if constexpr (MERGE) { const unsigned pw = __float_as_uint(p[r >> 1]); wv[e] = cvtpk(o[r] * f1 + bf_lo(pw) * f2, o[r + 1] * f1 + bf_hi(pw) * f2); }
            else wv[e] = cvtpk(o[r] * f1, o[r + 1] * f1); }
        __builtin_amdgcn_raw_buffer_store_b128(wv, crs, voff + D * 1024 + s2 * 512, 0, 16);
    }
}
template <bool MERGE, int... Ds> DI void ctx_outs(float f1, float f2, const __amdgpu_buffer_rsrc_t crs, int voff, iseq<Ds...>) { (ctx_out<MERGE, Ds>(f1, f2, crs, voff), ...); }
DI void poll4(const unsigned* fl, int lane) {
    unsigned spins = 0;
    while (true) {
        const unsigned v = __hip_atomic_load(fl + (lane & 3), RLX_AGENT);
        if (__all(v != 0u)) break;
        __builtin_amdgcn_s_sleep(1);
        if (++spins > (1u << 22)) break;
    }
    __builtin_amdgcn_fence(__ATOMIC_ACQUIRE, "wavefront");
}

__global__ __launch_bounds__(256, 1) void k2_attn(unsigned char* ws, const bf16_t* x, bf16_t* out) {
    extern __shared__ __attribute__((aligned(16))) unsigned char shm[];
    ldsp lds = (ldsp)shm;
    const int tid = threadIdx.x, wid = rfl(tid >> 6), lane = tid & 63, r32 = lane & 31, hi = lane >> 5;
    const int b = blockIdx.x, xcd = b & 7, j = b >> 3, head = 2 * xcd + (j >> 4), w16 = j & 15;
    const bool isA = w16 >= 8; const int i = w16 & 7, bq = 15 - i;
    const int seg = head * 8 + i;
    ATW w;
    w.Kh = (const char*)ws + WS_K + (size_t)head * MB; w.Vh = (const char*)ws + WS_V + (size_t)head * MB;
    const char* Qh = (const char*)ws + WS_Q + (size_t)head * MB;
    w.lds = lds; w.n1 = 15 - 2 * i; w.vs = 0; w.lane = lane; w.wid = wid; w.r32 = r32; w.hi = hi; w.isA = isA; w.qrel = wid * 32 + r32 - 4 * hi;
    AT c;
    const unsigned lds0 = (unsigned)(uintptr_t)lds;
    c.part = (const char*)ws + WS_PART + (size_t)(seg * 4 + wid) * 16384; c.mlp = (const char*)ws + WS_ML + (seg * 4 + wid) * 256;
    c.flag = (unsigned*)(ws + WS_FLAGS) + seg * FLAG_STRIDE + wid;
    c.isA = isA; c.early = false; c.ml2 = (f32x2){0.f, 0.f};
    OPJ& o = c.o;
    o.cv = (unsigned)lane * 16u;
    c.goff = (unsigned)wid * 8192u + (unsigned)lane * 16u;
    c.kb = lds + AT_K + lane * 16;
    w.mk = lds0 + AT_K + wid * 8192; w.mv = lds0 + AT_V + wid * 8192;
    TS(ws, 1, 0);
    asm volatile("s_nop 4" ::: ACLOB);
    dma_tile(w.mk, c.goff, w.Kh + (size_t)tile_of(w, 0) * 32768);
    q_loads<0>((unsigned)lane * 16u, Qh + (size_t)(bq * 4 + wid) * 16384, make_iseq<16>{});
    dma_tile(w.mk + 32768, c.goff, w.Kh + (size_t)tile_of(w, 1) * 32768);
    dma_tile(w.mv, c.goff, w.Vh + (size_t)tile_of(w, 0) * 32768);
    if (wid == 3) CODE_TOUCH2(K2_SYM, lds0 + AT_V + 65536, j & 7, lane);
    const bool q2now = !isA && w.n1 <= 3;
    const char* q2p = Qh + (size_t)(i * 4 + wid) * 16384;
    if (q2now) q_loads<16>((unsigned)lane * 16u, q2p, make_iseq<16>{});
    a_zeros(make_iseq<128>{});
    c.cm[0] = f32x16{}; c.cm[1] = f32x16{}; c.m = 0.f; c.l = 0.f; c.rs0 = 0.f; c.rs1 = 0.f; c.alpha = 1.f; c.pend = false; c.drain = false;
    c.aone = hi ? 0.f : 1.f;
    PIN(c.cm[0]);
    if (q2now) asm volatile("s_waitcnt vmcnt(32)" ::: "memory"); else asm volatile("s_waitcnt vmcnt(16)" ::: "memory");
    asm volatile("s_barrier" ::: "memory");
    SBAR();
    c.fr[0] = kfrag<0, 0>(c); c.fr[1] = kfrag<0, 1>(c); c.fr[2] = kfrag<0, 2>(c); c.fr[3] = kfrag<0, 3>(c); c.fr[4] = kfrag<0, 4>(c); c.fr[5] = kfrag<0, 5>(c);
    SBAR();
    phaseA<1, 0>(c, make_iseq<32>{});
    dma_tile(w.mv + 32768, c.goff, w.Vh + (size_t)tile_of(w, 1) * 32768);
    SBAR(); asm volatile("s_nop 7\n\ts_nop 7" : "+v"(c.s[0][0]), "+v"(c.s[0][1])); SBAR();
    c.first = true;
    maxsteps1(c, make_iseq<8>{});
    decide<1>(c);
    exps_from<0, 0>(c, make_iseq<NEB>{});
    asm volatile("s_waitcnt vmcnt(8) lgkmcnt(0)\n\ts_barrier" ::: "memory");
    SBAR();
    c.fr[0] = kfrag<1, 0>(c); c.fr[1] = kfrag<1, 1>(c); c.fr[2] = kfrag<1, 2>(c); c.fr[3] = kfrag<1, 3>(c); c.fr[4] = kfrag<1, 4>(c); c.fr[5] = kfrag<1, 5>(c);
    TS(ws, 1, 1);
    const int q2at = isA || q2now ? -1 : 2;
    for (int g0 = 0, gstop = isA ? 14 : w.n1 - 1; ; g0 += 2, gstop = 14) {
        for (; g0 < gstop; g0 += 2) {
            if (g0 == q2at) q_loads<16>(c.o.cv, q2p, make_iseq<16>{});
            pair<false, false>(c, w, g0);
        }
        if (g0 >= 14) break;
        pair<false, true>(c, w, g0);
    }
    TS(ws, 1, 2);
    if (!isA && w.n1 == 15) pair<true, true>(c, w, 14); else pair<true, false>(c, w, 14);
    TS(ws, 1, 3);
    int b2 = b; asm volatile("" : "+s"(b2), "+s"(ws));
    const int sk = b2 & 3, rb = 8 * ((b2 >> 2) & 1) + (b2 >> 5), cn = (b2 >> 3) & 3, hd2 = sk * 4 + cn;
    const int grp = rb * 4 + cn, fr = lane & 15, fq = lane >> 4;
#pragma unroll
    for (int q = 0; q < 4; ++q) {
        const int hq = OPMAP ? (cn + q) & 3 : q;
        o.wbase[q] = (const char*)ws + WS_WI + (size_t)(hd2 * 8 + hq * 2) * OP_WST + wid * 7168;
        o.cbase[q] = (const char*)ws + WS_CTX + ((size_t)(rb * 8 + 4 * (wid >> 1)) * 128 + sk * 32 + hq * 8 + (wid & 1)) * 1024;
    }
    o.lds0 = lds0 + wid * 7168; o.wp = lds + lane * 16 + (wid & 1) * 7168; o.ws = ws;
    c.vcur = lds + AT_V + 32768 + lane * 16;
    c.fr[0] = vfrag<0>(c.vcur); c.fr[1] = vfrag<1>(c.vcur); c.fr[2] = vfrag<2>(c.vcur); c.fr[3] = vfrag<3>(c.vcur); c.fr[4] = vfrag<4>(c.vcur); c.fr[5] = vfrag<5>(c.vcur);
    SBAR();
    exps_from<0, NEB>(c, make_iseq<32 - NEB>{});
    packs<0>(c, make_iseq<16>{});
    c.l += c.rs0 + c.rs1;
    PIN(c.p[0]); PIN(c.p[1]); PIN(c.p[2]); PIN(c.p[3]);
    SBAR();
    if (isA && !c.early) {
        asm volatile("s_waitcnt vmcnt(0)" ::: "memory");
        c.early = rfl(__float_as_uint(a_read<128>())) != 0u;
        __builtin_amdgcn_fence(__ATOMIC_ACQUIRE, "wavefront");
        if (c.early) { ml_load(c); p_loads(c, make_iseq<16>{}); }
    }
    SBAR(); NOPS2(); SBAR();
    phaseB<0, 64>(c, make_iseq<32>{});
    SBAR();
    if (isA && !c.early) {
        unsigned spins = 0;
        while (rfl(__hip_atomic_load(c.flag, RLX_AGENT)) == 0u) { __builtin_amdgcn_s_sleep(1); if (++spins > (1u << 22)) break; }
        __builtin_amdgcn_fence(__ATOMIC_ACQUIRE, "wavefront");
        ml_load(c);
        p_loads(c, make_iseq<16>{});
        asm volatile("s_waitcnt vmcnt(0)" : "+v"(c.ml2) :: "memory");
    }
    const float lt = halfsum(c.l);
    TS(ws, 1, 4);
    {
        const __amdgpu_buffer_rsrc_t crs = __builtin_amdgcn_make_buffer_rsrc(ws + WS_CTX, (short)0, (int)(16 * MB), 0x00020000);
        const int row = (isA ? bq : i) * 128 + wid * 32 + r32;
        const int voff = ((row >> 4) * 128 + head * 8) * 1024 + hi * 256 + (row & 15) * 16;
        if (isA) {
            asm volatile("s_waitcnt vmcnt(28)" : "+v"(c.ml2) :: "memory");
            const float mn = __builtin_fmaxf(c.m, c.ml2[0]);
            const float w1 = ex2(c.m - mn), w2 = ex2(c.ml2[0] - mn);
            const float inv = 1.0f / (lt * w1 + c.ml2[1] * w2);
            SBAR(); NOPS16(); SBAR();
            ctx_outs<true>(w1 * inv, w2 * inv, crs, voff, make_iseq<8>{});
        } else {
            const float inv = 1.0f / lt;
            SBAR(); NOPS16(); SBAR();
            ctx_outs<false>(inv, 0.f, crs, voff, make_iseq<8>{});
        }
    }
    TS(ws, 1, 5);
    unsigned* flags = (unsigned*)(ws + WS_FLAGS);
    f32x4 ac2[28];
#pragma unroll
    for (int f = 0; f < 28; ++f) { ac2[f] = (f32x4){0.f, 0.f, 0.f, 0.f}; PIN(ac2[f]); }
    asm volatile("s_waitcnt vmcnt(0)\n\ts_barrier" ::: "memory");
    if (tid == 0) __hip_atomic_store(flags + (N_AFLAGS + N_OFLAGS + (head >> 2) * 16 + (isA ? bq : i)) * FLAG_STRIDE + (head & 3), 1u, RLX_AGENT);
    TS(ws, 1, 7);
    TS(ws, 2, 0);
    const int trow = rb * 128 + wid * 32 + (sk >> 1) * 16 + fr, tcol = cn * 112 + (sk & 1) * 64 + 4 * fq;
    u32x2 xw[4], yw[4]; float gate;
    auto epi = [&]() {
        const bf16_t* y2p = (const bf16_t*)(ws + WS_Y2);
#pragma unroll
        for (int f = 0; f < 4; ++f) {
            const int cc = tcol + ((f == 3 && (sk & 1)) ? 0 : f * 16);
            xw[f] = *(const u32x2*)(x + (size_t)trow * DM + cc);
            const bf16_t* yp = y2p + (size_t)cc * SEQ + trow;
            yw[f] = (u32x2){yp[0] | (unsigned)yp[SEQ] << 16, yp[2 * SEQ] | (unsigned)yp[3 * SEQ] << 16};
        }
        gate = ((const float*)(ws + WS_G))[trow];
    };
    bf16x8 wf[8];
    wf[0] = op_wfrag<0>(o); wf[1] = op_wfrag<1>(o); wf[2] = op_wfrag<2>(o); wf[3] = op_wfrag<3>(o);
    SBAR();
    poll4(flags + (N_AFLAGS + N_OFLAGS + sk * 16 + rb) * FLAG_STRIDE, lane);
    asm volatile("s_waitcnt vmcnt(0)" ::: "memory");
    TS(ws, 2, 4);
    op_cloads<0>(o, make_iseq<16>{});
    op_cloads<16>(o, make_iseq<8>{});
    SBAR();
    op_steps(o, ac2, wf, epi, make_iseq<112>{});
#pragma unroll
    for (int f = 0; f < 28; ++f) PIN(ac2[f]);
    NOPS16();
#pragma unroll
    for (int f = 0; f < 28; ++f) PIN(ac2[f]);
    f32x4 acc[14];
    {
        ldsp xw_ = lds + (wid >> 1 ? op_slot(3) : op_slot(4)) + lane * 16;
        if (wid & 1) {
#pragma unroll
            for (int f = 0; f < 14; ++f) { *(LAS f32x4*)(xw_ + 14336 + f * 1024) = ac2[f]; acc[f] = ac2[14 + f]; }
        } else {
#pragma unroll
            for (int f = 0; f < 14; ++f) { *(LAS f32x4*)(xw_ + f * 1024) = ac2[14 + f]; acc[f] = ac2[f]; }
        }
        asm volatile("s_waitcnt lgkmcnt(0)\n\ts_barrier" ::: "memory");
        ldscp xr = xw_ + (wid & 1 ? 0 : 14336);
#pragma unroll
        for (int f = 0; f < 14; ++f) acc[f] += *(const LAS f32x4*)(xr + f * 1024);
    }
    TS(ws, 2, 1);
    float* slab = (float*)(ws + WS_OPART) + (size_t)grp * 4 * 14336;
    const __amdgpu_buffer_rsrc_t srs = __builtin_amdgcn_make_buffer_rsrc((void*)slab, (short)0, 4 * 57344, 0x00020000);
    const int soff = ((sk * 4 + wid) * 14 * 64 + lane) * 16;
    if (sk == 0) op_publish<0>(acc, srs, soff); else if (sk == 1) op_publish<1>(acc, srs, soff);
    else if (sk == 2) op_publish<2>(acc, srs, soff); else op_publish<3>(acc, srs, soff);
    asm volatile("s_waitcnt vmcnt(0)" ::: "memory");
    TS(ws, 2, 2);
    unsigned* fl = flags + (N_AFLAGS + grp) * FLAG_STRIDE + wid * 4;
    if (lane == 0) __hip_atomic_store(fl + sk, 1u, RLX_AGENT);
    poll4(fl, lane);
    TS(ws, 2, 3);
    bf16_t* outp = out + (size_t)trow * DM + tcol;
    const int loff = (wid * 14 * 64 + lane) * 16;
    if (sk == 0) op_finish<0>(acc, srs, loff, xw, yw, gate, outp); else if (sk == 1) op_finish<1>(acc, srs, loff, xw, yw, gate, outp);
    else if (sk == 2) op_finish<2>(acc, srs, loff, xw, yw, gate, outp); else op_finish<3>(acc, srs, loff, xw, yw, gate, outp);
    TS(ws, 2, 7);
}

extern "C" __device__ __attribute__((used, noinline)) void text_end_mark() { asm volatile("s_nop 0"); }

extern "C" void kernel_launch(void* const* d_in, const int* in_sizes, int n_in, void* d_out, int out_size, void* d_ws, size_t ws_size,
                              hipStream_t stream) {
    (void)in_sizes; (void)n_in; (void)out_size; (void)ws_size;
    (void)hipFuncSetAttribute((const void*)k1_proj, hipFuncAttributeMaxDynamicSharedMemorySize, K1_LDS);
    (void)hipFuncSetAttribute((const void*)k2_attn, hipFuncAttributeMaxDynamicSharedMemorySize, AT_LDS);
    K1Args P;
    P.x = (const bf16_t*)d_in[0]; P.ak = (const bf16_t*)d_in[1]; P.av = (const bf16_t*)d_in[2]; P.shift_k = (const bf16_t*)d_in[3];
    P.ssm_k = (const bf16_t*)d_in[4]; P.ssm_d = (const bf16_t*)d_in[5]; P.ckv = (const bf16_t*)d_in[7]; P.wq = (const bf16_t*)d_in[8];
    P.wuk = (const bf16_t*)d_in[9]; P.wuv = (const bf16_t*)d_in[10]; P.gate_w = (const bf16_t*)d_in[12]; P.wo = (const bf16_t*)d_in[11]; P.ws = (unsigned char*)d_ws;
#define DEV_STAGES 7
    if (DEV_STAGES & 1) k1_proj<<<256, 512, K1_LDS, stream>>>(P);
    if (DEV_STAGES & 2) k2_attn<<<256, 256, AT_LDS, stream>>>((unsigned char*)d_ws, (const bf16_t*)d_in[0], (bf16_t*)d_out);
}
```

```cpp
#include <hip/hip_runtime.h>
#include <hip/hip_bf16.h>
#include <stdint.h>
#include <utility>

#define LAS __attribute__((address_space(3)))
#define GAS __attribute__((address_space(1)))
#define CAS __attribute__((address_space(4)))
#define DI __device__ __forceinline__
typedef unsigned short bf16_t;
typedef short bf16x8 __attribute__((ext_vector_type(8)));
typedef short s16x4 __attribute__((ext_vector_type(4)));
typedef float f32x2 __attribute__((ext_vector_type(2)));
typedef float f32x4 __attribute__((ext_vector_type(4)));
typedef float f32x16 __attribute__((ext_vector_type(16)));
typedef unsigned u32x2 __attribute__((ext_vector_type(2)));
typedef unsigned u32x4 __attribute__((ext_vector_type(4)));
typedef __bf16 bfx2 __attribute__((ext_vector_type(2)));
typedef LAS unsigned char* ldsp;
typedef const LAS unsigned char* ldscp;

constexpr int SEQ = 2048, DM = 448, NH = 16, HD = 256, DC = 1024, NW_ = 4096;

constexpr size_t MB = 1024 * 1024;
constexpr size_t WS_Q = 0, WS_K = 16 * MB, WS_V = 32 * MB, WS_CTX = 48 * MB, WS_PART = 64 * MB, WS_ML = 80 * MB,
                 WS_Y2 = 81 * MB, WS_G = 84 * MB, WS_FLAGS = 100 * MB, WS_WI = 102 * MB;
constexpr int FLAG_STRIDE = 64;
constexpr int N_AFLAGS = 128, N_CFLAGS = 16;

#define TS(wsp, kern, slot) do { } while (0)
#define TSI(wsp, idx) do { } while (0)
#define SBAR() __builtin_amdgcn_sched_barrier(0)
#define PIN(x) asm volatile("" : "+v"(x))
#define RLX_AGENT __ATOMIC_RELAXED, __HIP_MEMORY_SCOPE_AGENT

DI unsigned cvtpk(float lo, float hi) { f32x2 v = {lo, hi}; bfx2 b = __builtin_convertvector(v, bfx2); return __builtin_bit_cast(unsigned, b); }
DI float bf_lo(unsigned w) { return __uint_as_float(w << 16); }
DI float bf_hi(unsigned w) { return __uint_as_float(w & 0xffff0000u); }
DI float rbf(float x) { return bf_lo(cvtpk(x, 0.f)); }
DI unsigned rfl(unsigned x) { return __builtin_amdgcn_readfirstlane(x); }
DI s16x4 trread(ldscp p) { return __builtin_amdgcn_ds_read_tr16_b64_v4i16((LAS s16x4*)p); }
DI bf16x8 cat8(s16x4 lo, s16x4 hi) { return __builtin_shufflevector(lo, hi, 0, 1, 2, 3, 4, 5, 6, 7); }
DI bf16x8 ldsrd16(ldscp p) { return *(const LAS bf16x8*)p; }
#define MFMA16(a, b, c) __builtin_amdgcn_mfma_f32_16x16x32_bf16((a), (b), (c), 0, 0, 0)
#define MFMA32(a, b, c) __builtin_amdgcn_mfma_f32_32x32x16_bf16((a), (b), (c), 0, 0, 0)

DI void touch(unsigned sink, unsigned voff, const char* g) {
    asm volatile("s_mov_b32 m0, %0\n\ts_nop 0\n\tglobal_load_lds_dword %1, %2" :: "s"(sink), "v"(voff), "s"(g) : "memory");
}
#define CODE_TOUCH(SYM, END, sink, chunk, lane) do { const char* _p; unsigned _n; \
        asm volatile("s_getpc_b64 s[90:91]\n\ts_add_u32 s90, s90, " SYM "@rel32@lo+4\n\ts_addc_u32 s91, s91, " SYM "@rel32@hi+12\n\ts_mov_b64 %0, s[90:91]\n\ts_mov_b32 %1, " END "-" SYM \
                     : "=s"(_p), "=s"(_n) :: "s90", "s91", "scc"); \
        const unsigned _o = ((unsigned)(chunk) * 64u + (unsigned)(lane)) * 128u; \
        if ((unsigned)(chunk) * 8192u < _n && _n < 262144u) touch((sink), _o < _n - 4u ? _o : _n - 4u, _p); } while (0)
#define K1_SYM "_Z7k1_proj6K1Args", "_Z7k2_attnPhPKtPt"
#define K2_SYM "_Z7k2_attnPhPKtPt", "text_end_mark"
#define CODE_TOUCH2(S, sink, chunk, lane) CODE_TOUCH(S, sink, chunk, lane)

constexpr int HTB = 128 * 64 * 2;
constexpr int K1_X = 8 * HTB;
constexpr int K1_LDS = K1_X + 16384 + 2048;

DI int lds_byte(int r, int c) { const int st = (r >> 4) * 2 + (c >> 5), rr = r & 15, cc = c & 31, ob = rr * 64 + cc * 2; return st * 1024 + (ob ^ (((ob >> 9) & 1) << 5)); }
DI void stage_rc(int b, int& R, int& C) { const int st = b / 1024, sb = b % 1024, swz = sb ^ (((sb >> 9) & 1) << 5); R = (st >> 1) * 16 + swz / 64; C = (st & 1) * 32 + (swz % 64) / 2; }

struct K1Args {
    const bf16_t *x, *ak, *av, *shift_k, *ssm_k, *ssm_d, *ckv, *wq, *wuk, *wuv, *gate_w, *wo;
    unsigned char* ws;
};

#define SA(b, h) (((b) * 2 + (h)) * HTB)
#define SB(b, h) ((4 + (b) * 2 + (h)) * HTB)
constexpr int ring_slot(int u) {
    switch (u & 7) { case 5: return SB(0, 0); case 6: return SA(0, 0); case 7: return SB(0, 1); case 0: return SA(0, 1);
                     case 1: return SB(1, 0); case 2: return SA(1, 0); case 3: return SB(1, 1); default: return SA(1, 1); }
}
struct G1 {
    ldsp lds; unsigned ldsw  , voffA_kv, voffA_q, voffB, sv; int aoff, boff0, boff1;
    const char *A_kv, *A_q, *B_kv, *B_q, *sbase; unsigned sstep, sdst, sdstep; int sadd;
};
DI int lane_id() { int l; asm volatile("v_mbcnt_lo_u32_b32 %0, -1, 0\n\tv_mbcnt_hi_u32_b32 %0, -1, %0" : "=v"(l)); return l; }
template <int OFF> DI void dma16(unsigned lbase, unsigned voff, const char* g) {
    asm volatile("s_add_i32 m0, %0, %c3\n\ts_nop 0\n\tglobal_load_lds_dwordx4 %1, %2" :: "s"(lbase), "v"(voff), "s"(g), "i"(OFF) : "memory", "scc");
}
template <int BUF, int STRIDE, int H> DI void stage_a(const G1& g, const char* base, unsigned voff) {
    dma16<BUF>(g.ldsw, voff, base + H * 128 * STRIDE); dma16<BUF + 8192>(g.ldsw, voff, base + (H * 128 + 64) * STRIDE);
}
template <int BUF, int H> DI void stage_b(const G1& g, const char* base) {
    dma16<BUF>(g.ldsw, g.voffB, base + H * 256); dma16<BUF + 8192>(g.ldsw, g.voffB, base + H * 256 + 32 * NW_ * 2);
}
template <int U> DI void stage_unit(const G1& g) {
    if constexpr (U < 21) {
        constexpr int t = U / 3, r = U % 3;
        if constexpr (r == 0) stage_b<ring_slot(U), 0>(g, g.B_q + (size_t)t * (64 * NW_ * 2));
        else stage_a<ring_slot(U), DM * 2, r - 1>(g, g.A_q + t * 128, g.voffA_q);
    } else {
        constexpr int t = (U - 21) >> 2, r = (U - 21) & 3;
        if constexpr ((r & 1) == 0) stage_b<ring_slot(U), (r >> 1)>(g, g.B_kv + (size_t)t * (64 * NW_ * 2));
        else stage_a<ring_slot(U), DC * 2, (r >> 1)>(g, g.A_kv + t * 128, g.voffA_kv);
    }
}
DI void side(const G1& g, int t) {
    const int tt = t + g.sadd > 15 ? 15 : t + g.sadd;
    asm volatile("s_mov_b32 m0, %0\n\ts_nop 0\n\tglobal_load_lds_dword %1, %2" :: "s"(g.sdst + (unsigned)t * g.sdstep), "v"(g.sv), "s"(g.sbase + rfl((unsigned)tt * g.sstep)) : "memory");
}
DI void lda(bf16x8 (&dst)[4][2], const G1& g, int buf) {
#pragma unroll
    for (int m = 0; m < 4; ++m)
#pragma unroll
        for (int k = 0; k < 2; ++k) dst[m][k] = ldsrd16(g.lds + buf + g.aoff + m * 2048 + k * 1024);
}
DI void ldb(bf16x8 (&dst)[2][2], const G1& g, int buf) {
#pragma unroll
    for (int n = 0; n < 2; ++n)
#pragma unroll
        for (int k = 0; k < 2; ++k) { ldscp p = g.lds + buf + (n ? g.boff1 : g.boff0) + k * 8192; dst[n][k] = cat8(trread(p), trread(p + 256)); }
}
template <bool VT> DI void mma(f32x4 (&acc)[4][2], const bf16x8 (&At)[4][2], const bf16x8 (&Bt)[2][2]) {
    __builtin_amdgcn_s_setprio(1);
#pragma unroll
    for (int m = 0; m < 4; ++m)
#pragma unroll
        for (int n = 0; n < 2; ++n)
#pragma unroll
            for (int k = 0; k < 2; ++k) acc[m][n] = VT ? MFMA16(At[m][k], Bt[n][k], acc[m][n]) : MFMA16(Bt[n][k], At[m][k], acc[m][n]);
    __builtin_amdgcn_s_setprio(0);
}
#define WAIT_V(n) asm volatile("s_waitcnt vmcnt(" #n ")" ::: "memory")
#define WAIT_L(n) asm volatile("s_waitcnt lgkmcnt(" #n ")" ::: "memory")
#define BAR __builtin_amdgcn_s_barrier()
DI void zero8(f32x4 (&a)[4][2]) {
#pragma unroll
    for (int m = 0; m < 4; ++m) { a[m][0] = (f32x4){0.f, 0.f, 0.f, 0.f}; a[m][1] = (f32x4){0.f, 0.f, 0.f, 0.f}; }
}

DI void pack_kq(const f32x4 (&acc)[4][2], u32x4* pk, float scale) {
#pragma unroll
    for (int m = 0; m < 4; ++m) {
        const f32x4 a0 = acc[m][0] * scale, a1 = acc[m][1] * scale;
        const unsigned w0 = cvtpk(a0[0], a0[1]), w1 = cvtpk(a0[2], a0[3]), x0 = cvtpk(a1[0], a1[1]), x1 = cvtpk(a1[2], a1[3]);
        auto r0 = __builtin_amdgcn_permlane16_swap(w0, x0, false, false);
        auto r1 = __builtin_amdgcn_permlane16_swap(w1, x1, false, false);
        pk[m] = (u32x4){r0[0], r1[0], r0[1], r1[1]};
    }
}
DI unsigned kq_off(int pm, int wr, int wc, int fr, int fq) {
    return (unsigned)((pm * 8 + wr * 2) * 16384 + fr * 16 + (2 * wc + (fq & 1)) * 1024 + (fq >> 1) * 512);
}
DI void pack_v(const f32x4 (&acc)[4][2], u32x4* pk) {
#pragma unroll
    for (int n = 0; n < 2; ++n)
#pragma unroll
        for (int mm = 0; mm < 2; ++mm) {
            const f32x4 a0 = acc[2 * mm][n], a1 = acc[2 * mm + 1][n];
            const unsigned w0 = cvtpk(a0[0], a0[1]), w1 = cvtpk(a0[2], a0[3]), x0 = cvtpk(a1[0], a1[1]), x1 = cvtpk(a1[2], a1[3]);
            auto r0 = __builtin_amdgcn_permlane32_swap(w0, x0, false, false);
            auto r1 = __builtin_amdgcn_permlane32_swap(w1, x1, false, false);
            pk[2 * mm + n] = (u32x4){r0[0], r1[0], r0[1], r1[1]};
        }
}
constexpr int ST_AUX = 16;
struct Out1 { u32x4 pk[16]; __amdgpu_buffer_rsrc_t rs; unsigned voff; int sbj, shi; };
template <int I> DI void st_piece(const Out1& o) {
    __builtin_amdgcn_raw_buffer_store_b128(o.pk[I], o.rs, o.voff + (I & 1) * 256, (I >> 3) * 65536 + ((I >> 2) & 1) * o.sbj + ((I >> 1) & 1) * o.shi, ST_AUX);
}
template <int A, int B> DI void st_range(const Out1& o) { if constexpr (A < B) { st_piece<A>(o); st_range<A + 1, B>(o); } }

template <int N, class W> DI void qphase(const G1& g, f32x4 (&a0)[4][2], f32x4 (&a1)[4][2], bf16x8 (&At)[4][2], bf16x8 (&B0)[2][2], W& warm) {
    constexpr int t = N >> 1;
    if constexpr ((N & 1) == 0) {
        ldb(B0, g, ring_slot(3 * t)); SBAR(); lda(At, g, ring_slot(3 * t + 1));
        if constexpr (t < 5) warm(t);
        if constexpr (N >= 2) stage_unit<3 * t + 6>(g);
        if constexpr (N == 0) WAIT_V(10); else WAIT_V(8);
        WAIT_L(8); BAR; WAIT_L(0); mma<false>(a0, At, B0); BAR; SBAR();
    } else {
        lda(At, g, ring_slot(3 * t + 2));
        if constexpr (t >= 1) stage_unit<3 * t + 7>(g);
        stage_unit<3 * t + 8>(g);
        WAIT_V(8);
        if constexpr (N == 13) WAIT_L(0);
        BAR; WAIT_L(0); mma<false>(a1, At, B0); BAR; SBAR();
    }
}
template <class W, int... Ns> DI void qphases(const G1& g, f32x4 (&a0)[4][2], f32x4 (&a1)[4][2], bf16x8 (&At)[4][2], bf16x8 (&B0)[2][2], W& warm, std::integer_sequence<int, Ns...>) {
    (qphase<Ns>(g, a0, a1, At, B0, warm), ...);
}
template <int... Us> DI void stage_units(const G1& g, std::integer_sequence<int, Us...>) { (stage_unit<Us>(g), ...); }

template <bool VT, int POS, class W>
DI void mainpair(const G1& g, int t, f32x4 (&acc)[2][2][4][2], bf16x8 (&At)[4][2], bf16x8 (&B0)[2][2], bf16x8 (&B1)[2][2], W& lastside) {
    const char* a1 = g.A_kv + (t + 1) * 128; const char* a2 = a1 + 128; const char* a3 = a1 + 256;
    const char* b2 = g.B_kv + (size_t)(t + 2) * (64 * NW_ * 2); const char* b3 = b2 + 64 * NW_ * 2;
    ldb(B0, g, SB(0, 0)); SBAR(); lda(At, g, SA(0, 0));
    if constexpr (POS != 0) stage_a<SA(1, 1), DC * 2, 1>(g, a1, g.voffA_kv); else WAIT_V(18);
    WAIT_L(8); BAR; WAIT_L(0); mma<VT>(acc[0][0], At, B0); BAR; SBAR();
    ldb(B1, g, SB(0, 1));
    if constexpr (POS == 2) lastside(0); else side(g, t);
    if constexpr (POS != 2) stage_b<SB(0, 0), 0>(g, b2);
    if constexpr (POS == 0) WAIT_V(19);
    BAR; WAIT_L(0); mma<VT>(acc[0][1], At, B1); BAR;
    lda(At, g, SA(0, 1));
    if constexpr (POS != 2) stage_a<SA(0, 0), DC * 2, 0>(g, a2, g.voffA_kv);
    BAR; WAIT_L(0); mma<VT>(acc[1][0], At, B0); BAR; SBAR();
    if constexpr (POS != 2) stage_b<SB(0, 1), 1>(g, b2);
    if constexpr (POS == 0) WAIT_V(15); else if constexpr (POS == 1) WAIT_V(7); else WAIT_V(1);
    BAR; mma<VT>(acc[1][1], At, B1); BAR;
    ldb(B0, g, SB(1, 0)); SBAR(); lda(At, g, SA(1, 0));
    if constexpr (POS != 2) stage_a<SA(0, 1), DC * 2, 1>(g, a2, g.voffA_kv);
    WAIT_L(8); BAR; WAIT_L(0); mma<VT>(acc[0][0], At, B0); BAR; SBAR();
    ldb(B1, g, SB(1, 1));
    if constexpr (POS == 2) lastside(1); else side(g, t + 1);
    if constexpr (POS != 2) stage_b<SB(1, 0), 0>(g, b3);
    BAR; WAIT_L(0); mma<VT>(acc[0][1], At, B1); BAR;
    lda(At, g, SA(1, 1));
    if constexpr (POS != 2) stage_a<SA(1, 0), DC * 2, 0>(g, a3, g.voffA_kv);
    BAR; WAIT_L(0); mma<VT>(acc[1][0], At, B0); BAR; SBAR();
    if constexpr (POS != 2) { stage_b<SB(1, 1), 1>(g, b3); WAIT_V(7); }
    BAR; mma<VT>(acc[1][1], At, B1); BAR;
}

constexpr int CV_RAW = SA(0, 0);
constexpr int K1_WO = SA(0, 1);
constexpr int CV_HSTR = 4256;
constexpr int CV_H = 2 * HTB;
constexpr int CV_USTR = 4608;
constexpr int CV_U = CV_H + 8 * CV_HSTR;
constexpr int CV_R = CV_U + 2 * CV_USTR;
static_assert(CV_R + 65536 <= K1_LDS && CV_R <= K1_X, "lds");

DI const char* wo_block(const K1Args& P, int e) { const int eb = e < 224 ? e : 0, kb = eb / 7, cn = eb - kb * 7; return (const char*)P.wo + ((size_t)kb * 128 * DM + cn * 64) * 2; }
DI void tail_stage(const G1& g, const K1Args& P, int c0, int e, int wid, int lane) {
    dma16<CV_RAW>(g.ldsw, (unsigned)lane * 16u, (const char*)P.ssm_k + (size_t)c0 * (SEQ * 2) + wid * 1024);
    const char* wg = wo_block(P, e);
    const unsigned vo = (unsigned)((wid * 8 + (lane >> 3)) * DM + (lane & 7) * 8) * 2u;
    dma16<K1_WO>(g.ldsw, vo, wg); dma16<K1_WO + 8192>(g.ldsw, vo, wg + 64 * DM * 2);
}
DI void wo_image(ldsp lds, const K1Args& P, int e, int wid, int lane) {
    const int fr = lane & 15, fq = lane >> 4, kb = e / 7, cn = e - kb * 7;
    if (e < 224) {
        ldscp wp = lds + K1_WO + (wid >> 1) * 4096 + (16 * (fq >> 1) + 4 * (fq & 1) + (fr >> 2)) * 128 + (fr & 3) * 8 + (wid & 1) * 64;
        unsigned char* op = P.ws + WS_WI + (size_t)cn * 524288 + kb * 16384 + wid * 2048 + lane * 16;
#pragma unroll
        for (int cb = 0; cb < 2; ++cb) *(bf16x8*)(op + cb * 1024) = cat8(trread(wp + cb * 32), trread(wp + cb * 32 + 1024));
    }
}
DI void conv_copies(ldsp q, const u32x4 d, const u32x2 e) {
    const unsigned a0 = __builtin_amdgcn_alignbit(d[1], d[0], 16), a1 = __builtin_amdgcn_alignbit(d[2], d[1], 16), a2 = __builtin_amdgcn_alignbit(d[3], d[2], 16),
                   a3 = __builtin_amdgcn_alignbit(e[0], d[3], 16), a4 = __builtin_amdgcn_alignbit(e[1], e[0], 16);
    *(LAS u32x4*)(q) = (u32x4){a0, a1, a2, a3};
    *(LAS u32x4*)(q + CV_HSTR) = (u32x4){d[1], d[2], d[3], e[0]};
    *(LAS u32x4*)(q + 2 * CV_HSTR) = (u32x4){a1, a2, a3, a4};
    *(LAS u32x4*)(q + 3 * CV_HSTR) = (u32x4){d[2], d[3], e[0], e[1]};
}
DI void conv_frags(bf16x8 (&A)[8], bf16x8 (&B)[8], ldscp hp, ldscp up) {
#pragma unroll
    for (int v = 0; v < 8; ++v) {
        B[v] = ldsrd16(up + (7 - v) * 512);
        const s16x4 lo = *(const LAS s16x4*)(hp + v * 512), hi = *(const LAS s16x4*)(hp + v * 512 + 8);
        A[v] = cat8(lo, hi);
    }
}
DI void conv_mma(f32x4 (&acc)[8], const bf16x8 (&A)[8], const bf16x8 (&B)[8]) {
#pragma unroll
    for (int J = 0; J < 8; ++J)
#pragma unroll
        for (int v = 0; v <= J; ++v) acc[J] = MFMA16(A[v], B[J - v], acc[J]);
}
DI void conv_part(ldsp lds, const K1Args& P, int c0, const u32x4 sw, const unsigned dw, int wid, int lane, const Out1& o) {
    const int tid = wid * 64 + lane, fr = lane & 15, fq = lane >> 4;
    {
        const int cq = tid & 255;
        const u32x4 d = *(const LAS u32x4*)(lds + CV_RAW + tid * 16);
        u32x2 e = *(const LAS u32x2*)(lds + CV_RAW + tid * 16 + 16);
        const u32x4 z4 = {0u, 0u, 0u, 0u}; const u32x2 z2 = {0u, 0u};
        if (cq == 255) e = z2;
        ldsp q = lds + CV_H + (tid >> 8) * 4 * CV_HSTR + (cq + 2) * 16;
        conv_copies(q, d, e);
        if (cq == 0) { conv_copies(q - 16, z4, (u32x2){d[0], d[1]}); conv_copies(q - 32, z4, z2); }
        if (cq == 255) { conv_copies(q + 16, z4, z2); conv_copies(q + 32, z4, z2); }
    }
    SBAR(); st_range<4, 6>(o); SBAR();
    {
        ldscp kp = lds + K1_X + tid * 16;
        u32x4 a = *(const LAS u32x4*)(kp - 16);
        const u32x4 b = *(const LAS u32x4*)(kp), vr = *(const LAS u32x4*)(kp + 8192);
        if (tid == 0) a = (u32x4){0u, 0u, 0u, 0u};
        const unsigned kr[7] = {a[1], a[2], a[3], b[0], b[1], b[2], b[3]};
#pragma unroll
        for (int c = 0; c < 2; ++c) {
            const float w0 = bf_lo(sw[2 * c]), w1 = bf_hi(sw[2 * c]), w2 = bf_lo(sw[2 * c + 1]), w3 = bf_hi(sw[2 * c + 1]);
            float kc[7], kv[4];
#pragma unroll
            for (int i = 0; i < 7; ++i) kc[i] = c ? bf_hi(kr[i]) : bf_lo(kr[i]);
#pragma unroll
            for (int i = 0; i < 4; ++i) {
                float s = rbf(kc[i] * w3);
                s = rbf(s + rbf(kc[i + 1] * w2));
                s = rbf(s + rbf(kc[i + 2] * w1));
                s = rbf(s + rbf(kc[i + 3] * w0));
                kv[i] = s * (c ? bf_hi(vr[i]) : bf_lo(vr[i]));
            }
            u32x2 wv = {cvtpk(kv[3], kv[2]), cvtpk(kv[1], kv[0])};
            *(LAS u32x2*)(lds + CV_U + c * CV_USTR + (2044 - 4 * tid) * 2) = wv;
            SBAR(); if (c == 0) st_range<6, 8>(o); else st_range<8, 10>(o); SBAR();
        }
        if (tid < 120) { const int c = tid / 60, j = tid - c * 60; *(LAS u32x2*)(lds + CV_U + c * CV_USTR + 4096 + j * 8) = (u32x2){0u, 0u}; }
    }
    __syncthreads();
    TS(P.ws, 0, 5);
    const int ch = wid >> 2, qw = wid & 3;
    {
        const int y1 = 24 + fr - 8 * fq;
        ldscp hb = lds + CV_H + ch * 4 * CV_HSTR + (y1 & 3) * CV_HSTR + (y1 & ~3) * 2 + qw * 128;
        ldscp ub = lds + CV_U + ch * CV_USTR + (2056 - 256 * 7 - 16 * fr - 8 * fq) * 2 + qw * 128;
        f32x4 acc[8];
#pragma unroll
        for (int i = 0; i < 8; ++i) acc[i] = (f32x4){0.f, 0.f, 0.f, 0.f};
        bf16x8 A0[8], B0[8], A1[8], B1[8];
        conv_frags(A0, B0, hb, ub); SBAR();
        conv_frags(A1, B1, hb + 64, ub + 64); SBAR();
        st_range<10, 12>(o); SBAR();
        conv_mma(acc, A0, B0); SBAR();
        st_range<12, 14>(o); SBAR();
        conv_mma(acc, A1, B1); SBAR();
        st_range<14, 16>(o); SBAR();
#pragma unroll
        for (int i = 0; i < 8; ++i) *(LAS f32x4*)(lds + CV_R + wid * 8192 + i * 1024 + lane * 16) = acc[i];
    }
    __syncthreads();
    TS(P.ws, 0, 6);
    {
        const float dd = ch ? bf_hi(dw) : bf_lo(dw);
#pragma unroll
        for (int i = 0; i < 2; ++i) {
            ldscp rp = lds + CV_R + ch * 32768 + (2 * qw + i) * 1024 + lane * 16;
            const f32x4 y = ((*(const LAS f32x4*)rp + *(const LAS f32x4*)(rp + 8192)) + *(const LAS f32x4*)(rp + 16384)) + *(const LAS f32x4*)(rp + 24576);
            const int t = 256 * (2 * qw + i) + 16 * fr + 4 * fq;
            const u32x2 uw = *(const LAS u32x2*)(lds + CV_U + ch * CV_USTR + (2044 - t) * 2);
            const float kv[4] = {bf_hi(uw[1]), bf_lo(uw[1]), bf_hi(uw[0]), bf_lo(uw[0])};
            float r[4];
#pragma unroll
            for (int e = 0; e < 4; ++e) r[e] = rbf(y[e]) + rbf(kv[e] * dd);
            u32x2 w = {cvtpk(r[0], r[1]), cvtpk(r[2], r[3])};
            *(u32x2*)(P.ws + WS_Y2 + ((size_t)(c0 + ch) * SEQ + t) * 2) = w;
        }
    }
}

DI void gate_part(const K1Args& P, int row0, int wid, int lane) {
    const int lc = lane < 56 ? lane : 0;
    u32x4 gw = *(const u32x4*)(P.gate_w + lc * 8);
    float* gp = (float*)(P.ws + WS_G);
    u32x4 xv[8];
#pragma unroll
    for (int r = 0; r < 8; ++r) xv[r] = *(const u32x4*)(P.x + (size_t)(row0 + wid * 8 + r) * DM + lc * 8);
    float mine = 0.f;
#pragma unroll
    for (int r = 0; r < 8; ++r) {
        float s = 0.f;
#pragma unroll
        for (int j = 0; j < 4; ++j) { s += rbf(bf_lo(xv[r][j]) * bf_lo(gw[j])); s += rbf(bf_hi(xv[r][j]) * bf_hi(gw[j])); }
        if (lane >= 56) s = 0.f;
#pragma unroll
        for (int o = 32; o >= 1; o >>= 1) s += __shfl_xor(s, o);
        if (lane == r) mine = s;
    }
    const float z = rbf(mine);
    const float g = rbf(1.0f / (1.0f + __expf(-z)));
    if (lane < 8) gp[row0 + wid * 8 + lane] = g;
}

template <bool VT>
DI void big_tile(const G1& g, const K1Args& P, int pm, int head, int c0, int e, int wid, bf16x8 (&At)[4][2], bf16x8 (&B0)[2][2], Out1& o) {
    f32x4 acc[2][2][4][2];
    zero8(acc[0][0]); zero8(acc[0][1]); zero8(acc[1][0]); zero8(acc[1][1]);
    bf16x8 B1[2][2];
    auto lastside = [&](int s) {
        if (wid < 4) side(g, 14 + s);
        else if (s == 0) {
            const unsigned l = (unsigned)lane_id();
            if (wid == 4) touch(g.sdst, l * 128u, (const char*)P.ssm_k + (size_t)c0 * (SEQ * 2));
            else touch(g.sdst, l * (DM * 2), wo_block(P, e) + (wid & 1) * (64 * DM * 2));
        }
    };
    mainpair<VT, 0>(g, 0, acc, At, B0, B1, lastside);
    for (int t = 2; t < 14; t += 2) mainpair<VT, 1>(g, t, acc, At, B0, B1, lastside);
    mainpair<VT, 2>(g, 14, acc, At, B0, B1, lastside);
    TS(P.ws, 0, 3);
    const int lane = lane_id();
    tail_stage(g, P, c0, e, wid, lane);
    if (wid == 7) CODE_TOUCH2(K2_SYM, g.sdst, e & 7, lane);
    SBAR();
    const int wr = wid >> 2, wc = wid & 3, fr = lane & 15, fq = lane >> 4;
    if (VT) {
        o.voff = (unsigned)((pm * 4 + wr) * 32768 + wc * 4096 + (fq >> 1) * 1024 + (fq & 1) * 512 + fr * 16);
        pack_v(acc[0][0], o.pk); pack_v(acc[0][1], o.pk + 4); pack_v(acc[1][0], o.pk + 8); pack_v(acc[1][1], o.pk + 12);
    } else {
        o.voff = kq_off(pm, wr, wc, fr, fq);
        pack_kq(acc[0][0], o.pk, 1.0f); pack_kq(acc[0][1], o.pk + 4, 1.0f); pack_kq(acc[1][0], o.pk + 8, 1.0f); pack_kq(acc[1][1], o.pk + 12, 1.0f);
    }
}

__global__ __launch_bounds__(512, 2) void k1_proj(K1Args P) {
    extern __shared__ __attribute__((aligned(16))) unsigned char shm[];
    ldsp lds = (ldsp)shm;
    const int b = blockIdx.x, xcd = b & 7, j = b >> 3, li = j & 15, pm = li & 7, head = 2 * xcd + (li >> 3), vt = j >> 4;
    const int tid = threadIdx.x, wid = rfl(tid >> 6), lane = tid & 63, wr = wid >> 2, wc = wid & 3, fr = lane & 15, fq = lane >> 4;
    const int c0 = (xcd * 28 + (j < 28 ? j : 0)) * 2, e = xcd * 32 + j;
    TS(P.ws, 0, 0);
    if (wid == 7) CODE_TOUCH2(K1_SYM, (unsigned)(uintptr_t)lds + K1_X + 16384 + 7 * 256, j, lane);
    u32x4 sw = *(const CAS u32x4*)(uintptr_t)(P.shift_k + c0 * 4); unsigned dw = *(const CAS unsigned*)(uintptr_t)(P.ssm_d + c0);
    G1 g;
    const unsigned lds0 = (unsigned)(uintptr_t)lds;
    g.lds = lds; g.ldsw = lds0 + (unsigned)wid * 1024u;
    { int R, C; stage_rc(tid * 16, R, C); g.voffA_kv = (unsigned)(R * DC + C) * 2u; g.voffA_q = (unsigned)(R * DM + C) * 2u; }
    { const int kg = wid >> 1, row = (lane >> 2) & 7, ch = (lane & 3) ^ (2 * (kg & 1)), n = 32 * (2 * (wid & 1) + (lane >> 5)) + 8 * ch;
      g.voffB = (unsigned)((8 * kg + row) * NW_ + n) * 2u; }
    g.aoff = lds_byte(wr * 64 + fr, fq * 8);
    g.boff0 = fq * 2048 + wc * 512 + (fr >> 2) * 64 + (32 * (fq & 1)) + 8 * (fr & 3);
    g.boff1 = g.boff0 ^ 32;
    g.A_kv = (const char*)P.ckv + (size_t)pm * 256 * DC * 2;
    g.A_q = (const char*)P.x + (size_t)pm * 256 * DM * 2;
    g.B_kv = (const char*)(vt ? P.wuv : P.wuk) + head * 512;
    g.B_q = (const char*)P.wq + head * 512 + vt * 256;
    if (wid < 4) {
        g.sbase = (const char*)(wid < 2 ? P.ak : P.av) + (wid & 1) * (64 * DM * 2) + c0 * 2; g.sstep = 128 * DM * 2; g.sv = (unsigned)lane * (DM * 2);
        g.sadd = 0; g.sdst = lds0 + K1_X + (wid >> 1) * 8192 + (wid & 1) * 256; g.sdstep = 512;
    } else {
        if (wid == 4) { g.sbase = g.A_kv + (size_t)((head & 1) * 2 + vt) * 64 * (DC * 2); g.sstep = 128; g.sv = (unsigned)lane * (DC * 2); }
        else if (wid == 5) { g.sbase = g.B_kv + (size_t)pm * 8 * (NW_ * 2); g.sstep = 64 * NW_ * 2; g.sv = (unsigned)((lane >> 2) & 7) * (NW_ * 2) + (lane & 3) * 128; }
        else { g.sbase = (const char*)(wid == 6 ? P.ak : P.av) + (size_t)j * 4 * (DM * 2) + xcd * 112; g.sstep = 128 * DM * 2; g.sv = (unsigned)(lane & 3) * (DM * 2) + ((lane >> 2) & 1) * 108; }
        g.sadd = 4; g.sdst = lds0 + K1_X + 16384 + wid * 256; g.sdstep = 0;
    }
    PIN(g.voffA_kv); PIN(g.voffB); PIN(g.sv); PIN(g.aoff); PIN(g.boff0); PIN(g.boff1);
    stage_units(g, std::make_integer_sequence<int, 8>{});
    WAIT_V(12);
    auto warm = [&](int slot) {
        const unsigned sink = lds0 + K1_X + 16384 + wid * 256;
        if (slot > 0) { if (wid >= 4) side(g, slot - 5); }
        else if (wid < 3) touch(sink, (unsigned)lane * (DM * 2), g.A_q + (size_t)((head & 1) * 2 + vt) * 64 * (DM * 2) + (4 + wid) * 128);
        else if (wid == 3) touch(sink, (unsigned)((lane < 48 ? lane : 0) >> 1) * (NW_ * 2) + (lane & 1) * 128, g.B_q + (size_t)(256 + pm * 24) * (NW_ * 2));
    };
    asm volatile("" : "+s"(sw), "+s"(dw));
    if (b == 0 && tid < N_AFLAGS + N_CFLAGS) {
#pragma unroll
        for (int q = 0; q < 4; ++q) *(u32x4*)((unsigned*)(P.ws + WS_FLAGS) + tid * FLAG_STRIDE + 4 * q) = (u32x4){0u, 0u, 0u, 0u};
    }
    bf16x8 At[4][2], B0[2][2];
    BAR;
    if (wr == 1) BAR;
    TS(P.ws, 0, 1);
    {
        f32x4 q0[4][2], q1[4][2];
        zero8(q0); zero8(q1);
        qphases(g, q0, q1, At, B0, warm, std::make_integer_sequence<int, 14>{});
        stage_unit<27>(g); stage_unit<28>(g); SBAR();
        Out1 o;
        o.rs = __builtin_amdgcn_make_buffer_rsrc(P.ws + WS_Q + (size_t)head * MB, (short)0, (int)MB, 0x00020000);
        o.voff = kq_off(pm, wr, wc, fr, fq) + vt * 8192; o.sbj = 65536; o.shi = 16384;
        pack_kq(q0, o.pk, 0.0625f * 1.4426950408889634f); pack_kq(q1, o.pk + 4, 0.0625f * 1.4426950408889634f);
        st_range<0, 8>(o);
        SBAR();
    }
    TS(P.ws, 0, 2);
    Out1 o;
    if (vt) big_tile<true>(g, P, pm, head, c0, e, wid, At, B0, o); else big_tile<false>(g, P, pm, head, c0, e, wid, At, B0, o);
    o.rs = __builtin_amdgcn_make_buffer_rsrc(P.ws + (vt ? WS_V : WS_K) + (size_t)head * MB, (short)0, (int)MB, 0x00020000);
    o.sbj = vt ? 16384 : 8192; o.shi = vt ? 2048 : 16384;
    SBAR(); st_range<0, 4>(o); SBAR();
    WAIT_V(4);
    if (wr == 0) BAR;
    BAR;
    TS(P.ws, 0, 4);
    const int lane2 = lane_id();
    wo_image(lds, P, e, wid, lane2);
    if (j < 28) conv_part(lds, P, c0, sw, dw, wid, lane2, o);
    else { st_range<4, 16>(o); gate_part(P, (xcd * 4 + (j - 28)) * 64, wid, lane2); }
    TS(P.ws, 0, 7);
}
#undef SA
#undef SB

constexpr int AT_K = 0, AT_V = 65536, AT_LDS = 163840;
constexpr float THR = 8.0f;
constexpr int PD = 6;
constexpr int NEB = 24;
template <int... Is> using iseq = std::integer_sequence<int, Is...>;
template <int N> using make_iseq = std::make_integer_sequence<int, N>;
#define ACLOB "a0","a1","a2","a3","a4","a5","a6","a7","a8","a9","a10","a11","a12","a13","a14","a15","a16","a17","a18","a19","a20","a21","a22","a23","a24","a25","a26","a27","a28","a29","a30","a31","a32","a33","a34","a35","a36","a37","a38","a39","a40","a41","a42","a43","a44","a45","a46","a47","a48","a49","a50","a51","a52","a53","a54","a55","a56","a57","a58","a59","a60","a61","a62","a63","a64","a65","a66","a67","a68","a69","a70","a71","a72","a73","a74","a75","a76","a77","a78","a79","a80","a81","a82","a83","a84","a85","a86","a87","a88","a89","a90","a91","a92","a93","a94","a95","a96","a97","a98","a99","a100","a101","a102","a103","a104","a105","a106","a107","a108","a109","a110","a111","a112","a113","a114","a115","a116","a117","a118","a119","a120","a121","a122","a123","a124","a125","a126","a127","a128","a129","a130","a131","a132","a133","a134","a135","a136","a137","a138","a139","a140","a141","a142","a143","a144","a145","a146","a147","a148","a149","a150","a151","a152","a153","a154","a155","a156","a157","a158","a159","a160","a161","a162","a163","a164","a165","a166","a167","a168","a169","a170","a171","a172","a173","a174","a175","a176","a177","a178","a179","a180","a181","a182","a183","a184","a185","a186","a187","a188","a189","a190","a191","a192","a193","a194","a195","a196","a197","a198","a199","a200","a201","a202","a203","a204","a205","a206","a207","a208","a209","a210","a211","a212","a213","a214","a215","a216","a217","a218","a219","a220","a221","a222","a223","a224","a225","a226","a227","a228","a229","a230","a231","a232","a233","a234","a235","a236","a237","a238","a239","a240","a241","a242","a243","a244","a245","a246","a247","a248","a249","a250","a251","a252","a253","a254","a255"

constexpr int op_slot(int s) { switch (s % 5) { case 0: return 0; case 1: return 32768; case 2: return 65536; case 3: return 131072; default: return 98304; } }
static_assert(AT_V + 32768 == 98304 && AT_LDS == 163840, "lds");
struct OPJ { const char *wbase, *cbase; unsigned cv, lds0; ldscp wp; const unsigned* fl; unsigned ready; unsigned char* ws; };
struct AT {
    OPJ o;
    u32x2 pk;
    f32x2 ml2;
    const char *part, *mlp;
    bool isA, early;
    unsigned* flag;
    f32x16 s[2][2];
    f32x16 cm[2];
    u32x4 p[4];
    bf16x8 fr[8];
    float m, l, rs0, rs1, mx0, mx1, alpha, aone;
    bool pend, first, drain;
    ldscp kb;
    ldscp vcur;
    unsigned goff;
    unsigned m0k, m0v;
    const char *gk, *gv;
};

DI void dma_piece(unsigned m0v, unsigned goff, const char* g, const int off) {
    switch (off) {
    case 0: asm volatile("s_mov_b32 m0, %0\n\ts_nop 0\n\tglobal_load_lds_dwordx4 %1, %2" :: "s"(m0v), "v"(goff), "s"(g) : "memory"); break;
    case 1: asm volatile("s_mov_b32 m0, %0\n\ts_nop 0\n\tglobal_load_lds_dwordx4 %1, %2 offset:1024" :: "s"(m0v), "v"(goff), "s"(g) : "memory"); break;
    case 2: asm volatile("s_mov_b32 m0, %0\n\ts_nop 0\n\tglobal_load_lds_dwordx4 %1, %2 offset:2048" :: "s"(m0v), "v"(goff), "s"(g) : "memory"); break;
    default: asm volatile("s_mov_b32 m0, %0\n\ts_nop 0\n\tglobal_load_lds_dwordx4 %1, %2 offset:3072" :: "s"(m0v), "v"(goff), "s"(g) : "memory"); break;
    }
}
template <int I> DI void dma8(unsigned m0base, unsigned goff, const char* g) { dma_piece(m0base + (I >> 2) * 4096, goff, g + (I >> 2) * 4096, I & 3); }
DI void dma_tile(unsigned m0base, unsigned goff, const char* g) {
    dma8<0>(m0base, goff, g); dma8<1>(m0base, goff, g); dma8<2>(m0base, goff, g); dma8<3>(m0base, goff, g);
    dma8<4>(m0base, goff, g); dma8<5>(m0base, goff, g); dma8<6>(m0base, goff, g); dma8<7>(m0base, goff, g);
}
DI float ex2(float x) { return __builtin_amdgcn_exp2f(x); }
DI float MX3(float a, float b, float c) { float r; asm("v_max3_f32 %0, %1, %2, %3" : "=v"(r) : "v"(a), "v"(b), "v"(c)); return r; }
DI float MX2(float a, float b) { float r; asm("v_max_f32_e32 %0, %1, %2" : "=v"(r) : "v"(a), "v"(b)); return r; }
template <int D0> DI void qk_init(f32x16& s, const bf16x8& k, const f32x16& c) {
    asm volatile("v_mfma_f32_32x32x16_bf16 %0, %1, a[%c3:%c4], %2" : "=&v"(s) : "v"(k), "v"(c), "i"(128 + 4 * D0), "i"(131 + 4 * D0)); }
template <int D0> DI void qk_acc(f32x16& s, const bf16x8& k) {
    asm volatile("v_mfma_f32_32x32x16_bf16 %0, %1, a[%c2:%c3], %0" : "+v"(s) : "v"(k), "i"(128 + 4 * D0), "i"(131 + 4 * D0)); }
template <int DB> DI void pv_acc(const bf16x8& v, const u32x4& p) {
    asm volatile("v_mfma_f32_32x32x16_bf16 a[%c2:%c3], %0, %1, a[%c2:%c3]" :: "v"(v), "v"(p), "i"(16 * DB), "i"(16 * DB + 15)); }
DI void add_bcast(f32x16& x, float aone, float b) { asm volatile("s_nop 1\n\tv_mfma_f32_32x32x2_f32 %0, %1, %2, %0" : "+v"(x) : "v"(aone), "v"(b)); }
#define NOPS16() asm volatile("s_nop 7\n\ts_nop 7" ::: "memory")
#define NOPS2() asm volatile("s_nop 1" ::: "memory")
template <int I> DI void a_zero() { asm volatile("v_accvgpr_write_b32 a[%c0], 0" :: "i"(I)); }
template <int I> DI void a_scale(float f) { float t; asm volatile("v_accvgpr_read_b32 %0, a[%c2]\n\tv_mul_f32 %0, %1, %0\n\tv_accvgpr_write_b32 a[%c2], %0" : "=&v"(t) : "v"(f), "i"(I)); }
template <int I> DI void a_qmov() { asm volatile("v_accvgpr_mov_b32 a[%c0], a[%c1]" :: "i"(128 + I), "i"(192 + I)); }
template <int I> DI float a_read() { float t; asm volatile("v_accvgpr_read_b32 %0, a[%c1]" : "=v"(t) : "i"(I)); return t; }
template <int F> DI void q_load(unsigned voff, const char* g) {
    asm volatile("global_load_dwordx4 a[%c2:%c3], %0, %1 offset:%c4" :: "v"(voff), "s"(g + ((F & 15) >> 2) * 4096), "i"(128 + 4 * F), "i"(131 + 4 * F), "i"((F & 3) * 1024) : "memory"); }
template <int K> DI void pk_store(const AT& c, const u32x4 pk) {
    asm volatile("global_store_dwordx4 %0, %1, %2 offset:%c3 sc1\n\ts_nop 1" :: "v"(c.o.cv), "v"(pk), "s"(c.part + (K >> 2) * 4096), "i"((K & 3) * 1024) : "memory"); }
template <int K> DI void p_load(const AT& c) {
    asm volatile("global_load_dwordx4 a[%c2:%c3], %0, %1 offset:%c4 sc1" :: "v"(c.o.cv), "s"(c.part + (K >> 2) * 4096), "i"(192 + 4 * K), "i"(195 + 4 * K), "i"((K & 3) * 1024) : "memory"); }
DI void ml_load(AT& c) { asm volatile("global_load_dwordx2 %0, %1, %2 sc1" : "+v"(c.ml2) : "v"((c.o.cv & 496u) >> 1), "s"(c.mlp) : "memory"); }
template <int I> DI void op_wpiece(const OPJ& o) {
    constexpr int S = I >> 3, H = (I & 4) * 1024;
    dma_piece(o.lds0 + op_slot(S) + H, o.cv, o.wbase + S * 32768 + H, I & 3);
}
template <int DB> DI void pv_new(const bf16x8& v, const u32x4& p) {
    asm volatile("v_mfma_f32_32x32x16_bf16 a[%c2:%c3], %0, %1, 0" :: "v"(v), "v"(p), "i"(16 * DB), "i"(16 * DB + 15)); }
template <int... Is> DI void a_zeros(iseq<Is...>) { (a_zero<Is>(), ...); }
template <int... Is> DI void a_scales(float f, iseq<Is...>) { (a_scale<Is>(f), ...); }
template <int... Is> DI void a_qmovs(iseq<Is...>) { (a_qmov<Is>(), ...); }
template <int B, int... Is> DI void q_loads(unsigned voff, const char* g, iseq<Is...>) { (q_load<B + Is>(voff, g), ...); }
template <int... Is> DI void p_loads(const AT& c, iseq<Is...>) { (p_load<Is>(c), ...); }
template <int B, int... Is> DI void a_reads(float (&o)[16], iseq<Is...>) { ((o[Is] = a_read<B + Is>()), ...); }

template <int KS, int R> DI bf16x8 kfrag(const AT& c) { return ldsrd16(c.kb + KS * 32768 + ((R & 1) * 16 + (R >> 1)) * 1024); }
template <int R, bool DBM = false> DI bf16x8 vfrag(ldscp v) {
    return ldsrd16(v + (DBM ? (R >> 2) * 4096 + (R & 3) * 1024 : (R & 7) * 4096 + (R >> 3) * 1024)); }
template <int Q> DI void pk_quad(AT& c) {
    const float t0 = a_read<4 * Q>(), t1 = a_read<4 * Q + 1>(), t2 = a_read<4 * Q + 2>(), t3 = a_read<4 * Q + 3>();
    if constexpr (Q & 1) pk_store<(Q >> 1)>(c, (u32x4){c.pk[0], c.pk[1], cvtpk(t0, t1), cvtpk(t2, t3)});
    else { c.pk = (u32x2){cvtpk(t0, t1), cvtpk(t2, t3)}; PIN(c.pk); }
}
template <int X, int E> DI void exp_sum(AT& c) {
    const float e = ex2(c.s[X][E >> 4][E & 15]); c.s[X][E >> 4][E & 15] = e;
    if constexpr (E & 1) c.rs1 += e; else c.rs0 += e;
}
template <int X, int W> DI void pack1(AT& c) {
    c.p[W >> 2][W & 3] = cvtpk(c.s[X][W >> 3][(2 * W) & 15], c.s[X][W >> 3][(2 * W + 1) & 15]);
}
template <int X, int MODE, int N> DI void gapA(AT& c) {
    constexpr int d0 = N >> 1, kb = N & 1, Y = X ^ 1;
    if constexpr (d0 == 0) qk_init<0>(c.s[Y][kb], c.fr[N & 7], c.cm[(MODE & 4) ? kb : 0]);
    else qk_acc<d0>(c.s[Y][kb], c.fr[N & 7]);
    constexpr int R = N + PD;
    if constexpr (R < 32) c.fr[R & 7] = kfrag<Y, R>(c); else if constexpr (MODE & 1) c.fr[R & 7] = vfrag<R - 32, (MODE & 18) == 18>(c.vcur);
    if constexpr (MODE & 1) {
        if constexpr (N < 32 - NEB) { exp_sum<X, NEB + N>(c); PIN(c.rs0); PIN(c.rs1); }
        if constexpr (N >= 10 && N < 26) { pack1<X, N - 10>(c); PIN(c.p[(N - 10) >> 2]); }
    }
    if constexpr ((MODE & 4) && N == 2) asm volatile("" :: "v"(c.cm[0]), "v"(c.cm[1]));
    if constexpr ((MODE & 18) == 16) pk_quad<N>(c);
    if constexpr ((MODE & 18) == 18 && N >= 24) pk_quad<N - 24>(c);
    if constexpr ((MODE & 2) && (N & 1)) {
        if constexpr (N < 16) dma8<(N >> 1)>(c.m0k, c.goff, c.gk); else dma8<((N - 16) >> 1)>(c.m0v, c.goff, c.gv);
    }
    SBAR();
}
template <int X> DI void decide(AT& c) {
    constexpr int Y = X ^ 1;
    float mm = MX2(c.mx0, c.mx1);
    auto rr = __builtin_amdgcn_permlane32_swap(__float_as_uint(mm), __float_as_uint(mm), false, false);
    const float rm = MX2(__uint_as_float(rr[0]), __uint_as_float(rr[1]));
    if (__builtin_expect(c.first || __any(rm > THR), 0)) {
        const float dl = c.first ? rm : MX2(rm, 0.f);
        c.m += dl; c.alpha = ex2(-dl); c.pend = !c.first;
        const float nd = -dl;
        add_bcast(c.s[Y][0], c.aone, nd); add_bcast(c.s[Y][1], c.aone, nd); add_bcast(c.cm[0], c.aone, nd);
        asm volatile("s_nop 7\n\ts_nop 7\n\ts_nop 3" : "+v"(c.s[Y][0]), "+v"(c.s[Y][1]), "+v"(c.cm[0]));
    }
}
template <int X, int I> DI void maxstep(AT& c) {
    constexpr int Y = X ^ 1, h = I >> 2, r = 4 * (I & 3);
    if constexpr (I == 0) { c.mx0 = MX2(c.s[Y][0][0], c.s[Y][0][1]); c.mx1 = MX2(c.s[Y][0][2], c.s[Y][0][3]); }
    else { c.mx0 = MX3(c.mx0, c.s[Y][h][r], c.s[Y][h][r + 1]); c.mx1 = MX3(c.mx1, c.s[Y][h][r + 2], c.s[Y][h][r + 3]); }
}
template <int X, int MODE, int N> DI void gapB(AT& c) {
    constexpr bool DBM = (MODE & 18) == 18;
    constexpr int ks = DBM ? N & 3 : N >> 3, db = DBM ? N >> 2 : N & 7, Y = X ^ 1;
    if constexpr ((MODE & 16) && ks == 0) pv_new<db>(c.fr[N & 7], c.p[ks]); else pv_acc<db>(c.fr[N & 7], c.p[ks]);
    if constexpr (DBM && N < 24) pk_quad<N + 8>(c);
    if constexpr ((MODE & 32) && N == 10) {
        if (c.isA) {
            asm volatile("s_waitcnt vmcnt(0)" ::: "memory");
            c.early = rfl(__float_as_uint(a_read<192>())) != 0u;
            __builtin_amdgcn_fence(__ATOMIC_ACQUIRE, "wavefront");
            if (c.early) ml_load(c);
        }
    }
    if constexpr ((MODE & 32) && N > 10 && N < 27) { if (c.early) p_load<N - 11>(c); }
    if constexpr ((MODE & 48) == 48 && N == 16) { asm volatile("s_waitcnt vmcnt(0)" ::: "memory"); __hip_atomic_store(c.flag, 1u, RLX_AGENT); }
    if constexpr ((MODE & 48) == 32 && N == 28) asm volatile("global_load_dword a128, %0, %1 sc1" :: "v"(0u), "s"(c.flag) : "memory");
    if constexpr (MODE & 64) op_wpiece<N>(c.o);
    constexpr int R = N + PD;
    if constexpr (R < 32) c.fr[R & 7] = vfrag<R, DBM>(c.vcur);
    if constexpr (MODE & 1) {
        if constexpr (MODE & 2) {
            if constexpr (N == 28) { c.fr[0] = kfrag<X, 0>(c); c.fr[1] = kfrag<X, 1>(c); }
            if constexpr (N == 29) { c.fr[2] = kfrag<X, 2>(c); c.fr[3] = kfrag<X, 3>(c); }
            if constexpr (N == 30) { c.fr[4] = kfrag<X, 4>(c); }
            if constexpr (N == 31) { c.fr[5] = kfrag<X, 5>(c); }
        }
        if constexpr (N == 1) { asm volatile("s_nop 7" : "+v"(c.s[Y][0]), "+v"(c.s[Y][1])); }
        if constexpr (N >= 2 && N < 6) { maxstep<X, 2 * (N - 2)>(c); maxstep<X, 2 * (N - 2) + 1>(c); PIN(c.mx0); PIN(c.mx1); }
        if constexpr (N == 6) { c.l += c.rs0 + c.rs1; c.rs0 = 0.f; c.rs1 = 0.f; PIN(c.l); }
        if constexpr (N == 7) decide<X>(c);
        if constexpr (N >= 8) { exp_sum<Y, N - 8>(c); PIN(c.rs0); PIN(c.rs1); }
    }
    SBAR();
    if constexpr ((MODE & 1) && N == 27) {
        if constexpr (MODE & 2) {
            if (X == 0 && c.drain) asm volatile("s_waitcnt vmcnt(0)" ::: "memory");
            if constexpr (MODE & 16) asm volatile("s_waitcnt vmcnt(24) lgkmcnt(0)\n\ts_barrier" ::: "memory");
            else asm volatile("s_waitcnt vmcnt(8) lgkmcnt(0)\n\ts_barrier" ::: "memory");
        } else {
            if (!c.isA) asm volatile("s_waitcnt vmcnt(0)" ::: "memory");
            asm volatile("s_waitcnt lgkmcnt(0)\n\ts_barrier" ::: "memory");
        }
        SBAR();
    }
}
template <int X, int MODE, int... Ns> DI void phaseA(AT& c, iseq<Ns...>) { (gapA<X, MODE, Ns>(c), ...); }
template <int X, int MODE, int... Ns> DI void phaseB(AT& c, iseq<Ns...>) { (gapB<X, MODE, Ns>(c), ...); }
template <int X, int B, int... Es> DI void exps_from(AT& c, iseq<Es...>) { (exp_sum<X, B + Es>(c), ...); }
template <int X, int... Ws> DI void packs(AT& c, iseq<Ws...>) { (pack1<X, Ws>(c), ...); }
template <int... Is> DI void maxsteps1(AT& c, iseq<Is...>) { (maxstep<1, Is>(c), ...); }
DI void rescale_o(AT& c) {
    if (__builtin_expect(c.pend, 0)) {
        NOPS16();
        a_scales(c.alpha, make_iseq<128>{});
        NOPS2();
        c.l *= c.alpha; c.pend = false;
    }
}
DI float halfsum(float x) { auto rr = __builtin_amdgcn_permlane32_swap(__float_as_uint(x), __float_as_uint(x), false, false); return __uint_as_float(rr[0]) + __uint_as_float(rr[1]); }
DI void build_mask(AT& c, int mrel) {
    const float NEG = -__builtin_inff(), nm = -c.m;
#pragma unroll
    for (int r = 0; r < 16; ++r) { const int kk = (r & 3) + 8 * (r >> 2); c.cm[0][r] = kk > mrel ? NEG : nm; c.cm[1][r] = kk + 32 > mrel ? NEG : nm; }
    PIN(c.cm[0]); PIN(c.cm[1]); NOPS2();
}

struct ATW {
    const char *Kh, *Vh; ldsp lds; unsigned mk, mv; int n1, vs, lane, wid, r32, hi, qrel; bool isA;
};
DI int tile_of(const ATW& w, int g) { return w.isA ? w.n1 + g : (g < w.n1 ? g : g - w.n1); }
template <int X, bool DMA> DI void setup(AT& c, const ATW& w, int g) {
    c.vcur = w.lds + AT_V + w.vs * 32768 + w.lane * 16;
    if constexpr (DMA) {
        const int v2 = w.vs == 0 ? 2 : w.vs - 1, tn = tile_of(w, g + 2);
        c.gk = w.Kh + (size_t)tn * 32768; c.gv = w.Vh + (size_t)tn * 32768; c.m0k = w.mk + X * 32768; c.m0v = w.mv + v2 * 32768;
    }
}
template <bool LAST, bool seam> DI void pair(AT& c, ATW& w, int g0) {
    setup<0, true>(c, w, g0);
    c.first = seam;
    const float m_old = c.m;
    if constexpr (seam) { a_qmovs(make_iseq<64>{}); NOPS2(); }
    int qr = w.qrel;
    if constexpr (LAST) {
        if constexpr (seam) asm volatile("; last pair, two segments" : "+v"(qr), "+v"(c.s[0][0]), "+v"(c.s[0][1]), "+v"(c.m));
        else asm volatile("; last pair" : "+v"(qr), "+v"(c.s[0][0]), "+v"(c.s[0][1]), "+v"(c.m));
        build_mask(c, qr);
    }
    SBAR();
    phaseA<0, LAST ? 7 : 3>(c, make_iseq<32>{});
    phaseB<0, 3>(c, make_iseq<32>{});
    rescale_o(c);
    if (c.drain) { __hip_atomic_store(c.flag, 1u, RLX_AGENT); c.drain = false; }
    c.first = false;
    w.vs = w.vs == 2 ? 0 : w.vs + 1;
    setup<1, !LAST>(c, w, g0 + 1);
    if constexpr (LAST || seam) asm volatile("" : "+s"(c.part));
    if constexpr (LAST) {
        asm volatile("global_load_dword a192, %0, %1 sc1" :: "v"(0u), "s"(c.flag) : "memory");
        PIN(qr); build_mask(c, qr - 64);
    }
    if constexpr (seam) {
        const float lt = halfsum(c.l);
        __hip_atomic_store((unsigned long long*)c.mlp + w.r32, ((unsigned long long)__float_as_uint(lt) << 32) | __float_as_uint(m_old), RLX_AGENT);
        c.l = 0.f;
        asm volatile("; second segment" : "+v"(c.s[1][0]), "+v"(c.s[1][1]), "+v"(c.rs0), "+v"(c.rs1), "+v"(c.l));
        SBAR();
        phaseA<1, (LAST ? 5 : 3) | 16>(c, make_iseq<32>{});
        phaseB<1, (LAST ? 33 : 3) | 16>(c, make_iseq<32>{});
        if constexpr (!LAST) c.drain = true;
    } else {
        SBAR();
        phaseA<1, LAST ? 5 : 3>(c, make_iseq<32>{});
        phaseB<1, LAST ? 33 : 3>(c, make_iseq<32>{});
    }
    rescale_o(c);
    w.vs = w.vs == 2 ? 0 : w.vs + 1;
}

template <int F> DI void op_cload(const OPJ& o) {
    asm volatile("global_load_dwordx4 a[%c2:%c3], %0, %1 offset:%c4 sc1" :: "v"(o.cv), "s"(o.cbase + (F >> 2) * 4096),
                 "i"(4 * (F & 63)), "i"(4 * (F & 63) + 3), "i"((F & 3) * 1024) : "memory"); }
template <int B, int... Is> DI void op_cloads(const OPJ& o, iseq<Is...>) { (op_cload<B + Is>(o), ...); }
template <int F> DI void op_mfma(f32x4& acc, const bf16x8& wf) {
    asm volatile("v_mfma_f32_16x16x32_bf16 %0, %1, a[%c2:%c3], %0" : "+v"(acc) : "v"(wf), "i"(4 * (F & 63)), "i"(4 * (F & 63) + 3)); }
DI void op_need(OPJ& o, unsigned mask) {
    if (__builtin_expect((o.ready & mask) != mask, 0)) {
        for (unsigned spins = 0; spins < (1u << 22); ++spins) {
            const unsigned v = __hip_atomic_load(o.fl + ((o.cv >> 4) & 15), RLX_AGENT);
            o.ready = (unsigned)__ballot(v != 0u);
            if ((o.ready & mask) == mask) break;
            __builtin_amdgcn_s_sleep(1);
        }
        __builtin_amdgcn_fence(__ATOMIC_ACQUIRE, "wavefront");
    }
}
template <int R> DI bf16x8 op_wfrag(const OPJ& o) { return ldsrd16(o.wp + op_slot(R >> 5) + (R & 31) * 1024); }
template <int I, class E> DI void op_step(OPJ& o, f32x4 (&acc)[4], bf16x8 (&wf)[16], E& epi) {
    constexpr int S = I >> 3;
    if constexpr ((I & 7) == 0) {
        if constexpr (S == 0) asm volatile("s_waitcnt vmcnt(24)\n\ts_barrier" ::: "memory");
        else if constexpr (S == 2) asm volatile("s_waitcnt vmcnt(40)\n\ts_barrier" ::: "memory");
        else if constexpr (S <= 12) asm volatile("s_waitcnt vmcnt(32)\n\ts_barrier" ::: "memory");
        else if constexpr (S == 13) asm volatile("s_waitcnt vmcnt(16)\n\ts_barrier" ::: "memory");
        else if constexpr (S == 14) { asm volatile("s_waitcnt vmcnt(0)\n\ts_barrier" ::: "memory"); epi(); }
        if constexpr (S < 12) op_need(o, 1u << (S + 4));
        SBAR();
    }
    op_mfma<I>(acc[0], wf[(4 * I) & 15]);
    if constexpr (I + 2 < 128) { wf[(4 * I + 8) & 15] = op_wfrag<4 * I + 8>(o); wf[(4 * I + 9) & 15] = op_wfrag<4 * I + 9>(o); }
    op_mfma<I>(acc[1], wf[(4 * I + 1) & 15]);
    if constexpr (S < 12) op_cload<I + 32>(o);
    op_mfma<I>(acc[2], wf[(4 * I + 2) & 15]);
    if constexpr (I + 2 < 128) { wf[(4 * I + 10) & 15] = op_wfrag<4 * I + 10>(o); wf[(4 * I + 11) & 15] = op_wfrag<4 * I + 11>(o); }
    op_mfma<I>(acc[3], wf[(4 * I + 3) & 15]);
    if constexpr (S < 12) op_wpiece<I + 32>(o);
    SBAR();
}
template <class E, int... Is> DI void op_steps(OPJ& o, f32x4 (&acc)[4], bf16x8 (&wf)[16], E& epi, iseq<Is...>) { (op_step<Is>(o, acc, wf, epi), ...); }
template <int... Rs> DI void op_wfrags(const OPJ& o, bf16x8 (&wf)[16], iseq<Rs...>) { ((wf[Rs] = op_wfrag<Rs>(o)), ...); }

template <bool MERGE, int D> DI void ctx_out(float f1, float f2, const __amdgpu_buffer_rsrc_t crs, int voff) {
    float o[16], p[16];
    a_reads<16 * D>(o, make_iseq<16>{});
    if constexpr (MERGE) a_reads<192 + 8 * D>(p, make_iseq<8>{});
#pragma unroll
    for (int s2 = 0; s2 < 2; ++s2) {
        u32x4 wv;
#pragma unroll
        for (int e = 0; e < 4; ++e) { const int r = 8 * s2 + 2 * e;
            if constexpr (MERGE) { const unsigned pw = __float_as_uint(p[r >> 1]); wv[e] = cvtpk(o[r] * f1 + bf_lo(pw) * f2, o[r + 1] * f1 + bf_hi(pw) * f2); }
            else wv[e] = cvtpk(o[r] * f1, o[r + 1] * f1); }
        __builtin_amdgcn_raw_buffer_store_b128(wv, crs, voff + D * 1024 + s2 * 512, 0, 16);
    }
}
template <bool MERGE, int... Ds> DI void ctx_outs(float f1, float f2, const __amdgpu_buffer_rsrc_t crs, int voff, iseq<Ds...>) { (ctx_out<MERGE, Ds>(f1, f2, crs, voff), ...); }

__global__ __launch_bounds__(256, 1) void k2_attn(unsigned char* ws, const bf16_t* x, bf16_t* out) {
    extern __shared__ __attribute__((aligned(16))) unsigned char shm[];
    ldsp lds = (ldsp)shm;
    const int tid = threadIdx.x, wid = rfl(tid >> 6), lane = tid & 63, r32 = lane & 31, hi = lane >> 5;
    const int b = blockIdx.x, xcd = b & 7, j = b >> 3, head = 2 * xcd + (j >> 4), w16 = j & 15;
    const bool isA = w16 >= 8; const int i = w16 & 7, bq = 15 - i;
    const int seg = head * 8 + i;
    ATW w;
    w.Kh = (const char*)ws + WS_K + (size_t)head * MB; w.Vh = (const char*)ws + WS_V + (size_t)head * MB;
    const char* Qh = (const char*)ws + WS_Q + (size_t)head * MB;
    w.lds = lds; w.n1 = 15 - 2 * i; w.vs = 0; w.lane = lane; w.wid = wid; w.r32 = r32; w.hi = hi; w.isA = isA; w.qrel = wid * 32 + r32 - 4 * hi;
    AT c;
    const unsigned lds0 = (unsigned)(uintptr_t)lds;
    c.part = (const char*)ws + WS_PART + (size_t)(seg * 4 + wid) * 16384; c.mlp = (const char*)ws + WS_ML + (seg * 4 + wid) * 256;
    c.flag = (unsigned*)(ws + WS_FLAGS) + seg * FLAG_STRIDE + wid;
    c.isA = isA; c.early = false; c.ml2 = (f32x2){0.f, 0.f};
    OPJ& o = c.o;
    o.cv = (unsigned)lane * 16u;
    c.goff = (unsigned)wid * 8192u + (unsigned)lane * 16u;
    c.kb = lds + AT_K + lane * 16;
    w.mk = lds0 + AT_K + wid * 8192; w.mv = lds0 + AT_V + wid * 8192;
    TS(ws, 1, 0);
    asm volatile("s_nop 4" ::: ACLOB);
    dma_tile(w.mk, c.goff, w.Kh + (size_t)tile_of(w, 0) * 32768);
    q_loads<0>((unsigned)lane * 16u, Qh + (size_t)(bq * 4 + wid) * 16384, make_iseq<16>{});
    dma_tile(w.mk + 32768, c.goff, w.Kh + (size_t)tile_of(w, 1) * 32768);
    dma_tile(w.mv, c.goff, w.Vh + (size_t)tile_of(w, 0) * 32768);
    if (wid == 3) CODE_TOUCH2(K2_SYM, lds0 + AT_V + 65536, j & 7, lane);
    const bool q2now = !isA && w.n1 <= 3;
    const char* q2p = Qh + (size_t)(i * 4 + wid) * 16384;
    if (q2now) q_loads<16>((unsigned)lane * 16u, q2p, make_iseq<16>{});
    a_zeros(make_iseq<128>{});
    c.cm[0] = f32x16{}; c.cm[1] = f32x16{}; c.m = 0.f; c.l = 0.f; c.rs0 = 0.f; c.rs1 = 0.f; c.alpha = 1.f; c.pend = false; c.drain = false;
    c.aone = hi ? 0.f : 1.f;
    PIN(c.cm[0]);
    if (q2now) asm volatile("s_waitcnt vmcnt(32)" ::: "memory"); else asm volatile("s_waitcnt vmcnt(16)" ::: "memory");
    asm volatile("s_barrier" ::: "memory");
    SBAR();
    c.fr[0] = kfrag<0, 0>(c); c.fr[1] = kfrag<0, 1>(c); c.fr[2] = kfrag<0, 2>(c); c.fr[3] = kfrag<0, 3>(c); c.fr[4] = kfrag<0, 4>(c); c.fr[5] = kfrag<0, 5>(c);
    SBAR();
    phaseA<1, 0>(c, make_iseq<32>{});
    dma_tile(w.mv + 32768, c.goff, w.Vh + (size_t)tile_of(w, 1) * 32768);
    SBAR(); asm volatile("s_nop 7\n\ts_nop 7" : "+v"(c.s[0][0]), "+v"(c.s[0][1])); SBAR();
    c.first = true;
    maxsteps1(c, make_iseq<8>{});
    decide<1>(c);
    exps_from<0, 0>(c, make_iseq<NEB>{});
    asm volatile("s_waitcnt vmcnt(8) lgkmcnt(0)\n\ts_barrier" ::: "memory");
    SBAR();
    c.fr[0] = kfrag<1, 0>(c); c.fr[1] = kfrag<1, 1>(c); c.fr[2] = kfrag<1, 2>(c); c.fr[3] = kfrag<1, 3>(c); c.fr[4] = kfrag<1, 4>(c); c.fr[5] = kfrag<1, 5>(c);
    TS(ws, 1, 1);
    const int q2at = isA || q2now ? -1 : 2;
    for (int g0 = 0, gstop = isA ? 14 : w.n1 - 1; ; g0 += 2, gstop = 14) {
        for (; g0 < gstop; g0 += 2) {
            if (g0 == q2at) q_loads<16>(c.o.cv, q2p, make_iseq<16>{});
            pair<false, false>(c, w, g0);
        }
        if (g0 >= 14) break;
        pair<false, true>(c, w, g0);
    }
    TS(ws, 1, 2);
    if (!isA && w.n1 == 15) pair<true, true>(c, w, 14); else pair<true, false>(c, w, 14);
    TS(ws, 1, 3);
    int b2 = b; asm volatile("" : "+s"(b2), "+s"(ws));
    const int j2 = b2 >> 3, jj = j2 - (j2 > 7) - (j2 > 8) - (j2 > 23) - (j2 > 24), rb = 4 * (b2 & 7) + jj / 7, cn = jj % 7, fr = lane & 15, fq = lane >> 4;
    const bool job = (j2 & 15) != 7 && (j2 & 15) != 8;
    unsigned* flags = (unsigned*)(ws + WS_FLAGS) + N_AFLAGS * FLAG_STRIDE;
    o.wbase = (const char*)ws + WS_WI + (size_t)cn * 524288 + wid * 8192;
    o.cbase = (const char*)ws + WS_CTX + (size_t)(rb * 4 + wid) * 131072;
    o.lds0 = lds0 + wid * 8192; o.wp = lds + lane * 16; o.ws = ws; o.fl = flags + (rb >> 1) * FLAG_STRIDE; o.ready = 0u;
    c.vcur = lds + AT_V + 32768 + lane * 16;
    c.fr[0] = vfrag<0>(c.vcur); c.fr[1] = vfrag<1>(c.vcur); c.fr[2] = vfrag<2>(c.vcur); c.fr[3] = vfrag<3>(c.vcur); c.fr[4] = vfrag<4>(c.vcur); c.fr[5] = vfrag<5>(c.vcur);
    SBAR();
    exps_from<0, NEB>(c, make_iseq<32 - NEB>{});
    packs<0>(c, make_iseq<16>{});
    c.l += c.rs0 + c.rs1;
    PIN(c.p[0]); PIN(c.p[1]); PIN(c.p[2]); PIN(c.p[3]);
    SBAR();
    if (isA && !c.early) {
        asm volatile("s_waitcnt vmcnt(0)" ::: "memory");
        c.early = rfl(__float_as_uint(a_read<128>())) != 0u;
        __builtin_amdgcn_fence(__ATOMIC_ACQUIRE, "wavefront");
        if (c.early) { ml_load(c); p_loads(c, make_iseq<16>{}); }
    }
    SBAR(); NOPS2(); SBAR();
    phaseB<0, 64>(c, make_iseq<32>{});
    SBAR();
    if (isA && !c.early) {
        unsigned spins = 0;
        while (rfl(__hip_atomic_load(c.flag, RLX_AGENT)) == 0u) { __builtin_amdgcn_s_sleep(1); if (++spins > (1u << 22)) break; }
        __builtin_amdgcn_fence(__ATOMIC_ACQUIRE, "wavefront");
        ml_load(c);
        p_loads(c, make_iseq<16>{});
        asm volatile("s_waitcnt vmcnt(0)" : "+v"(c.ml2) :: "memory");
    }
    const float lt = halfsum(c.l);
    TS(ws, 1, 4);
    {
        const __amdgpu_buffer_rsrc_t crs = __builtin_amdgcn_make_buffer_rsrc(ws + WS_CTX, (short)0, (int)(16 * MB), 0x00020000);
        const int row = (isA ? bq : i) * 128 + wid * 32 + r32;
        const int voff = ((row >> 4) * 128 + head * 8) * 1024 + hi * 256 + (row & 15) * 16;
        if (isA) {
            asm volatile("s_waitcnt vmcnt(32)" : "+v"(c.ml2) :: "memory");
            const float mn = __builtin_fmaxf(c.m, c.ml2[0]);
            const float w1 = ex2(c.m - mn), w2 = ex2(c.ml2[0] - mn);
            const float inv = 1.0f / (lt * w1 + c.ml2[1] * w2);
            SBAR(); NOPS16(); SBAR();
            ctx_outs<true>(w1 * inv, w2 * inv, crs, voff, make_iseq<8>{});
        } else {
            const float inv = 1.0f / lt;
            SBAR(); NOPS16(); SBAR();
            ctx_outs<false>(inv, 0.f, crs, voff, make_iseq<8>{});
        }
    }
    TS(ws, 1, 5);
    f32x4 acc[4];
#pragma unroll
    for (int f = 0; f < 4; ++f) { acc[f] = (f32x4){0.f, 0.f, 0.f, 0.f}; PIN(acc[f]); }
    asm volatile("s_waitcnt vmcnt(0)\n\ts_barrier" ::: "memory");
    if (tid == 0) __hip_atomic_store(flags + (isA ? bq : i) * FLAG_STRIDE + head, 1u, RLX_AGENT);
    TS(ws, 1, 7);
    TS(ws, 2, 0);
    if (!job) return;
    const int trow = rb * 64 + wid * 16 + fr, tcol = cn * 64 + 4 * fq;
    u32x2 xw[4], yw[4]; float g;
    auto epi = [&]() {
        const bf16_t* y2p = (const bf16_t*)(ws + WS_Y2);
#pragma unroll
        for (int f = 0; f < 4; ++f) {
            xw[f] = *(const u32x2*)(x + (size_t)trow * DM + tcol + f * 16);
            const bf16_t* yp = y2p + (size_t)(tcol + f * 16) * SEQ + trow;
            yw[f] = (u32x2){yp[0] | (unsigned)yp[SEQ] << 16, yp[2 * SEQ] | (unsigned)yp[3 * SEQ] << 16};
        }
        g = ((const float*)(ws + WS_G))[trow];
    };
    bf16x8 wf[16];
    op_wfrags(o, wf, make_iseq<8>{});
    SBAR();
    op_need(o, 1u); op_cloads<0>(o, make_iseq<8>{});
    op_need(o, 2u); op_cloads<8>(o, make_iseq<8>{});
    op_need(o, 4u); op_cloads<16>(o, make_iseq<8>{});
    op_need(o, 8u); op_cloads<24>(o, make_iseq<8>{});
    TS(ws, 2, 4);
    SBAR();
    op_steps(o, acc, wf, epi, make_iseq<128>{});
#pragma unroll
    for (int f = 0; f < 4; ++f) PIN(acc[f]);
    NOPS16();
#pragma unroll
    for (int f = 0; f < 4; ++f) PIN(acc[f]);
    TS(ws, 2, 1);
    const float og = rbf(1.0f - g);
#pragma unroll
    for (int f = 0; f < 4; ++f) {
        const float xv[4] = {bf_lo(xw[f][0]), bf_hi(xw[f][0]), bf_lo(xw[f][1]), bf_hi(xw[f][1])};
        const float yv[4] = {bf_lo(yw[f][0]), bf_hi(yw[f][0]), bf_lo(yw[f][1]), bf_hi(yw[f][1])};
        float r[4];
#pragma unroll
        for (int e = 0; e < 4; ++e) r[e] = rbf(g * rbf(xv[e] * yv[e])) + rbf(og * rbf(acc[f][e]));
        u32x2 wv = {cvtpk(r[0], r[1]), cvtpk(r[2], r[3])};
        *(u32x2*)(out + (size_t)trow * DM + tcol + f * 16) = wv;
    }
    TS(ws, 2, 7);
}

extern "C" __device__ __attribute__((used, noinline)) void text_end_mark() { asm volatile("s_nop 0"); }

extern "C" void kernel_launch(void* const* d_in, const int* in_sizes, int n_in, void* d_out, int out_size, void* d_ws, size_t ws_size,
                              hipStream_t stream) {
    (void)in_sizes; (void)n_in; (void)out_size; (void)ws_size;
    (void)hipFuncSetAttribute((const void*)k1_proj, hipFuncAttributeMaxDynamicSharedMemorySize, K1_LDS);
    (void)hipFuncSetAttribute((const void*)k2_attn, hipFuncAttributeMaxDynamicSharedMemorySize, AT_LDS);
    K1Args P;
    P.x = (const bf16_t*)d_in[0]; P.ak = (const bf16_t*)d_in[1]; P.av = (const bf16_t*)d_in[2]; P.shift_k = (const bf16_t*)d_in[3];
    P.ssm_k = (const bf16_t*)d_in[4]; P.ssm_d = (const bf16_t*)d_in[5]; P.ckv = (const bf16_t*)d_in[7]; P.wq = (const bf16_t*)d_in[8];
    P.wuk = (const bf16_t*)d_in[9]; P.wuv = (const bf16_t*)d_in[10]; P.gate_w = (const bf16_t*)d_in[12]; P.wo = (const bf16_t*)d_in[11]; P.ws = (unsigned char*)d_ws;
    k1_proj<<<256, 512, K1_LDS, stream>>>(P);
    k2_attn<<<256, 256, AT_LDS, stream>>>((unsigned char*)d_ws, (const bf16_t*)d_in[0], (bf16_t*)d_out);
}
```

```cpp
#include <hip/hip_runtime.h>
#include <hip/hip_bf16.h>
#include <stdint.h>
#include <utility>

#define LAS __attribute__((address_space(3)))
#define GAS __attribute__((address_space(1)))
#define CAS __attribute__((address_space(4)))
#define DI __device__ __forceinline__
typedef unsigned short bf16_t;
typedef short bf16x8 __attribute__((ext_vector_type(8)));
typedef short s16x4 __attribute__((ext_vector_type(4)));
typedef float f32x2 __attribute__((ext_vector_type(2)));
typedef float f32x4 __attribute__((ext_vector_type(4)));
typedef float f32x16 __attribute__((ext_vector_type(16)));
typedef unsigned u32x2 __attribute__((ext_vector_type(2)));
typedef unsigned u32x4 __attribute__((ext_vector_type(4)));
typedef __bf16 bfx2 __attribute__((ext_vector_type(2)));
typedef LAS unsigned char* ldsp;
typedef const LAS unsigned char* ldscp;

constexpr int SEQ = 2048, DM = 448, NH = 16, HD = 256, DC = 1024, NW_ = 4096;

constexpr size_t MB = 1024 * 1024;
constexpr size_t WS_Q = 0, WS_K = 16 * MB, WS_V = 32 * MB, WS_CTX = 48 * MB, WS_PART = 64 * MB, WS_ML = 80 * MB,
                 WS_Y2 = 81 * MB, WS_G = 84 * MB, WS_FLAGS = 100 * MB, WS_WI = 102 * MB;
constexpr int FLAG_STRIDE = 64;
constexpr int N_AFLAGS = 128, N_CFLAGS = 16;

#define TS(wsp, kern, slot) do { } while (0)
#define TSI(wsp, idx) do { } while (0)
#define SBAR() __builtin_amdgcn_sched_barrier(0)
#define PIN(x) asm volatile("" : "+v"(x))
#define RLX_AGENT __ATOMIC_RELAXED, __HIP_MEMORY_SCOPE_AGENT

DI unsigned cvtpk(float lo, float hi) { f32x2 v = {lo, hi}; bfx2 b = __builtin_convertvector(v, bfx2); return __builtin_bit_cast(unsigned, b); }
DI float bf_lo(unsigned w) { return __uint_as_float(w << 16); }
DI float bf_hi(unsigned w) { return __uint_as_float(w & 0xffff0000u); }
DI float rbf(float x) { return bf_lo(cvtpk(x, 0.f)); }
DI unsigned rfl(unsigned x) { return __builtin_amdgcn_readfirstlane(x); }
DI s16x4 trread(ldscp p) { return __builtin_amdgcn_ds_read_tr16_b64_v4i16((LAS s16x4*)p); }
DI bf16x8 cat8(s16x4 lo, s16x4 hi) { return __builtin_shufflevector(lo, hi, 0, 1, 2, 3, 4, 5, 6, 7); }
DI bf16x8 ldsrd16(ldscp p) { return *(const LAS bf16x8*)p; }
#define MFMA16(a, b, c) __builtin_amdgcn_mfma_f32_16x16x32_bf16((a), (b), (c), 0, 0, 0)
#define MFMA32(a, b, c) __builtin_amdgcn_mfma_f32_32x32x16_bf16((a), (b), (c), 0, 0, 0)

DI void touch(unsigned sink, unsigned voff, const char* g) {
    asm volatile("s_mov_b32 m0, %0\n\ts_nop 0\n\tglobal_load_lds_dword %1, %2" :: "s"(sink), "v"(voff), "s"(g) : "memory");
}
#define CODE_TOUCH(SYM, END, sink, chunk, lane) do { const char* _p; unsigned _n; \
        asm volatile("s_getpc_b64 s[90:91]\n\ts_add_u32 s90, s90, " SYM "@rel32@lo+4\n\ts_addc_u32 s91, s91, " SYM "@rel32@hi+12\n\ts_mov_b64 %0, s[90:91]\n\ts_mov_b32 %1, " END "-" SYM \
                     : "=s"(_p), "=s"(_n) :: "s90", "s91", "scc"); \
        const unsigned _o = ((unsigned)(chunk) * 64u + (unsigned)(lane)) * 128u; \
        if ((unsigned)(chunk) * 8192u < _n && _n < 262144u) touch((sink), _o < _n - 4u ? _o : _n - 4u, _p); } while (0)
#define K1_SYM "_Z7k1_proj6K1Args", "_Z7k2_attnPhPKtPt"
#define K2_SYM "_Z7k2_attnPhPKtPt", "text_end_mark"
#define CODE_TOUCH2(S, sink, chunk, lane) CODE_TOUCH(S, sink, chunk, lane)

constexpr int HTB = 128 * 64 * 2;
constexpr int K1_X = 8 * HTB;
constexpr int K1_LDS = K1_X + 16384 + 2048;

DI int lds_byte(int r, int c) { const int st = (r >> 4) * 2 + (c >> 5), rr = r & 15, cc = c & 31, ob = rr * 64 + cc * 2; return st * 1024 + (ob ^ (((ob >> 9) & 1) << 5)); }
DI void stage_rc(int b, int& R, int& C) { const int st = b / 1024, sb = b % 1024, swz = sb ^ (((sb >> 9) & 1) << 5); R = (st >> 1) * 16 + swz / 64; C = (st & 1) * 32 + (swz % 64) / 2; }

struct K1Args {
    const bf16_t *x, *ak, *av, *shift_k, *ssm_k, *ssm_d, *ckv, *wq, *wuk, *wuv, *gate_w, *wo;
    unsigned char* ws;
};

#define SA(b, h) (((b) * 2 + (h)) * HTB)
#define SB(b, h) ((4 + (b) * 2 + (h)) * HTB)
constexpr int ring_slot(int u) {
    switch (u & 7) { case 5: return SB(0, 0); case 6: return SA(0, 0); case 7: return SB(0, 1); case 0: return SA(0, 1);
                     case 1: return SB(1, 0); case 2: return SA(1, 0); case 3: return SB(1, 1); default: return SA(1, 1); }
}
struct G1 {
    ldsp lds; unsigned ldsw  , voffA_kv, voffA_q, voffB, sv; int aoff, boff0, boff1;
    const char *A_kv, *A_q, *B_kv, *B_q, *sbase; unsigned sstep, sdst, sdstep; int sadd;
};
DI int lane_id() { int l; asm volatile("v_mbcnt_lo_u32_b32 %0, -1, 0\n\tv_mbcnt_hi_u32_b32 %0, -1, %0" : "=v"(l)); return l; }
template <int OFF> DI void dma16(unsigned lbase, unsigned voff, const char* g) {
    asm volatile("s_add_i32 m0, %0, %c3\n\ts_nop 0\n\tglobal_load_lds_dwordx4 %1, %2" :: "s"(lbase), "v"(voff), "s"(g), "i"(OFF) : "memory", "scc");
}
template <int BUF, int STRIDE, int H> DI void stage_a(const G1& g, const char* base, unsigned voff) {
    dma16<BUF>(g.ldsw, voff, base + H * 128 * STRIDE); dma16<BUF + 8192>(g.ldsw, voff, base + (H * 128 + 64) * STRIDE);
}
template <int BUF, int H> DI void stage_b(const G1& g, const char* base) {
    dma16<BUF>(g.ldsw, g.voffB, base + H * 256); dma16<BUF + 8192>(g.ldsw, g.voffB, base + H * 256 + 32 * NW_ * 2);
}
template <int U> DI void stage_unit(const G1& g) {
    if constexpr (U < 21) {
        constexpr int t = U / 3, r = U % 3;
        if constexpr (r == 0) stage_b<ring_slot(U), 0>(g, g.B_q + (size_t)t * (64 * NW_ * 2));
        else stage_a<ring_slot(U), DM * 2, r - 1>(g, g.A_q + t * 128, g.voffA_q);
    } else {
        constexpr int t = (U - 21) >> 2, r = (U - 21) & 3;
        if constexpr ((r & 1) == 0) stage_b<ring_slot(U), (r >> 1)>(g, g.B_kv + (size_t)t * (64 * NW_ * 2));
        else stage_a<ring_slot(U), DC * 2, (r >> 1)>(g, g.A_kv + t * 128, g.voffA_kv);
    }
}
DI void side(const G1& g, int t) {
    const int tt = t + g.sadd > 15 ? 15 : t + g.sadd;
    asm volatile("s_mov_b32 m0, %0\n\ts_nop 0\n\tglobal_load_lds_dword %1, %2" :: "s"(g.sdst + (unsigned)t * g.sdstep), "v"(g.sv), "s"(g.sbase + rfl((unsigned)tt * g.sstep)) : "memory");
}
DI void lda(bf16x8 (&dst)[4][2], const G1& g, int buf) {
#pragma unroll
    for (int m = 0; m < 4; ++m)
#pragma unroll
        for (int k = 0; k < 2; ++k) dst[m][k] = ldsrd16(g.lds + buf + g.aoff + m * 2048 + k * 1024);
}
DI void ldb(bf16x8 (&dst)[2][2], const G1& g, int buf) {
#pragma unroll
    for (int n = 0; n < 2; ++n)
#pragma unroll
        for (int k = 0; k < 2; ++k) { ldscp p = g.lds + buf + (n ? g.boff1 : g.boff0) + k * 8192; dst[n][k] = cat8(trread(p), trread(p + 256)); }
}
template <bool VT> DI void mma(f32x4 (&acc)[4][2], const bf16x8 (&At)[4][2], const bf16x8 (&Bt)[2][2]) {
    __builtin_amdgcn_s_setprio(1);
#pragma unroll
    for (int m = 0; m < 4; ++m)
#pragma unroll
        for (int n = 0; n < 2; ++n)
#pragma unroll
            for (int k = 0; k < 2; ++k) acc[m][n] = VT ? MFMA16(At[m][k], Bt[n][k], acc[m][n]) : MFMA16(Bt[n][k], At[m][k], acc[m][n]);
    __builtin_amdgcn_s_setprio(0);
}
#define WAIT_V(n) asm volatile("s_waitcnt vmcnt(" #n ")" ::: "memory")
#define WAIT_L(n) asm volatile("s_waitcnt lgkmcnt(" #n ")" ::: "memory")
#define BAR __builtin_amdgcn_s_barrier()
DI void zero8(f32x4 (&a)[4][2]) {
#pragma unroll
    for (int m = 0; m < 4; ++m) { a[m][0] = (f32x4){0.f, 0.f, 0.f, 0.f}; a[m][1] = (f32x4){0.f, 0.f, 0.f, 0.f}; }
}

DI void pack_kq(const f32x4 (&acc)[4][2], u32x4* pk, float scale) {
#pragma unroll
    for (int m = 0; m < 4; ++m) {
        const f32x4 a0 = acc[m][0] * scale, a1 = acc[m][1] * scale;
        const unsigned w0 = cvtpk(a0[0], a0[1]), w1 = cvtpk(a0[2], a0[3]), x0 = cvtpk(a1[0], a1[1]), x1 = cvtpk(a1[2], a1[3]);
        auto r0 = __builtin_amdgcn_permlane16_swap(w0, x0, false, false);
        auto r1 = __builtin_amdgcn_permlane16_swap(w1, x1, false, false);
        pk[m] = (u32x4){r0[0], r1[0], r0[1], r1[1]};
    }
}
DI unsigned kq_off(int pm, int wr, int wc, int fr, int fq) {
    return (unsigned)((pm * 8 + wr * 2) * 16384 + fr * 16 + (2 * wc + (fq & 1)) * 1024 + (fq >> 1) * 512);
}
DI void pack_v(const f32x4 (&acc)[4][2], u32x4* pk) {
#pragma unroll
    for (int n = 0; n < 2; ++n)
#pragma unroll
        for (int mm = 0; mm < 2; ++mm) {
            const f32x4 a0 = acc[2 * mm][n], a1 = acc[2 * mm + 1][n];
            const unsigned w0 = cvtpk(a0[0], a0[1]), w1 = cvtpk(a0[2], a0[3]), x0 = cvtpk(a1[0], a1[1]), x1 = cvtpk(a1[2], a1[3]);
            auto r0 = __builtin_amdgcn_permlane32_swap(w0, x0, false, false);
            auto r1 = __builtin_amdgcn_permlane32_swap(w1, x1, false, false);
            pk[2 * mm + n] = (u32x4){r0[0], r1[0], r0[1], r1[1]};
        }
}
#define ST_AUX 16
struct Out1 { u32x4 pk[16]; __amdgpu_buffer_rsrc_t rs; unsigned voff; int sbj, shi; };
template <int I> DI void st_piece(const Out1& o) {
    __builtin_amdgcn_raw_buffer_store_b128(o.pk[I], o.rs, o.voff + (I & 1) * 256, (I >> 3) * 65536 + ((I >> 2) & 1) * o.sbj + ((I >> 1) & 1) * o.shi, ST_AUX);
}
template <int A, int B> DI void st_range(const Out1& o) { if constexpr (A < B) { st_piece<A>(o); st_range<A + 1, B>(o); } }

template <int N, class W> DI void qphase(const G1& g, f32x4 (&a0)[4][2], f32x4 (&a1)[4][2], bf16x8 (&At)[4][2], bf16x8 (&B0)[2][2], W& warm) {
    constexpr int t = N >> 1;
    if constexpr ((N & 1) == 0) {
        ldb(B0, g, ring_slot(3 * t)); SBAR(); lda(At, g, ring_slot(3 * t + 1));
        if constexpr (t < 5) warm(t);
        if constexpr (N >= 2) stage_unit<3 * t + 6>(g);
        if constexpr (N == 0) WAIT_V(10); else WAIT_V(8);
        WAIT_L(8); BAR; WAIT_L(0); mma<false>(a0, At, B0); BAR; SBAR();
    } else {
        lda(At, g, ring_slot(3 * t + 2));
        if constexpr (t >= 1) stage_unit<3 * t + 7>(g);
        stage_unit<3 * t + 8>(g);
        WAIT_V(8);
        if constexpr (N == 13) WAIT_L(0);
        BAR; WAIT_L(0); mma<false>(a1, At, B0); BAR; SBAR();
    }
}
template <class W, int... Ns> DI void qphases(const G1& g, f32x4 (&a0)[4][2], f32x4 (&a1)[4][2], bf16x8 (&At)[4][2], bf16x8 (&B0)[2][2], W& warm, std::integer_sequence<int, Ns...>) {
    (qphase<Ns>(g, a0, a1, At, B0, warm), ...);
}
template <int... Us> DI void stage_units(const G1& g, std::integer_sequence<int, Us...>) { (stage_unit<Us>(g), ...); }

template <bool VT, int POS, class W>
DI void mainpair(const G1& g, int t, f32x4 (&acc)[2][2][4][2], bf16x8 (&At)[4][2], bf16x8 (&B0)[2][2], bf16x8 (&B1)[2][2], W& lastside) {
    const char* a1 = g.A_kv + (t + 1) * 128; const char* a2 = a1 + 128; const char* a3 = a1 + 256;
    const char* b2 = g.B_kv + (size_t)(t + 2) * (64 * NW_ * 2); const char* b3 = b2 + 64 * NW_ * 2;
    ldb(B0, g, SB(0, 0)); SBAR(); lda(At, g, SA(0, 0));
    if constexpr (POS != 0) stage_a<SA(1, 1), DC * 2, 1>(g, a1, g.voffA_kv); else WAIT_V(18);
    WAIT_L(8); BAR; WAIT_L(0); mma<VT>(acc[0][0], At, B0); BAR; SBAR();
    ldb(B1, g, SB(0, 1));
    if constexpr (POS == 2) lastside(0); else side(g, t);
    if constexpr (POS != 2) stage_b<SB(0, 0), 0>(g, b2);
    if constexpr (POS == 0) WAIT_V(19);
    BAR; WAIT_L(0); mma<VT>(acc[0][1], At, B1); BAR;
    lda(At, g, SA(0, 1));
    if constexpr (POS != 2) stage_a<SA(0, 0), DC * 2, 0>(g, a2, g.voffA_kv);
    BAR; WAIT_L(0); mma<VT>(acc[1][0], At, B0); BAR; SBAR();
    if constexpr (POS != 2) stage_b<SB(0, 1), 1>(g, b2);
    if constexpr (POS == 0) WAIT_V(15); else if constexpr (POS == 1) WAIT_V(7); else WAIT_V(1);
    BAR; mma<VT>(acc[1][1], At, B1); BAR;
    ldb(B0, g, SB(1, 0)); SBAR(); lda(At, g, SA(1, 0));
    if constexpr (POS != 2) stage_a<SA(0, 1), DC * 2, 1>(g, a2, g.voffA_kv);
    WAIT_L(8); BAR; WAIT_L(0); mma<VT>(acc[0][0], At, B0); BAR; SBAR();
    ldb(B1, g, SB(1, 1));
    if constexpr (POS == 2) lastside(1); else side(g, t + 1);
    if constexpr (POS != 2) stage_b<SB(1, 0), 0>(g, b3);
    BAR; WAIT_L(0); mma<VT>(acc[0][1], At, B1); BAR;
    lda(At, g, SA(1, 1));
    if constexpr (POS != 2) stage_a<SA(1, 0), DC * 2, 0>(g, a3, g.voffA_kv);
    BAR; WAIT_L(0); mma<VT>(acc[1][0], At, B0); BAR; SBAR();
    if constexpr (POS != 2) { stage_b<SB(1, 1), 1>(g, b3); WAIT_V(7); }
    BAR; mma<VT>(acc[1][1], At, B1); BAR;
}

constexpr int CV_RAW = SA(0, 0);
constexpr int K1_WO = SA(0, 1);
constexpr int CV_HSTR = 4256;
constexpr int CV_H = 2 * HTB;
constexpr int CV_USTR = 4608;
constexpr int CV_U = CV_H + 8 * CV_HSTR;
constexpr int CV_R = CV_U + 2 * CV_USTR;
static_assert(CV_R + 65536 <= K1_LDS && CV_R <= K1_X, "lds");

DI const char* wo_block(const K1Args& P, int e) { const int eb = e < 224 ? e : 0, kb = eb / 7, cn = eb - kb * 7; return (const char*)P.wo + ((size_t)kb * 128 * DM + cn * 64) * 2; }
DI void tail_stage(const G1& g, const K1Args& P, int c0, int e, int wid, int lane) {
    dma16<CV_RAW>(g.ldsw, (unsigned)lane * 16u, (const char*)P.ssm_k + (size_t)c0 * (SEQ * 2) + wid * 1024);
    const char* wg = wo_block(P, e);
    const unsigned vo = (unsigned)((wid * 8 + (lane >> 3)) * DM + (lane & 7) * 8) * 2u;
    dma16<K1_WO>(g.ldsw, vo, wg); dma16<K1_WO + 8192>(g.ldsw, vo, wg + 64 * DM * 2);
}
DI void wo_image(ldsp lds, const K1Args& P, int e, int wid, int lane) {
    const int fr = lane & 15, fq = lane >> 4, kb = e / 7, cn = e - kb * 7;
    if (e < 224) {
        ldscp wp = lds + K1_WO + (wid >> 1) * 4096 + (16 * (fq >> 1) + 4 * (fq & 1) + (fr >> 2)) * 128 + (fr & 3) * 8 + (wid & 1) * 64;
        unsigned char* op = P.ws + WS_WI + (size_t)cn * 524288 + kb * 16384 + wid * 2048 + lane * 16;
#pragma unroll
        for (int cb = 0; cb < 2; ++cb) *(bf16x8*)(op + cb * 1024) = cat8(trread(wp + cb * 32), trread(wp + cb * 32 + 1024));
    }
}
DI void conv_copies(ldsp q, const u32x4 d, const u32x2 e) {
    const unsigned a0 = __builtin_amdgcn_alignbit(d[1], d[0], 16), a1 = __builtin_amdgcn_alignbit(d[2], d[1], 16), a2 = __builtin_amdgcn_alignbit(d[3], d[2], 16),
                   a3 = __builtin_amdgcn_alignbit(e[0], d[3], 16), a4 = __builtin_amdgcn_alignbit(e[1], e[0], 16);
    *(LAS u32x4*)(q) = (u32x4){a0, a1, a2, a3};
    *(LAS u32x4*)(q + CV_HSTR) = (u32x4){d[1], d[2], d[3], e[0]};
    *(LAS u32x4*)(q + 2 * CV_HSTR) = (u32x4){a1, a2, a3, a4};
    *(LAS u32x4*)(q + 3 * CV_HSTR) = (u32x4){d[2], d[3], e[0], e[1]};
}
DI void conv_frags(bf16x8 (&A)[8], bf16x8 (&B)[8], ldscp hp, ldscp up) {
#pragma unroll
    for (int v = 0; v < 8; ++v) {
        B[v] = ldsrd16(up + (7 - v) * 512);
        const s16x4 lo = *(const LAS s16x4*)(hp + v * 512), hi = *(const LAS s16x4*)(hp + v * 512 + 8);
        A[v] = cat8(lo, hi);
    }
}
DI void conv_mma(f32x4 (&acc)[8], const bf16x8 (&A)[8], const bf16x8 (&B)[8]) {
#pragma unroll
    for (int J = 0; J < 8; ++J)
#pragma unroll
        for (int v = 0; v <= J; ++v) acc[J] = MFMA16(A[v], B[J - v], acc[J]);
}
DI void conv_part(ldsp lds, const K1Args& P, int c0, const u32x4 sw, const unsigned dw, int wid, int lane, const Out1& o) {
    const int tid = wid * 64 + lane, fr = lane & 15, fq = lane >> 4;
    {
        const int cq = tid & 255;
        const u32x4 d = *(const LAS u32x4*)(lds + CV_RAW + tid * 16);
        u32x2 e = *(const LAS u32x2*)(lds + CV_RAW + tid * 16 + 16);
        const u32x4 z4 = {0u, 0u, 0u, 0u}; const u32x2 z2 = {0u, 0u};
        if (cq == 255) e = z2;
        ldsp q = lds + CV_H + (tid >> 8) * 4 * CV_HSTR + (cq + 2) * 16;
        conv_copies(q, d, e);
        if (cq == 0) { conv_copies(q - 16, z4, (u32x2){d[0], d[1]}); conv_copies(q - 32, z4, z2); }
        if (cq == 255) { conv_copies(q + 16, z4, z2); conv_copies(q + 32, z4, z2); }
    }
    SBAR(); st_range<4, 6>(o); SBAR();
    {
        ldscp kp = lds + K1_X + tid * 16;
        u32x4 a = *(const LAS u32x4*)(kp - 16);
        const u32x4 b = *(const LAS u32x4*)(kp), vr = *(const LAS u32x4*)(kp + 8192);
        if (tid == 0) a = (u32x4){0u, 0u, 0u, 0u};
        const unsigned kr[7] = {a[1], a[2], a[3], b[0], b[1], b[2], b[3]};
#pragma unroll
        for (int c = 0; c < 2; ++c) {
            const float w0 = bf_lo(sw[2 * c]), w1 = bf_hi(sw[2 * c]), w2 = bf_lo(sw[2 * c + 1]), w3 = bf_hi(sw[2 * c + 1]);
            float kc[7], kv[4];
#pragma unroll
            for (int i = 0; i < 7; ++i) kc[i] = c ? bf_hi(kr[i]) : bf_lo(kr[i]);
#pragma unroll
            for (int i = 0; i < 4; ++i) {
                float s = rbf(kc[i] * w3);
                s = rbf(s + rbf(kc[i + 1] * w2));
                s = rbf(s + rbf(kc[i + 2] * w1));
                s = rbf(s + rbf(kc[i + 3] * w0));
                kv[i] = s * (c ? bf_hi(vr[i]) : bf_lo(vr[i]));
            }
            u32x2 wv = {cvtpk(kv[3], kv[2]), cvtpk(kv[1], kv[0])};
            *(LAS u32x2*)(lds + CV_U + c * CV_USTR + (2044 - 4 * tid) * 2) = wv;
            SBAR(); if (c == 0) st_range<6, 8>(o); else st_range<8, 10>(o); SBAR();
        }
        if (tid < 120) { const int c = tid / 60, j = tid - c * 60; *(LAS u32x2*)(lds + CV_U + c * CV_USTR + 4096 + j * 8) = (u32x2){0u, 0u}; }
    }
    __syncthreads();
    TS(P.ws, 0, 5);
    const int ch = wid >> 2, qw = wid & 3;
    {
        const int y1 = 24 + fr - 8 * fq;
        ldscp hb = lds + CV_H + ch * 4 * CV_HSTR + (y1 & 3) * CV_HSTR + (y1 & ~3) * 2 + qw * 128;
        ldscp ub = lds + CV_U + ch * CV_USTR + (2056 - 256 * 7 - 16 * fr - 8 * fq) * 2 + qw * 128;
        f32x4 acc[8];
#pragma unroll
        for (int i = 0; i < 8; ++i) acc[i] = (f32x4){0.f, 0.f, 0.f, 0.f};
        bf16x8 A0[8], B0[8], A1[8], B1[8];
        conv_frags(A0, B0, hb, ub); SBAR();
        conv_frags(A1, B1, hb + 64, ub + 64); SBAR();
        st_range<10, 12>(o); SBAR();
        conv_mma(acc, A0, B0); SBAR();
        st_range<12, 14>(o); SBAR();
        conv_mma(acc, A1, B1); SBAR();
        st_range<14, 16>(o); SBAR();
#pragma unroll
        for (int i = 0; i < 8; ++i) *(LAS f32x4*)(lds + CV_R + wid * 8192 + i * 1024 + lane * 16) = acc[i];
    }
    __syncthreads();
    TS(P.ws, 0, 6);
    {
        const float dd = ch ? bf_hi(dw) : bf_lo(dw);
#pragma unroll
        for (int i = 0; i < 2; ++i) {
            ldscp rp = lds + CV_R + ch * 32768 + (2 * qw + i) * 1024 + lane * 16;
            const f32x4 y = ((*(const LAS f32x4*)rp + *(const LAS f32x4*)(rp + 8192)) + *(const LAS f32x4*)(rp + 16384)) + *(const LAS f32x4*)(rp + 24576);
            const int t = 256 * (2 * qw + i) + 16 * fr + 4 * fq;
            const u32x2 uw = *(const LAS u32x2*)(lds + CV_U + ch * CV_USTR + (2044 - t) * 2);
            const float kv[4] = {bf_hi(uw[1]), bf_lo(uw[1]), bf_hi(uw[0]), bf_lo(uw[0])};
            float r[4];
#pragma unroll
            for (int e = 0; e < 4; ++e) r[e] = rbf(y[e]) + rbf(kv[e] * dd);
            u32x2 w = {cvtpk(r[0], r[1]), cvtpk(r[2], r[3])};
            *(u32x2*)(P.ws + WS_Y2 + ((size_t)(c0 + ch) * SEQ + t) * 2) = w;
        }
    }
}

DI void gate_part(const K1Args& P, int row0, int wid, int lane) {
    const int lc = lane < 56 ? lane : 0;
    u32x4 gw = *(const u32x4*)(P.gate_w + lc * 8);
    float* gp = (float*)(P.ws + WS_G);
    u32x4 xv[8];
#pragma unroll
    for (int r = 0; r < 8; ++r) xv[r] = *(const u32x4*)(P.x + (size_t)(row0 + wid * 8 + r) * DM + lc * 8);
    float mine = 0.f;
#pragma unroll
    for (int r = 0; r < 8; ++r) {
        float s = 0.f;
#pragma unroll
        for (int j = 0; j < 4; ++j) { s += rbf(bf_lo(xv[r][j]) * bf_lo(gw[j])); s += rbf(bf_hi(xv[r][j]) * bf_hi(gw[j])); }
        if (lane >= 56) s = 0.f;
#pragma unroll
        for (int o = 32; o >= 1; o >>= 1) s += __shfl_xor(s, o);
        if (lane == r) mine = s;
    }
    const float z = rbf(mine);
    const float g = rbf(1.0f / (1.0f + __expf(-z)));
    if (lane < 8) gp[row0 + wid * 8 + lane] = g;
}

template <bool VT>
DI void big_tile(const G1& g, const K1Args& P, int pm, int head, int c0, int e, int wid, bf16x8 (&At)[4][2], bf16x8 (&B0)[2][2], Out1& o) {
    f32x4 acc[2][2][4][2];
    zero8(acc[0][0]); zero8(acc[0][1]); zero8(acc[1][0]); zero8(acc[1][1]);
    bf16x8 B1[2][2];
    auto lastside = [&](int s) {
        if (wid < 4) side(g, 14 + s);
        else if (s == 0) {
            const unsigned l = (unsigned)lane_id();
            if (wid == 4) touch(g.sdst, l * 128u, (const char*)P.ssm_k + (size_t)c0 * (SEQ * 2));
            else touch(g.sdst, l * (DM * 2), wo_block(P, e) + (wid & 1) * (64 * DM * 2));
        }
    };
    mainpair<VT, 0>(g, 0, acc, At, B0, B1, lastside);
    for (int t = 2; t < 14; t += 2) mainpair<VT, 1>(g, t, acc, At, B0, B1, lastside);
    mainpair<VT, 2>(g, 14, acc, At, B0, B1, lastside);
    TS(P.ws, 0, 3);
    const int lane = lane_id();
    tail_stage(g, P, c0, e, wid, lane);
    if (wid == 7) CODE_TOUCH2(K2_SYM, g.sdst, e & 7, lane);
    SBAR();
    const int wr = wid >> 2, wc = wid & 3, fr = lane & 15, fq = lane >> 4;
    if (VT) {
        o.voff = (unsigned)((pm * 4 + wr) * 32768 + wc * 4096 + (fq >> 1) * 1024 + (fq & 1) * 512 + fr * 16);
        pack_v(acc[0][0], o.pk); pack_v(acc[0][1], o.pk + 4); pack_v(acc[1][0], o.pk + 8); pack_v(acc[1][1], o.pk + 12);
    } else {
        o.voff = kq_off(pm, wr, wc, fr, fq);
        pack_kq(acc[0][0], o.pk, 1.0f); pack_kq(acc[0][1], o.pk + 4, 1.0f); pack_kq(acc[1][0], o.pk + 8, 1.0f); pack_kq(acc[1][1], o.pk + 12, 1.0f);
    }
}

__global__ __launch_bounds__(512, 2) void k1_proj(K1Args P) {
    extern __shared__ __attribute__((aligned(16))) unsigned char shm[];
    ldsp lds = (ldsp)shm;
    const int b = blockIdx.x, xcd = b & 7, j = b >> 3, li = j & 15, pm = li & 7, head = 2 * xcd + (li >> 3), vt = j >> 4;
    const int tid = threadIdx.x, wid = rfl(tid >> 6), lane = tid & 63, wr = wid >> 2, wc = wid & 3, fr = lane & 15, fq = lane >> 4;
    const int c0 = (xcd * 28 + (j < 28 ? j : 0)) * 2, e = xcd * 32 + j;
    TS(P.ws, 0, 0);
    if (wid == 7) CODE_TOUCH2(K1_SYM, (unsigned)(uintptr_t)lds + K1_X + 16384 + 7 * 256, j, lane);
    u32x4 sw = *(const CAS u32x4*)(uintptr_t)(P.shift_k + c0 * 4); unsigned dw = *(const CAS unsigned*)(uintptr_t)(P.ssm_d + c0);
    G1 g;
    const unsigned lds0 = (unsigned)(uintptr_t)lds;
    g.lds = lds; g.ldsw = lds0 + (unsigned)wid * 1024u;
    { int R, C; stage_rc(tid * 16, R, C); g.voffA_kv = (unsigned)(R * DC + C) * 2u; g.voffA_q = (unsigned)(R * DM + C) * 2u; }
    { const int kg = wid >> 1, row = (lane >> 2) & 7, ch = (lane & 3) ^ (2 * (kg & 1)), n = 32 * (2 * (wid & 1) + (lane >> 5)) + 8 * ch;
      g.voffB = (unsigned)((8 * kg + row) * NW_ + n) * 2u; }
    g.aoff = lds_byte(wr * 64 + fr, fq * 8);
    g.boff0 = fq * 2048 + wc * 512 + (fr >> 2) * 64 + (32 * (fq & 1)) + 8 * (fr & 3);
    g.boff1 = g.boff0 ^ 32;
    g.A_kv = (const char*)P.ckv + (size_t)pm * 256 * DC * 2;
    g.A_q = (const char*)P.x + (size_t)pm * 256 * DM * 2;
    g.B_kv = (const char*)(vt ? P.wuv : P.wuk) + head * 512;
    g.B_q = (const char*)P.wq + head * 512 + vt * 256;
    if (wid < 4) {
        g.sbase = (const char*)(wid < 2 ? P.ak : P.av) + (wid & 1) * (64 * DM * 2) + c0 * 2; g.sstep = 128 * DM * 2; g.sv = (unsigned)lane * (DM * 2);
        g.sadd = 0; g.sdst = lds0 + K1_X + (wid >> 1) * 8192 + (wid & 1) * 256; g.sdstep = 512;
    } else {
        if (wid == 4) { g.sbase = g.A_kv + (size_t)((head & 1) * 2 + vt) * 64 * (DC * 2); g.sstep = 128; g.sv = (unsigned)lane * (DC * 2); }
        else if (wid == 5) { g.sbase = g.B_kv + (size_t)pm * 8 * (NW_ * 2); g.sstep = 64 * NW_ * 2; g.sv = (unsigned)((lane >> 2) & 7) * (NW_ * 2) + (lane & 3) * 128; }
        else { g.sbase = (const char*)(wid == 6 ? P.ak : P.av) + (size_t)j * 4 * (DM * 2) + xcd * 112; g.sstep = 128 * DM * 2; g.sv = (unsigned)(lane & 3) * (DM * 2) + ((lane >> 2) & 1) * 108; }
        g.sadd = 4; g.sdst = lds0 + K1_X + 16384 + wid * 256; g.sdstep = 0;
    }
    PIN(g.voffA_kv); PIN(g.voffB); PIN(g.sv); PIN(g.aoff); PIN(g.boff0); PIN(g.boff1);
    stage_units(g, std::make_integer_sequence<int, 8>{});
    WAIT_V(12);
    auto warm = [&](int slot) {
        const unsigned sink = lds0 + K1_X + 16384 + wid * 256;
        if (slot > 0) { if (wid >= 4) side(g, slot - 5); }
        else if (wid < 3) touch(sink, (unsigned)lane * (DM * 2), g.A_q + (size_t)((head & 1) * 2 + vt) * 64 * (DM * 2) + (4 + wid) * 128);
        else if (wid == 3) touch(sink, (unsigned)((lane < 48 ? lane : 0) >> 1) * (NW_ * 2) + (lane & 1) * 128, g.B_q + (size_t)(256 + pm * 24) * (NW_ * 2));
    };
    asm volatile("" : "+s"(sw), "+s"(dw));
    if (b == 0 && tid < N_AFLAGS + N_CFLAGS) {
#pragma unroll
        for (int q = 0; q < 4; ++q) *(u32x4*)((unsigned*)(P.ws + WS_FLAGS) + tid * FLAG_STRIDE + 4 * q) = (u32x4){0u, 0u, 0u, 0u};
    }
    bf16x8 At[4][2], B0[2][2];
    BAR;
    if (wr == 1) BAR;
    TS(P.ws, 0, 1);
    {
        f32x4 q0[4][2], q1[4][2];
        zero8(q0); zero8(q1);
        qphases(g, q0, q1, At, B0, warm, std::make_integer_sequence<int, 14>{});
        stage_unit<27>(g); stage_unit<28>(g); SBAR();
        Out1 o;
        o.rs = __builtin_amdgcn_make_buffer_rsrc(P.ws + WS_Q + (size_t)head * MB, (short)0, (int)MB, 0x00020000);
        o.voff = kq_off(pm, wr, wc, fr, fq) + vt * 8192; o.sbj = 65536; o.shi = 16384;
        pack_kq(q0, o.pk, 0.0625f * 1.4426950408889634f); pack_kq(q1, o.pk + 4, 0.0625f * 1.4426950408889634f);
        st_range<0, 8>(o);
        SBAR();
    }
    TS(P.ws, 0, 2);
    Out1 o;
    if (vt) big_tile<true>(g, P, pm, head, c0, e, wid, At, B0, o); else big_tile<false>(g, P, pm, head, c0, e, wid, At, B0, o);
    o.rs = __builtin_amdgcn_make_buffer_rsrc(P.ws + (vt ? WS_V : WS_K) + (size_t)head * MB, (short)0, (int)MB, 0x00020000);
    o.sbj = vt ? 16384 : 8192; o.shi = vt ? 2048 : 16384;
    SBAR(); st_range<0, 4>(o); SBAR();
    WAIT_V(4);
    if (wr == 0) BAR;
    BAR;
    TS(P.ws, 0, 4);
    const int lane2 = lane_id();
    wo_image(lds, P, e, wid, lane2);
    if (j < 28) conv_part(lds, P, c0, sw, dw, wid, lane2, o);
    else { st_range<4, 16>(o); gate_part(P, (xcd * 4 + (j - 28)) * 64, wid, lane2); }
    TS(P.ws, 0, 7);
}
#undef SA
#undef SB

constexpr int AT_K = 0, AT_V = 65536, AT_LDS = 163840;
constexpr float THR = 8.0f;
constexpr int PD = 6;
constexpr int NEB = 24;
template <int... Is> using iseq = std::integer_sequence<int, Is...>;
template <int N> using make_iseq = std::make_integer_sequence<int, N>;
#define ACLOB "a0","a1","a2","a3","a4","a5","a6","a7","a8","a9","a10","a11","a12","a13","a14","a15","a16","a17","a18","a19","a20","a21","a22","a23","a24","a25","a26","a27","a28","a29","a30","a31","a32","a33","a34","a35","a36","a37","a38","a39","a40","a41","a42","a43","a44","a45","a46","a47","a48","a49","a50","a51","a52","a53","a54","a55","a56","a57","a58","a59","a60","a61","a62","a63","a64","a65","a66","a67","a68","a69","a70","a71","a72","a73","a74","a75","a76","a77","a78","a79","a80","a81","a82","a83","a84","a85","a86","a87","a88","a89","a90","a91","a92","a93","a94","a95","a96","a97","a98","a99","a100","a101","a102","a103","a104","a105","a106","a107","a108","a109","a110","a111","a112","a113","a114","a115","a116","a117","a118","a119","a120","a121","a122","a123","a124","a125","a126","a127","a128","a129","a130","a131","a132","a133","a134","a135","a136","a137","a138","a139","a140","a141","a142","a143","a144","a145","a146","a147","a148","a149","a150","a151","a152","a153","a154","a155","a156","a157","a158","a159","a160","a161","a162","a163","a164","a165","a166","a167","a168","a169","a170","a171","a172","a173","a174","a175","a176","a177","a178","a179","a180","a181","a182","a183","a184","a185","a186","a187","a188","a189","a190","a191","a192","a193","a194","a195","a196","a197","a198","a199","a200","a201","a202","a203","a204","a205","a206","a207","a208","a209","a210","a211","a212","a213","a214","a215","a216","a217","a218","a219","a220","a221","a222","a223","a224","a225","a226","a227","a228","a229","a230","a231","a232","a233","a234","a235","a236","a237","a238","a239","a240","a241","a242","a243","a244","a245","a246","a247","a248","a249","a250","a251","a252","a253","a254","a255"

#define PH(i) do { } while (0)
constexpr int op_slot(int s) { switch (s % 5) { case 0: return 0; case 1: return 32768; case 2: return 65536; case 3: return 131072; default: return 98304; } }
static_assert(AT_V + 32768 == 98304 && AT_LDS == 163840, "lds");
struct OPJ { const char *wbase, *cbase; unsigned cv, lds0; ldscp wp; const unsigned* fl; unsigned ready; unsigned char* ws; };
struct AT {
    OPJ o;
    u32x2 pk;
    f32x2 ml2;
    const char *part, *mlp;
    bool isA, early;
    unsigned* flag;
    f32x16 s[2][2];
    f32x16 cm[2];
    u32x4 p[4];
    bf16x8 fr[8];
    float m, l, rs0, rs1, mx0, mx1, alpha, aone;
    bool pend, first, drain;
    ldscp kb;
    ldscp vcur;
    unsigned goff;
    unsigned m0k, m0v;
    const char *gk, *gv;
};

DI void dma_piece(unsigned m0v, unsigned goff, const char* g, const int off) {
    switch (off) {
    case 0: asm volatile("s_mov_b32 m0, %0\n\ts_nop 0\n\tglobal_load_lds_dwordx4 %1, %2" :: "s"(m0v), "v"(goff), "s"(g) : "memory"); break;
    case 1: asm volatile("s_mov_b32 m0, %0\n\ts_nop 0\n\tglobal_load_lds_dwordx4 %1, %2 offset:1024" :: "s"(m0v), "v"(goff), "s"(g) : "memory"); break;
    case 2: asm volatile("s_mov_b32 m0, %0\n\ts_nop 0\n\tglobal_load_lds_dwordx4 %1, %2 offset:2048" :: "s"(m0v), "v"(goff), "s"(g) : "memory"); break;
    default: asm volatile("s_mov_b32 m0, %0\n\ts_nop 0\n\tglobal_load_lds_dwordx4 %1, %2 offset:3072" :: "s"(m0v), "v"(goff), "s"(g) : "memory"); break;
    }
}
template <int I> DI void dma8(unsigned m0base, unsigned goff, const char* g) { dma_piece(m0base + (I >> 2) * 4096, goff, g + (I >> 2) * 4096, I & 3); }
DI void dma_tile(unsigned m0base, unsigned goff, const char* g) {
    dma8<0>(m0base, goff, g); dma8<1>(m0base, goff, g); dma8<2>(m0base, goff, g); dma8<3>(m0base, goff, g);
    dma8<4>(m0base, goff, g); dma8<5>(m0base, goff, g); dma8<6>(m0base, goff, g); dma8<7>(m0base, goff, g);
}
DI float ex2(float x) { return __builtin_amdgcn_exp2f(x); }
DI float MX3(float a, float b, float c) { float r; asm("v_max3_f32 %0, %1, %2, %3" : "=v"(r) : "v"(a), "v"(b), "v"(c)); return r; }
DI float MX2(float a, float b) { float r; asm("v_max_f32_e32 %0, %1, %2" : "=v"(r) : "v"(a), "v"(b)); return r; }
template <int D0> DI void qk_init(f32x16& s, const bf16x8& k, const f32x16& c) {
    asm volatile("v_mfma_f32_32x32x16_bf16 %0, %1, a[%c3:%c4], %2" : "=&v"(s) : "v"(k), "v"(c), "i"(128 + 4 * D0), "i"(131 + 4 * D0)); }
template <int D0> DI void qk_acc(f32x16& s, const bf16x8& k) {
    asm volatile("v_mfma_f32_32x32x16_bf16 %0, %1, a[%c2:%c3], %0" : "+v"(s) : "v"(k), "i"(128 + 4 * D0), "i"(131 + 4 * D0)); }
template <int DB> DI void pv_acc(const bf16x8& v, const u32x4& p) {
    asm volatile("v_mfma_f32_32x32x16_bf16 a[%c2:%c3], %0, %1, a[%c2:%c3]" :: "v"(v), "v"(p), "i"(16 * DB), "i"(16 * DB + 15)); }
DI void add_bcast(f32x16& x, float aone, float b) { asm volatile("s_nop 1\n\tv_mfma_f32_32x32x2_f32 %0, %1, %2, %0" : "+v"(x) : "v"(aone), "v"(b)); }
#define NOPS16() asm volatile("s_nop 7\n\ts_nop 7" ::: "memory")
#define NOPS2() asm volatile("s_nop 1" ::: "memory")
template <int I> DI void a_zero() { asm volatile("v_accvgpr_write_b32 a[%c0], 0" :: "i"(I)); }
template <int I> DI void a_scale(float f) { float t; asm volatile("v_accvgpr_read_b32 %0, a[%c2]\n\tv_mul_f32 %0, %1, %0\n\tv_accvgpr_write_b32 a[%c2], %0" : "=&v"(t) : "v"(f), "i"(I)); }
template <int I> DI void a_qmov() { asm volatile("v_accvgpr_mov_b32 a[%c0], a[%c1]" :: "i"(128 + I), "i"(192 + I)); }
template <int I> DI float a_read() { float t; asm volatile("v_accvgpr_read_b32 %0, a[%c1]" : "=v"(t) : "i"(I)); return t; }
template <int F> DI void q_load(unsigned voff, const char* g) {
    asm volatile("global_load_dwordx4 a[%c2:%c3], %0, %1 offset:%c4" :: "v"(voff), "s"(g + ((F & 15) >> 2) * 4096), "i"(128 + 4 * F), "i"(131 + 4 * F), "i"((F & 3) * 1024) : "memory"); }
template <int K> DI void pk_store(const AT& c, const u32x4 pk) {
    asm volatile("global_store_dwordx4 %0, %1, %2 offset:%c3 sc1\n\ts_nop 1" :: "v"(c.o.cv), "v"(pk), "s"(c.part + (K >> 2) * 4096), "i"((K & 3) * 1024) : "memory"); }
template <int K> DI void p_load(const AT& c) {
    asm volatile("global_load_dwordx4 a[%c2:%c3], %0, %1 offset:%c4 sc1" :: "v"(c.o.cv), "s"(c.part + (K >> 2) * 4096), "i"(192 + 4 * K), "i"(195 + 4 * K), "i"((K & 3) * 1024) : "memory"); }
DI void ml_load(AT& c) { asm volatile("global_load_dwordx2 %0, %1, %2 sc1" : "+v"(c.ml2) : "v"((c.o.cv & 496u) >> 1), "s"(c.mlp) : "memory"); }
template <int I> DI void op_wpiece(const OPJ& o) {
    constexpr int S = I >> 3, H = (I & 4) * 1024;
    dma_piece(o.lds0 + op_slot(S) + H, o.cv, o.wbase + S * 32768 + H, I & 3);
}
template <int DB> DI void pv_new(const bf16x8& v, const u32x4& p) {
    asm volatile("v_mfma_f32_32x32x16_bf16 a[%c2:%c3], %0, %1, 0" :: "v"(v), "v"(p), "i"(16 * DB), "i"(16 * DB + 15)); }
template <int... Is> DI void a_zeros(iseq<Is...>) { (a_zero<Is>(), ...); }
template <int... Is> DI void a_scales(float f, iseq<Is...>) { (a_scale<Is>(f), ...); }
template <int... Is> DI void a_qmovs(iseq<Is...>) { (a_qmov<Is>(), ...); }
template <int B, int... Is> DI void q_loads(unsigned voff, const char* g, iseq<Is...>) { (q_load<B + Is>(voff, g), ...); }
template <int... Is> DI void p_loads(const AT& c, iseq<Is...>) { (p_load<Is>(c), ...); }
template <int B, int... Is> DI void a_reads(float (&o)[16], iseq<Is...>) { ((o[Is] = a_read<B + Is>()), ...); }

template <int KS, int R> DI bf16x8 kfrag(const AT& c) { return ldsrd16(c.kb + KS * 32768 + ((R & 1) * 16 + (R >> 1)) * 1024); }
template <int R, bool DBM = false> DI bf16x8 vfrag(ldscp v) {
    return ldsrd16(v + (DBM ? (R >> 2) * 4096 + (R & 3) * 1024 : (R & 7) * 4096 + (R >> 3) * 1024)); }
template <int Q> DI void pk_quad(AT& c) {
    const float t0 = a_read<4 * Q>(), t1 = a_read<4 * Q + 1>(), t2 = a_read<4 * Q + 2>(), t3 = a_read<4 * Q + 3>();
    if constexpr (Q & 1) pk_store<(Q >> 1)>(c, (u32x4){c.pk[0], c.pk[1], cvtpk(t0, t1), cvtpk(t2, t3)});
    else { c.pk = (u32x2){cvtpk(t0, t1), cvtpk(t2, t3)}; PIN(c.pk); }
}
template <int X, int E> DI void exp_sum(AT& c) {
    const float e = ex2(c.s[X][E >> 4][E & 15]); c.s[X][E >> 4][E & 15] = e;
    if constexpr (E & 1) c.rs1 += e; else c.rs0 += e;
}
template <int X, int W> DI void pack1(AT& c) {
    c.p[W >> 2][W & 3] = cvtpk(c.s[X][W >> 3][(2 * W) & 15], c.s[X][W >> 3][(2 * W + 1) & 15]);
}
template <int X, int MODE, int N> DI void gapA(AT& c) {
    constexpr int d0 = N >> 1, kb = N & 1, Y = X ^ 1;
    if constexpr (d0 == 0) qk_init<0>(c.s[Y][kb], c.fr[N & 7], c.cm[(MODE & 4) ? kb : 0]);
    else qk_acc<d0>(c.s[Y][kb], c.fr[N & 7]);
    constexpr int R = N + PD;
    if constexpr (R < 32) c.fr[R & 7] = kfrag<Y, R>(c); else if constexpr (MODE & 1) c.fr[R & 7] = vfrag<R - 32, (MODE & 18) == 18>(c.vcur);
    if constexpr (MODE & 1) {
        if constexpr (N < 32 - NEB) { exp_sum<X, NEB + N>(c); PIN(c.rs0); PIN(c.rs1); }
        if constexpr (N >= 10 && N < 26) { pack1<X, N - 10>(c); PIN(c.p[(N - 10) >> 2]); }
    }
    if constexpr ((MODE & 4) && N == 2) asm volatile("" :: "v"(c.cm[0]), "v"(c.cm[1]));
    if constexpr ((MODE & 18) == 16) pk_quad<N>(c);
    if constexpr ((MODE & 18) == 18 && N >= 24) pk_quad<N - 24>(c);
    if constexpr ((MODE & 2) && (N & 1)) {
        if constexpr (N < 16) dma8<(N >> 1)>(c.m0k, c.goff, c.gk); else dma8<((N - 16) >> 1)>(c.m0v, c.goff, c.gv);
    }
    SBAR();
}
template <int X> DI void decide(AT& c) {
    constexpr int Y = X ^ 1;
    float mm = MX2(c.mx0, c.mx1);
    auto rr = __builtin_amdgcn_permlane32_swap(__float_as_uint(mm), __float_as_uint(mm), false, false);
    const float rm = MX2(__uint_as_float(rr[0]), __uint_as_float(rr[1]));
    if (__builtin_expect(c.first || __any(rm > THR), 0)) {
        const float dl = c.first ? rm : MX2(rm, 0.f);
        c.m += dl; c.alpha = ex2(-dl); c.pend = !c.first;
        const float nd = -dl;
        add_bcast(c.s[Y][0], c.aone, nd); add_bcast(c.s[Y][1], c.aone, nd); add_bcast(c.cm[0], c.aone, nd);
        asm volatile("s_nop 7\n\ts_nop 7\n\ts_nop 3" : "+v"(c.s[Y][0]), "+v"(c.s[Y][1]), "+v"(c.cm[0]));
    }
}
template <int X, int I> DI void maxstep(AT& c) {
    constexpr int Y = X ^ 1, h = I >> 2, r = 4 * (I & 3);
    if constexpr (I == 0) { c.mx0 = MX2(c.s[Y][0][0], c.s[Y][0][1]); c.mx1 = MX2(c.s[Y][0][2], c.s[Y][0][3]); }
    else { c.mx0 = MX3(c.mx0, c.s[Y][h][r], c.s[Y][h][r + 1]); c.mx1 = MX3(c.mx1, c.s[Y][h][r + 2], c.s[Y][h][r + 3]); }
}
template <int X, int MODE, int N> DI void gapB(AT& c) {
    constexpr bool DBM = (MODE & 18) == 18;
    constexpr int ks = DBM ? N & 3 : N >> 3, db = DBM ? N >> 2 : N & 7, Y = X ^ 1;
    if constexpr ((MODE & 16) && ks == 0) pv_new<db>(c.fr[N & 7], c.p[ks]); else pv_acc<db>(c.fr[N & 7], c.p[ks]);
    if constexpr (DBM && N < 24) pk_quad<N + 8>(c);
    if constexpr ((MODE & 32) && N == 10) {
        if (c.isA) {
            asm volatile("s_waitcnt vmcnt(0)" ::: "memory");
            c.early = rfl(__float_as_uint(a_read<192>())) != 0u;
            __builtin_amdgcn_fence(__ATOMIC_ACQUIRE, "wavefront");
            if (c.early) ml_load(c);
        }
    }
    if constexpr ((MODE & 32) && N > 10 && N < 27) { if (c.early) p_load<N - 11>(c); }
    if constexpr ((MODE & 48) == 48 && N == 16) { asm volatile("s_waitcnt vmcnt(0)" ::: "memory"); __hip_atomic_store(c.flag, 1u, RLX_AGENT); }
    if constexpr ((MODE & 48) == 32 && N == 28) asm volatile("global_load_dword a128, %0, %1 sc1" :: "v"(0u), "s"(c.flag) : "memory");
    if constexpr (MODE & 64) op_wpiece<N>(c.o);
    constexpr int R = N + PD;
    if constexpr (R < 32) c.fr[R & 7] = vfrag<R, DBM>(c.vcur);
    if constexpr (MODE & 1) {
        if constexpr (MODE & 2) {
            if constexpr (N == 28) { c.fr[0] = kfrag<X, 0>(c); c.fr[1] = kfrag<X, 1>(c); }
            if constexpr (N == 29) { c.fr[2] = kfrag<X, 2>(c); c.fr[3] = kfrag<X, 3>(c); }
            if constexpr (N == 30) { c.fr[4] = kfrag<X, 4>(c); }
            if constexpr (N == 31) { c.fr[5] = kfrag<X, 5>(c); }
        }
        if constexpr (N == 1) { asm volatile("s_nop 7" : "+v"(c.s[Y][0]), "+v"(c.s[Y][1])); }
        if constexpr (N >= 2 && N < 6) { maxstep<X, 2 * (N - 2)>(c); maxstep<X, 2 * (N - 2) + 1>(c); PIN(c.mx0); PIN(c.mx1); }
        if constexpr (N == 6) { c.l += c.rs0 + c.rs1; c.rs0 = 0.f; c.rs1 = 0.f; PIN(c.l); }
        if constexpr (N == 7) decide<X>(c);
        if constexpr (N >= 8) { exp_sum<Y, N - 8>(c); PIN(c.rs0); PIN(c.rs1); }
    }
    SBAR();
    if constexpr (N == 8 && MODE == 3) PH(6 * X + 2);
    if constexpr ((MODE & 1) && N == 27) {
        if constexpr (MODE == 3) PH(6 * X + 3);
        if constexpr (MODE & 2) {
            if (X == 0 && c.drain) asm volatile("s_waitcnt vmcnt(0)" ::: "memory");
            if constexpr (MODE & 16) asm volatile("s_waitcnt vmcnt(24) lgkmcnt(0)\n\ts_barrier" ::: "memory");
            else asm volatile("s_waitcnt vmcnt(8) lgkmcnt(0)\n\ts_barrier" ::: "memory");
        } else {
            if (!c.isA) asm volatile("s_waitcnt vmcnt(0)" ::: "memory");
            asm volatile("s_waitcnt lgkmcnt(0)\n\ts_barrier" ::: "memory");
        }
        if constexpr (MODE == 3) PH(6 * X + 4);
        SBAR();
    }
}
template <int X, int MODE, int... Ns> DI void phaseA(AT& c, iseq<Ns...>) { (gapA<X, MODE, Ns>(c), ...); }
template <int X, int MODE, int... Ns> DI void phaseB(AT& c, iseq<Ns...>) { (gapB<X, MODE, Ns>(c), ...); }
template <int X, int B, int... Es> DI void exps_from(AT& c, iseq<Es...>) { (exp_sum<X, B + Es>(c), ...); }
template <int X, int... Ws> DI void packs(AT& c, iseq<Ws...>) { (pack1<X, Ws>(c), ...); }
template <int... Is> DI void maxsteps1(AT& c, iseq<Is...>) { (maxstep<1, Is>(c), ...); }
DI void rescale_o(AT& c) {
    if (__builtin_expect(c.pend, 0)) {
        NOPS16();
        a_scales(c.alpha, make_iseq<128>{});
        NOPS2();
        c.l *= c.alpha; c.pend = false;
    }
}
DI float halfsum(float x) { auto rr = __builtin_amdgcn_permlane32_swap(__float_as_uint(x), __float_as_uint(x), false, false); return __uint_as_float(rr[0]) + __uint_as_float(rr[1]); }
DI void build_mask(AT& c, int mrel) {
    const float NEG = -__builtin_inff(), nm = -c.m;
#pragma unroll
    for (int r = 0; r < 16; ++r) { const int kk = (r & 3) + 8 * (r >> 2); c.cm[0][r] = kk > mrel ? NEG : nm; c.cm[1][r] = kk + 32 > mrel ? NEG : nm; }
    PIN(c.cm[0]); PIN(c.cm[1]); NOPS2();
}

struct ATW {
    const char *Kh, *Vh; ldsp lds; unsigned mk, mv; int n1, vs, lane, wid, r32, hi, qrel; bool isA;
};
DI int tile_of(const ATW& w, int g) { return w.isA ? w.n1 + g : (g < w.n1 ? g : g - w.n1); }
template <int X, bool DMA> DI void setup(AT& c, const ATW& w, int g) {
    c.vcur = w.lds + AT_V + w.vs * 32768 + w.lane * 16;
    if constexpr (DMA) {
        const int v2 = w.vs == 0 ? 2 : w.vs - 1, tn = tile_of(w, g + 2);
        c.gk = w.Kh + (size_t)tn * 32768; c.gv = w.Vh + (size_t)tn * 32768; c.m0k = w.mk + X * 32768; c.m0v = w.mv + v2 * 32768;
    }
}
template <bool LAST, bool seam> DI void pair(AT& c, ATW& w, int g0) {
    setup<0, true>(c, w, g0);
    c.first = seam;
    const float m_old = c.m;
    if constexpr (seam) { a_qmovs(make_iseq<64>{}); NOPS2(); }
    int qr = w.qrel;
    if constexpr (LAST) {
        if constexpr (seam) asm volatile("; last pair, two segments" : "+v"(qr), "+v"(c.s[0][0]), "+v"(c.s[0][1]), "+v"(c.m));
        else asm volatile("; last pair" : "+v"(qr), "+v"(c.s[0][0]), "+v"(c.s[0][1]), "+v"(c.m));
        build_mask(c, qr);
    }
    SBAR();
    if constexpr (!LAST && !seam) PH(0);
    phaseA<0, LAST ? 7 : 3>(c, make_iseq<32>{});
    if constexpr (!LAST && !seam) PH(1);
    phaseB<0, LAST || seam ? 11 : 3>(c, make_iseq<32>{});
    if constexpr (!LAST && !seam) PH(5);
    rescale_o(c);
    if (c.drain) { __hip_atomic_store(c.flag, 1u, RLX_AGENT); c.drain = false; }
    c.first = false;
    w.vs = w.vs == 2 ? 0 : w.vs + 1;
    setup<1, !LAST>(c, w, g0 + 1);
    if constexpr (LAST || seam) asm volatile("" : "+s"(c.part));
    if constexpr (LAST) {
        asm volatile("global_load_dword a192, %0, %1 sc1" :: "v"(0u), "s"(c.flag) : "memory");
        PIN(qr); build_mask(c, qr - 64);
    }
    if constexpr (seam) {
        const float lt = halfsum(c.l);
        __hip_atomic_store((unsigned long long*)c.mlp + w.r32, ((unsigned long long)__float_as_uint(lt) << 32) | __float_as_uint(m_old), RLX_AGENT);
        c.l = 0.f;
        asm volatile("; second segment" : "+v"(c.s[1][0]), "+v"(c.s[1][1]), "+v"(c.rs0), "+v"(c.rs1), "+v"(c.l));
        SBAR();
        phaseA<1, (LAST ? 5 : 3) | 16>(c, make_iseq<32>{});
        phaseB<1, (LAST ? 33 : 11) | 16>(c, make_iseq<32>{});
        if constexpr (!LAST) c.drain = true;
    } else {
        SBAR();
        if constexpr (!LAST) PH(6);
        phaseA<1, LAST ? 5 : 3>(c, make_iseq<32>{});
        if constexpr (!LAST) PH(7);
        phaseB<1, LAST ? 33 : 3>(c, make_iseq<32>{});
        if constexpr (!LAST) PH(11);
    }
    rescale_o(c);
    w.vs = w.vs == 2 ? 0 : w.vs + 1;
}

template <int F> DI void op_cload(const OPJ& o) {
    asm volatile("global_load_dwordx4 a[%c2:%c3], %0, %1 offset:%c4 sc1" :: "v"(o.cv), "s"(o.cbase + (F >> 2) * 4096),
                 "i"(4 * (F & 63)), "i"(4 * (F & 63) + 3), "i"((F & 3) * 1024) : "memory"); }
template <int B, int... Is> DI void op_cloads(const OPJ& o, iseq<Is...>) { (op_cload<B + Is>(o), ...); }
template <int F> DI void op_mfma(f32x4& acc, const bf16x8& wf) {
    asm volatile("v_mfma_f32_16x16x32_bf16 %0, %1, a[%c2:%c3], %0" : "+v"(acc) : "v"(wf), "i"(4 * (F & 63)), "i"(4 * (F & 63) + 3)); }
DI void op_need(OPJ& o, unsigned mask) {
    if (__builtin_expect((o.ready & mask) != mask, 0)) {
        for (unsigned spins = 0; spins < (1u << 22); ++spins) {
            const unsigned v = __hip_atomic_load(o.fl + ((o.cv >> 4) & 15), RLX_AGENT);
            o.ready = (unsigned)__ballot(v != 0u);
            if ((o.ready & mask) == mask) break;
            __builtin_amdgcn_s_sleep(1);
        }
        __builtin_amdgcn_fence(__ATOMIC_ACQUIRE, "wavefront");
    }
}
template <int R> DI bf16x8 op_wfrag(const OPJ& o) { return ldsrd16(o.wp + op_slot(R >> 5) + (R & 31) * 1024); }
template <int I, class E> DI void op_step(OPJ& o, f32x4 (&acc)[4], bf16x8 (&wf)[16], E& epi) {
    constexpr int S = I >> 3;
    if constexpr ((I & 7) == 0) {
        if constexpr (S == 0) asm volatile("s_waitcnt vmcnt(24)\n\ts_barrier" ::: "memory");
        else if constexpr (S == 2) asm volatile("s_waitcnt vmcnt(40)\n\ts_barrier" ::: "memory");
        else if constexpr (S <= 12) asm volatile("s_waitcnt vmcnt(32)\n\ts_barrier" ::: "memory");
        else if constexpr (S == 13) asm volatile("s_waitcnt vmcnt(16)\n\ts_barrier" ::: "memory");
        else if constexpr (S == 14) { asm volatile("s_waitcnt vmcnt(0)\n\ts_barrier" ::: "memory"); epi(); }
        if constexpr (S < 12) op_need(o, 1u << (S + 4));
        SBAR();
    }
    op_mfma<I>(acc[0], wf[(4 * I) & 15]);
    if constexpr (I + 2 < 128) { wf[(4 * I + 8) & 15] = op_wfrag<4 * I + 8>(o); wf[(4 * I + 9) & 15] = op_wfrag<4 * I + 9>(o); }
    op_mfma<I>(acc[1], wf[(4 * I + 1) & 15]);
    if constexpr (S < 12) op_cload<I + 32>(o);
    op_mfma<I>(acc[2], wf[(4 * I + 2) & 15]);
    if constexpr (I + 2 < 128) { wf[(4 * I + 10) & 15] = op_wfrag<4 * I + 10>(o); wf[(4 * I + 11) & 15] = op_wfrag<4 * I + 11>(o); }
    op_mfma<I>(acc[3], wf[(4 * I + 3) & 15]);
    if constexpr (S < 12) op_wpiece<I + 32>(o);
    SBAR();
}
template <class E, int... Is> DI void op_steps(OPJ& o, f32x4 (&acc)[4], bf16x8 (&wf)[16], E& epi, iseq<Is...>) { (op_step<Is>(o, acc, wf, epi), ...); }
template <int... Rs> DI void op_wfrags(const OPJ& o, bf16x8 (&wf)[16], iseq<Rs...>) { ((wf[Rs] = op_wfrag<Rs>(o)), ...); }

template <bool MERGE, int D> DI void ctx_out(float f1, float f2, const __amdgpu_buffer_rsrc_t crs, int voff) {
    float o[16], p[16];
    a_reads<16 * D>(o, make_iseq<16>{});
    if constexpr (MERGE) a_reads<192 + 8 * D>(p, make_iseq<8>{});
#pragma unroll
    for (int s2 = 0; s2 < 2; ++s2) {
        u32x4 wv;
#pragma unroll
        for (int e = 0; e < 4; ++e) { const int r = 8 * s2 + 2 * e;
            if constexpr (MERGE) { const unsigned pw = __float_as_uint(p[r >> 1]); wv[e] = cvtpk(o[r] * f1 + bf_lo(pw) * f2, o[r + 1] * f1 + bf_hi(pw) * f2); }
            else wv[e] = cvtpk(o[r] * f1, o[r + 1] * f1); }
        __builtin_amdgcn_raw_buffer_store_b128(wv, crs, voff + D * 1024 + s2 * 512, 0, 16);
    }
}
template <bool MERGE, int... Ds> DI void ctx_outs(float f1, float f2, const __amdgpu_buffer_rsrc_t crs, int voff, iseq<Ds...>) { (ctx_out<MERGE, Ds>(f1, f2, crs, voff), ...); }

__global__ __launch_bounds__(256, 1) void k2_attn(unsigned char* ws, const bf16_t* x, bf16_t* out) {
    extern __shared__ __attribute__((aligned(16))) unsigned char shm[];
    ldsp lds = (ldsp)shm;
    const int tid = threadIdx.x, wid = rfl(tid >> 6), lane = tid & 63, r32 = lane & 31, hi = lane >> 5;
    const int b = blockIdx.x, xcd = b & 7, j = b >> 3, head = 2 * xcd + (j >> 4), w16 = j & 15;
    const bool isA = w16 >= 8; const int i = w16 & 7, bq = 15 - i;
    const int seg = head * 8 + i;
    ATW w;
    w.Kh = (const char*)ws + WS_K + (size_t)head * MB; w.Vh = (const char*)ws + WS_V + (size_t)head * MB;
    const char* Qh = (const char*)ws + WS_Q + (size_t)head * MB;
    w.lds = lds; w.n1 = 15 - 2 * i; w.vs = 0; w.lane = lane; w.wid = wid; w.r32 = r32; w.hi = hi; w.isA = isA; w.qrel = wid * 32 + r32 - 4 * hi;
    AT c;
    const unsigned lds0 = (unsigned)(uintptr_t)lds;
    c.part = (const char*)ws + WS_PART + (size_t)(seg * 4 + wid) * 16384; c.mlp = (const char*)ws + WS_ML + (seg * 4 + wid) * 256;
    c.flag = (unsigned*)(ws + WS_FLAGS) + seg * FLAG_STRIDE + wid;
    c.isA = isA; c.early = false; c.ml2 = (f32x2){0.f, 0.f};
    OPJ& o = c.o;
    o.cv = (unsigned)lane * 16u;
    c.goff = (unsigned)wid * 8192u + (unsigned)lane * 16u;
    c.kb = lds + AT_K + lane * 16;
    w.mk = lds0 + AT_K + wid * 8192; w.mv = lds0 + AT_V + wid * 8192;
    TS(ws, 1, 0);
    asm volatile("s_nop 4" ::: ACLOB);
    dma_tile(w.mk, c.goff, w.Kh + (size_t)tile_of(w, 0) * 32768);
    q_loads<0>((unsigned)lane * 16u, Qh + (size_t)(bq * 4 + wid) * 16384, make_iseq<16>{});
    dma_tile(w.mk + 32768, c.goff, w.Kh + (size_t)tile_of(w, 1) * 32768);
    dma_tile(w.mv, c.goff, w.Vh + (size_t)tile_of(w, 0) * 32768);
    if (wid == 3) CODE_TOUCH2(K2_SYM, lds0 + AT_V + 65536, j & 7, lane);
    const bool q2now = !isA && w.n1 <= 3;
    const char* q2p = Qh + (size_t)(i * 4 + wid) * 16384;
    if (q2now) q_loads<16>((unsigned)lane * 16u, q2p, make_iseq<16>{});
    a_zeros(make_iseq<128>{});
    c.cm[0] = f32x16{}; c.cm[1] = f32x16{}; c.m = 0.f; c.l = 0.f; c.rs0 = 0.f; c.rs1 = 0.f; c.alpha = 1.f; c.pend = false; c.drain = false;
    c.aone = hi ? 0.f : 1.f;
    PIN(c.cm[0]);
    if (q2now) asm volatile("s_waitcnt vmcnt(32)" ::: "memory"); else asm volatile("s_waitcnt vmcnt(16)" ::: "memory");
    asm volatile("s_barrier" ::: "memory");
    SBAR();
    c.fr[0] = kfrag<0, 0>(c); c.fr[1] = kfrag<0, 1>(c); c.fr[2] = kfrag<0, 2>(c); c.fr[3] = kfrag<0, 3>(c); c.fr[4] = kfrag<0, 4>(c); c.fr[5] = kfrag<0, 5>(c);
    SBAR();
    phaseA<1, 0>(c, make_iseq<32>{});
    dma_tile(w.mv + 32768, c.goff, w.Vh + (size_t)tile_of(w, 1) * 32768);
    SBAR(); asm volatile("s_nop 7\n\ts_nop 7" : "+v"(c.s[0][0]), "+v"(c.s[0][1])); SBAR();
    c.first = true;
    maxsteps1(c, make_iseq<8>{});
    decide<1>(c);
    exps_from<0, 0>(c, make_iseq<NEB>{});
    asm volatile("s_waitcnt vmcnt(8) lgkmcnt(0)\n\ts_barrier" ::: "memory");
    SBAR();
    c.fr[0] = kfrag<1, 0>(c); c.fr[1] = kfrag<1, 1>(c); c.fr[2] = kfrag<1, 2>(c); c.fr[3] = kfrag<1, 3>(c); c.fr[4] = kfrag<1, 4>(c); c.fr[5] = kfrag<1, 5>(c);
    TS(ws, 1, 1);
    const int q2at = isA || q2now ? -1 : 2;
    for (int g0 = 0, gstop = isA ? 14 : w.n1 - 1; ; g0 += 2, gstop = 14) {
        for (; g0 < gstop; g0 += 2) {
            if (g0 == q2at) q_loads<16>(c.o.cv, q2p, make_iseq<16>{});
            pair<false, false>(c, w, g0);
        }
        if (g0 >= 14) break;
        pair<false, true>(c, w, g0);
    }
    TS(ws, 1, 2);
    if (!isA && w.n1 == 15) pair<true, true>(c, w, 14); else pair<true, false>(c, w, 14);
    TS(ws, 1, 3);
    int b2 = b; asm volatile("" : "+s"(b2), "+s"(ws));
    const int j2 = b2 >> 3, jj = j2 - (j2 > 7) - (j2 > 8) - (j2 > 23) - (j2 > 24), rb = 4 * (b2 & 7) + jj / 7, cn = jj % 7, fr = lane & 15, fq = lane >> 4;
    const bool job = (j2 & 15) != 7 && (j2 & 15) != 8;
    unsigned* flags = (unsigned*)(ws + WS_FLAGS) + N_AFLAGS * FLAG_STRIDE;
    o.wbase = (const char*)ws + WS_WI + (size_t)cn * 524288 + wid * 8192;
    o.cbase = (const char*)ws + WS_CTX + (size_t)(rb * 4 + wid) * 131072;
    o.lds0 = lds0 + wid * 8192; o.wp = lds + lane * 16; o.ws = ws; o.fl = flags + (rb >> 1) * FLAG_STRIDE; o.ready = 0u;
    c.vcur = lds + AT_V + 32768 + lane * 16;
    c.fr[0] = vfrag<0>(c.vcur); c.fr[1] = vfrag<1>(c.vcur); c.fr[2] = vfrag<2>(c.vcur); c.fr[3] = vfrag<3>(c.vcur); c.fr[4] = vfrag<4>(c.vcur); c.fr[5] = vfrag<5>(c.vcur);
    SBAR();
    exps_from<0, NEB>(c, make_iseq<32 - NEB>{});
    packs<0>(c, make_iseq<16>{});
    c.l += c.rs0 + c.rs1;
    PIN(c.p[0]); PIN(c.p[1]); PIN(c.p[2]); PIN(c.p[3]);
    SBAR();
    if (isA && !c.early) {
        asm volatile("s_waitcnt vmcnt(0)" ::: "memory");
        c.early = rfl(__float_as_uint(a_read<128>())) != 0u;
        __builtin_amdgcn_fence(__ATOMIC_ACQUIRE, "wavefront");
        if (c.early) { ml_load(c); p_loads(c, make_iseq<16>{}); }
    }
    SBAR(); NOPS2(); SBAR();
    phaseB<0, 64>(c, make_iseq<32>{});
    SBAR();
    if (isA && !c.early) {
        unsigned spins = 0;
        while (rfl(__hip_atomic_load(c.flag, RLX_AGENT)) == 0u) { __builtin_amdgcn_s_sleep(1); if (++spins > (1u << 22)) break; }
        __builtin_amdgcn_fence(__ATOMIC_ACQUIRE, "wavefront");
        ml_load(c);
        p_loads(c, make_iseq<16>{});
        asm volatile("s_waitcnt vmcnt(0)" : "+v"(c.ml2) :: "memory");
    }
    const float lt = halfsum(c.l);
    TS(ws, 1, 4);
    {
        const __amdgpu_buffer_rsrc_t crs = __builtin_amdgcn_make_buffer_rsrc(ws + WS_CTX, (short)0, (int)(16 * MB), 0x00020000);
        const int row = (isA ? bq : i) * 128 + wid * 32 + r32;
        const int voff = ((row >> 4) * 128 + head * 8) * 1024 + hi * 256 + (row & 15) * 16;
        if (isA) {
            asm volatile("s_waitcnt vmcnt(32)" : "+v"(c.ml2) :: "memory");
            const float mn = __builtin_fmaxf(c.m, c.ml2[0]);
            const float w1 = ex2(c.m - mn), w2 = ex2(c.ml2[0] - mn);
            const float inv = 1.0f / (lt * w1 + c.ml2[1] * w2);
            SBAR(); NOPS16(); SBAR();
            ctx_outs<true>(w1 * inv, w2 * inv, crs, voff, make_iseq<8>{});
        } else {
            const float inv = 1.0f / lt;
            SBAR(); NOPS16(); SBAR();
            ctx_outs<false>(inv, 0.f, crs, voff, make_iseq<8>{});
        }
    }
    TS(ws, 1, 5);
    f32x4 acc[4];
#pragma unroll
    for (int f = 0; f < 4; ++f) { acc[f] = (f32x4){0.f, 0.f, 0.f, 0.f}; PIN(acc[f]); }
    asm volatile("s_waitcnt vmcnt(0)\n\ts_barrier" ::: "memory");
    if (tid == 0) __hip_atomic_store(flags + (isA ? bq : i) * FLAG_STRIDE + head, 1u, RLX_AGENT);
    TS(ws, 1, 7);
    TS(ws, 2, 0);
    if (!job) return;
    const int trow = rb * 64 + wid * 16 + fr, tcol = cn * 64 + 4 * fq;
    u32x2 xw[4], yw[4]; float g;
    auto epi = [&]() {
        const bf16_t* y2p = (const bf16_t*)(ws + WS_Y2);
#pragma unroll
        for (int f = 0; f < 4; ++f) {
            xw[f] = *(const u32x2*)(x + (size_t)trow * DM + tcol + f * 16);
            const bf16_t* yp = y2p + (size_t)(tcol + f * 16) * SEQ + trow;
            yw[f] = (u32x2){yp[0] | (unsigned)yp[SEQ] << 16, yp[2 * SEQ] | (unsigned)yp[3 * SEQ] << 16};
        }
        g = ((const float*)(ws + WS_G))[trow];
    };
    bf16x8 wf[16];
    op_wfrags(o, wf, make_iseq<8>{});
    SBAR();
    op_need(o, 1u); op_cloads<0>(o, make_iseq<8>{});
    op_need(o, 2u); op_cloads<8>(o, make_iseq<8>{});
    op_need(o, 4u); op_cloads<16>(o, make_iseq<8>{});
    op_need(o, 8u); op_cloads<24>(o, make_iseq<8>{});
    TS(ws, 2, 4);
    SBAR();
    op_steps(o, acc, wf, epi, make_iseq<128>{});
#pragma unroll
    for (int f = 0; f < 4; ++f) PIN(acc[f]);
    NOPS16();
#pragma unroll
    for (int f = 0; f < 4; ++f) PIN(acc[f]);
    TS(ws, 2, 1);
    const float og = rbf(1.0f - g);
#pragma unroll
    for (int f = 0; f < 4; ++f) {
        const float xv[4] = {bf_lo(xw[f][0]), bf_hi(xw[f][0]), bf_lo(xw[f][1]), bf_hi(xw[f][1])};
        const float yv[4] = {bf_lo(yw[f][0]), bf_hi(yw[f][0]), bf_lo(yw[f][1]), bf_hi(yw[f][1])};
        float r[4];
#pragma unroll
        for (int e = 0; e < 4; ++e) r[e] = rbf(g * rbf(xv[e] * yv[e])) + rbf(og * rbf(acc[f][e]));
        u32x2 wv = {cvtpk(r[0], r[1]), cvtpk(r[2], r[3])};
        *(u32x2*)(out + (size_t)trow * DM + tcol + f * 16) = wv;
    }
    TS(ws, 2, 7);
}

extern "C" __device__ __attribute__((used, noinline)) void text_end_mark() { asm volatile("s_nop 0"); }

extern "C" void kernel_launch(void* const* d_in, const int* in_sizes, int n_in, void* d_out, int out_size, void* d_ws, size_t ws_size,
                              hipStream_t stream) {
    (void)in_sizes; (void)n_in; (void)out_size; (void)ws_size;
    (void)hipFuncSetAttribute((const void*)k1_proj, hipFuncAttributeMaxDynamicSharedMemorySize, K1_LDS);
    (void)hipFuncSetAttribute((const void*)k2_attn, hipFuncAttributeMaxDynamicSharedMemorySize, AT_LDS);
    K1Args P;
    P.x = (const bf16_t*)d_in[0]; P.ak = (const bf16_t*)d_in[1]; P.av = (const bf16_t*)d_in[2]; P.shift_k = (const bf16_t*)d_in[3];
    P.ssm_k = (const bf16_t*)d_in[4]; P.ssm_d = (const bf16_t*)d_in[5]; P.ckv = (const bf16_t*)d_in[7]; P.wq = (const bf16_t*)d_in[8];
    P.wuk = (const bf16_t*)d_in[9]; P.wuv = (const bf16_t*)d_in[10]; P.gate_w = (const bf16_t*)d_in[12]; P.wo = (const bf16_t*)d_in[11]; P.ws = (unsigned char*)d_ws;
#define DEV_STAGES 7
    if (DEV_STAGES & 1) k1_proj<<<256, 512, K1_LDS, stream>>>(P);
    if (DEV_STAGES & 2) k2_attn<<<256, 256, AT_LDS, stream>>>((unsigned char*)d_ws, (const bf16_t*)d_in[0], (bf16_t*)d_out);
}
```

```cpp
#include <hip/hip_runtime.h>
#include <hip/hip_bf16.h>
#include <stdint.h>
#include <utility>

#define LAS __attribute__((address_space(3)))
#define GAS __attribute__((address_space(1)))
#define CAS __attribute__((address_space(4)))
#define DI __device__ __forceinline__
typedef unsigned short bf16_t;
typedef short bf16x8 __attribute__((ext_vector_type(8)));
typedef short s16x4 __attribute__((ext_vector_type(4)));
typedef float f32x2 __attribute__((ext_vector_type(2)));
typedef float f32x4 __attribute__((ext_vector_type(4)));
typedef float f32x16 __attribute__((ext_vector_type(16)));
typedef unsigned u32x2 __attribute__((ext_vector_type(2)));
typedef unsigned u32x4 __attribute__((ext_vector_type(4)));
typedef __bf16 bfx2 __attribute__((ext_vector_type(2)));
typedef LAS unsigned char* ldsp;
typedef const LAS unsigned char* ldscp;

constexpr int SEQ = 2048, DM = 448, NH = 16, HD = 256, DC = 1024, NW_ = 4096;

constexpr size_t MB = 1024 * 1024;
constexpr size_t WS_Q = 0, WS_K = 16 * MB, WS_V = 32 * MB, WS_PART = 64 * MB, WS_ML = 80 * MB,
                 WS_Y2 = 81 * MB, WS_G = 84 * MB, WS_FLAGS = 100 * MB, WS_WI = 102 * MB, WS_OP = 128 * MB;
constexpr int FLAG_STRIDE = 64;
constexpr int N_AFLAGS = 128, N_CFLAGS = 64;

#define TS(wsp, kern, slot) do { } while (0)
#define TSI(wsp, idx) do { } while (0)
#define SBAR() __builtin_amdgcn_sched_barrier(0)
#define PIN(x) asm volatile("" : "+v"(x))
#define RLX_AGENT __ATOMIC_RELAXED, __HIP_MEMORY_SCOPE_AGENT

DI unsigned cvtpk(float lo, float hi) { f32x2 v = {lo, hi}; bfx2 b = __builtin_convertvector(v, bfx2); return __builtin_bit_cast(unsigned, b); }
DI float bf_lo(unsigned w) { return __uint_as_float(w << 16); }
DI float bf_hi(unsigned w) { return __uint_as_float(w & 0xffff0000u); }
DI float rbf(float x) { return bf_lo(cvtpk(x, 0.f)); }
DI unsigned rfl(unsigned x) { return __builtin_amdgcn_readfirstlane(x); }
DI s16x4 trread(ldscp p) { return __builtin_amdgcn_ds_read_tr16_b64_v4i16((LAS s16x4*)p); }
DI bf16x8 cat8(s16x4 lo, s16x4 hi) { return __builtin_shufflevector(lo, hi, 0, 1, 2, 3, 4, 5, 6, 7); }
DI bf16x8 ldsrd16(ldscp p) { return *(const LAS bf16x8*)p; }
#define MFMA16(a, b, c) __builtin_amdgcn_mfma_f32_16x16x32_bf16((a), (b), (c), 0, 0, 0)
#define MFMA32(a, b, c) __builtin_amdgcn_mfma_f32_32x32x16_bf16((a), (b), (c), 0, 0, 0)

DI void touch(unsigned sink, unsigned voff, const char* g) {
    asm volatile("s_mov_b32 m0, %0\n\ts_nop 0\n\tglobal_load_lds_dword %1, %2" :: "s"(sink), "v"(voff), "s"(g) : "memory");
}
#define CODE_TOUCH(SYM, END, sink, chunk, lane) do { const char* _p; unsigned _n; \
        asm volatile("s_getpc_b64 s[90:91]\n\ts_add_u32 s90, s90, " SYM "@rel32@lo+4\n\ts_addc_u32 s91, s91, " SYM "@rel32@hi+12\n\ts_mov_b64 %0, s[90:91]\n\ts_mov_b32 %1, " END "-" SYM \
                     : "=s"(_p), "=s"(_n) :: "s90", "s91", "scc"); \
        const unsigned _o = ((unsigned)(chunk) * 64u + (unsigned)(lane)) * 128u; \
        if ((unsigned)(chunk) * 8192u < _n && _n < 262144u) touch((sink), _o < _n - 4u ? _o : _n - 4u, _p); } while (0)
#define K1_SYM "_Z7k1_proj6K1Args", "_Z7k2_attnPhPKtPt"
#define K2_SYM "_Z7k2_attnPhPKtPt", "text_end_mark"
#define CODE_TOUCH2(S, sink, chunk, lane) CODE_TOUCH(S, sink, chunk, lane)

constexpr int HTB = 128 * 64 * 2;
constexpr int K1_X = 8 * HTB;
constexpr int K1_LDS = K1_X + 16384 + 2048;

DI int lds_byte(int r, int c) { const int st = (r >> 4) * 2 + (c >> 5), rr = r & 15, cc = c & 31, ob = rr * 64 + cc * 2; return st * 1024 + (ob ^ (((ob >> 9) & 1) << 5)); }
DI void stage_rc(int b, int& R, int& C) { const int st = b / 1024, sb = b % 1024, swz = sb ^ (((sb >> 9) & 1) << 5); R = (st >> 1) * 16 + swz / 64; C = (st & 1) * 32 + (swz % 64) / 2; }

struct K1Args {
    const bf16_t *x, *ak, *av, *shift_k, *ssm_k, *ssm_d, *ckv, *wq, *wuk, *wuv, *gate_w, *wo;
    unsigned char* ws;
};

#define SA(b, h) (((b) * 2 + (h)) * HTB)
#define SB(b, h) ((4 + (b) * 2 + (h)) * HTB)
constexpr int ring_slot(int u) {
    switch (u & 7) { case 5: return SB(0, 0); case 6: return SA(0, 0); case 7: return SB(0, 1); case 0: return SA(0, 1);
                     case 1: return SB(1, 0); case 2: return SA(1, 0); case 3: return SB(1, 1); default: return SA(1, 1); }
}
struct G1 {
    ldsp lds; unsigned ldsw  , voffA_kv, voffA_q, voffB, sv; int aoff, boff0, boff1;
    const char *A_kv, *A_q, *B_kv, *B_q, *sbase; unsigned sstep, sdst, sdstep; int sadd;
};
DI int lane_id() { int l; asm volatile("v_mbcnt_lo_u32_b32 %0, -1, 0\n\tv_mbcnt_hi_u32_b32 %0, -1, %0" : "=v"(l)); return l; }
template <int OFF> DI void dma16(unsigned lbase, unsigned voff, const char* g) {
    asm volatile("s_add_i32 m0, %0, %c3\n\ts_nop 0\n\tglobal_load_lds_dwordx4 %1, %2" :: "s"(lbase), "v"(voff), "s"(g), "i"(OFF) : "memory", "scc");
}
template <int BUF, int STRIDE, int H> DI void stage_a(const G1& g, const char* base, unsigned voff) {
    dma16<BUF>(g.ldsw, voff, base + H * 128 * STRIDE); dma16<BUF + 8192>(g.ldsw, voff, base + (H * 128 + 64) * STRIDE);
}
template <int BUF, int H> DI void stage_b(const G1& g, const char* base) {
    dma16<BUF>(g.ldsw, g.voffB, base + H * 256); dma16<BUF + 8192>(g.ldsw, g.voffB, base + H * 256 + 32 * NW_ * 2);
}
template <int U> DI void stage_unit(const G1& g) {
    if constexpr (U < 21) {
        constexpr int t = U / 3, r = U % 3;
        if constexpr (r == 0) stage_b<ring_slot(U), 0>(g, g.B_q + (size_t)t * (64 * NW_ * 2));
        else stage_a<ring_slot(U), DM * 2, r - 1>(g, g.A_q + t * 128, g.voffA_q);
    } else {
        constexpr int t = (U - 21) >> 2, r = (U - 21) & 3;
        if constexpr ((r & 1) == 0) stage_b<ring_slot(U), (r >> 1)>(g, g.B_kv + (size_t)t * (64 * NW_ * 2));
        else stage_a<ring_slot(U), DC * 2, (r >> 1)>(g, g.A_kv + t * 128, g.voffA_kv);
    }
}
DI void side(const G1& g, int t) {
    const int tt = t + g.sadd > 15 ? 15 : t + g.sadd;
    asm volatile("s_mov_b32 m0, %0\n\ts_nop 0\n\tglobal_load_lds_dword %1, %2" :: "s"(g.sdst + (unsigned)t * g.sdstep), "v"(g.sv), "s"(g.sbase + rfl((unsigned)tt * g.sstep)) : "memory");
}
DI void lda(bf16x8 (&dst)[4][2], const G1& g, int buf) {
#pragma unroll
    for (int m = 0; m < 4; ++m)
#pragma unroll
        for (int k = 0; k < 2; ++k) dst[m][k] = ldsrd16(g.lds + buf + g.aoff + m * 2048 + k * 1024);
}
DI void ldb(bf16x8 (&dst)[2][2], const G1& g, int buf) {
#pragma unroll
    for (int n = 0; n < 2; ++n)
#pragma unroll
        for (int k = 0; k < 2; ++k) { ldscp p = g.lds + buf + (n ? g.boff1 : g.boff0) + k * 8192; dst[n][k] = cat8(trread(p), trread(p + 256)); }
}
template <bool VT> DI void mma(f32x4 (&acc)[4][2], const bf16x8 (&At)[4][2], const bf16x8 (&Bt)[2][2]) {
    __builtin_amdgcn_s_setprio(1);
#pragma unroll
    for (int m = 0; m < 4; ++m)
#pragma unroll
        for (int n = 0; n < 2; ++n)
#pragma unroll
            for (int k = 0; k < 2; ++k) acc[m][n] = VT ? MFMA16(At[m][k], Bt[n][k], acc[m][n]) : MFMA16(Bt[n][k], At[m][k], acc[m][n]);
    __builtin_amdgcn_s_setprio(0);
}
#define WAIT_V(n) asm volatile("s_waitcnt vmcnt(" #n ")" ::: "memory")
#define WAIT_L(n) asm volatile("s_waitcnt lgkmcnt(" #n ")" ::: "memory")
#define BAR __builtin_amdgcn_s_barrier()
DI void zero8(f32x4 (&a)[4][2]) {
#pragma unroll
    for (int m = 0; m < 4; ++m) { a[m][0] = (f32x4){0.f, 0.f, 0.f, 0.f}; a[m][1] = (f32x4){0.f, 0.f, 0.f, 0.f}; }
}

DI void pack_kq(const f32x4 (&acc)[4][2], u32x4* pk, float scale) {
#pragma unroll
    for (int m = 0; m < 4; ++m) {
        const f32x4 a0 = acc[m][0] * scale, a1 = acc[m][1] * scale;
        const unsigned w0 = cvtpk(a0[0], a0[1]), w1 = cvtpk(a0[2], a0[3]), x0 = cvtpk(a1[0], a1[1]), x1 = cvtpk(a1[2], a1[3]);
        auto r0 = __builtin_amdgcn_permlane16_swap(w0, x0, false, false);
        auto r1 = __builtin_amdgcn_permlane16_swap(w1, x1, false, false);
        pk[m] = (u32x4){r0[0], r1[0], r0[1], r1[1]};
    }
}
DI unsigned kq_off(int pm, int wr, int wc, int fr, int fq) {
    return (unsigned)((pm * 8 + wr * 2) * 16384 + fr * 16 + (2 * wc + (fq & 1)) * 1024 + (fq >> 1) * 512);
}
DI void pack_v(const f32x4 (&acc)[4][2], u32x4* pk) {
#pragma unroll
    for (int n = 0; n < 2; ++n)
#pragma unroll
        for (int mm = 0; mm < 2; ++mm) {
            const f32x4 a0 = acc[2 * mm][n], a1 = acc[2 * mm + 1][n];
            const unsigned w0 = cvtpk(a0[0], a0[1]), w1 = cvtpk(a0[2], a0[3]), x0 = cvtpk(a1[0], a1[1]), x1 = cvtpk(a1[2], a1[3]);
            auto r0 = __builtin_amdgcn_permlane32_swap(w0, x0, false, false);
            auto r1 = __builtin_amdgcn_permlane32_swap(w1, x1, false, false);
            pk[2 * mm + n] = (u32x4){r0[0], r1[0], r0[1], r1[1]};
        }
}
constexpr int ST_AUX = 16;
struct Out1 { u32x4 pk[16]; __amdgpu_buffer_rsrc_t rs; unsigned voff; int sbj, shi; };
template <int I> DI void st_piece(const Out1& o) {
    __builtin_amdgcn_raw_buffer_store_b128(o.pk[I], o.rs, o.voff + (I & 1) * 256, (I >> 3) * 65536 + ((I >> 2) & 1) * o.sbj + ((I >> 1) & 1) * o.shi, ST_AUX);
}
template <int A, int B> DI void st_range(const Out1& o) { if constexpr (A < B) { st_piece<A>(o); st_range<A + 1, B>(o); } }

template <int N, class W> DI void qphase(const G1& g, f32x4 (&a0)[4][2], f32x4 (&a1)[4][2], bf16x8 (&At)[4][2], bf16x8 (&B0)[2][2], W& warm) {
    constexpr int t = N >> 1;
    if constexpr ((N & 1) == 0) {
        ldb(B0, g, ring_slot(3 * t)); SBAR(); lda(At, g, ring_slot(3 * t + 1));
        if constexpr (t < 5) warm(t);
        if constexpr (N >= 2) stage_unit<3 * t + 6>(g);
        if constexpr (N == 0) WAIT_V(10); else WAIT_V(8);
        WAIT_L(8); BAR; WAIT_L(0); mma<false>(a0, At, B0); BAR; SBAR();
    } else {
        lda(At, g, ring_slot(3 * t + 2));
        if constexpr (t >= 1) stage_unit<3 * t + 7>(g);
        stage_unit<3 * t + 8>(g);
        WAIT_V(8);
        if constexpr (N == 13) WAIT_L(0);
        BAR; WAIT_L(0); mma<false>(a1, At, B0); BAR; SBAR();
    }
}
template <class W, int... Ns> DI void qphases(const G1& g, f32x4 (&a0)[4][2], f32x4 (&a1)[4][2], bf16x8 (&At)[4][2], bf16x8 (&B0)[2][2], W& warm, std::integer_sequence<int, Ns...>) {
    (qphase<Ns>(g, a0, a1, At, B0, warm), ...);
}
template <int... Us> DI void stage_units(const G1& g, std::integer_sequence<int, Us...>) { (stage_unit<Us>(g), ...); }

template <bool VT, int POS, class W>
DI void mainpair(const G1& g, int t, f32x4 (&acc)[2][2][4][2], bf16x8 (&At)[4][2], bf16x8 (&B0)[2][2], bf16x8 (&B1)[2][2], W& lastside) {
    const char* a1 = g.A_kv + (t + 1) * 128; const char* a2 = a1 + 128; const char* a3 = a1 + 256;
    const char* b2 = g.B_kv + (size_t)(t + 2) * (64 * NW_ * 2); const char* b3 = b2 + 64 * NW_ * 2;
    ldb(B0, g, SB(0, 0)); SBAR(); lda(At, g, SA(0, 0));
    if constexpr (POS != 0) stage_a<SA(1, 1), DC * 2, 1>(g, a1, g.voffA_kv); else WAIT_V(18);
    WAIT_L(8); BAR; WAIT_L(0); mma<VT>(acc[0][0], At, B0); BAR; SBAR();
    ldb(B1, g, SB(0, 1));
    if constexpr (POS == 2) lastside(0); else side(g, t);
    if constexpr (POS != 2) stage_b<SB(0, 0), 0>(g, b2);
    if constexpr (POS == 0) WAIT_V(19);
    BAR; WAIT_L(0); mma<VT>(acc[0][1], At, B1); BAR;
    lda(At, g, SA(0, 1));
    if constexpr (POS != 2) stage_a<SA(0, 0), DC * 2, 0>(g, a2, g.voffA_kv);
    BAR; WAIT_L(0); mma<VT>(acc[1][0], At, B0); BAR; SBAR();
    if constexpr (POS != 2) stage_b<SB(0, 1), 1>(g, b2);
    if constexpr (POS == 0) WAIT_V(15); else if constexpr (POS == 1) WAIT_V(7); else WAIT_V(1);
    BAR; mma<VT>(acc[1][1], At, B1); BAR;
    ldb(B0, g, SB(1, 0)); SBAR(); lda(At, g, SA(1, 0));
    if constexpr (POS != 2) stage_a<SA(0, 1), DC * 2, 1>(g, a2, g.voffA_kv);
    WAIT_L(8); BAR; WAIT_L(0); mma<VT>(acc[0][0], At, B0); BAR; SBAR();
    ldb(B1, g, SB(1, 1));
    if constexpr (POS == 2) lastside(1); else side(g, t + 1);
    if constexpr (POS != 2) stage_b<SB(1, 0), 0>(g, b3);
    BAR; WAIT_L(0); mma<VT>(acc[0][1], At, B1); BAR;
    lda(At, g, SA(1, 1));
    if constexpr (POS != 2) stage_a<SA(1, 0), DC * 2, 0>(g, a3, g.voffA_kv);
    BAR; WAIT_L(0); mma<VT>(acc[1][0], At, B0); BAR; SBAR();
    if constexpr (POS != 2) { stage_b<SB(1, 1), 1>(g, b3); WAIT_V(7); }
    BAR; mma<VT>(acc[1][1], At, B1); BAR;
}

constexpr int CV_RAW = SA(0, 0);
constexpr int K1_WO = SA(0, 1);
constexpr int CV_HSTR = 4256;
constexpr int CV_H = 2 * HTB;
constexpr int CV_USTR = 4608;
constexpr int CV_U = CV_H + 8 * CV_HSTR;
constexpr int CV_R = CV_U + 2 * CV_USTR;
static_assert(CV_R + 65536 <= K1_LDS && CV_R <= K1_X, "lds");

DI const char* wo_block(const K1Args& P, int e) { const int eb = e < 224 ? e : 0, kb = eb / 7, cn = eb - kb * 7; return (const char*)P.wo + ((size_t)kb * 128 * DM + cn * 64) * 2; }
DI void tail_stage(const G1& g, const K1Args& P, int c0, int e, int wid, int lane) {
    dma16<CV_RAW>(g.ldsw, (unsigned)lane * 16u, (const char*)P.ssm_k + (size_t)c0 * (SEQ * 2) + wid * 1024);
    const char* wg = wo_block(P, e);
    const unsigned vo = (unsigned)((wid * 8 + (lane >> 3)) * DM + (lane & 7) * 8) * 2u;
    dma16<K1_WO>(g.ldsw, vo, wg); dma16<K1_WO + 8192>(g.ldsw, vo, wg + 64 * DM * 2);
}
DI void wo_image(ldsp lds, const K1Args& P, int e, int wid, int lane) {
    const int fr = lane & 15, fq = lane >> 4, kb = e / 7, cn = e - kb * 7;
    if (e < 224) {
        ldscp wp = lds + K1_WO + (wid * 16 + 4 * (fq >> 1) + (fr >> 2)) * 128 + (fr & 1) * 32 + (fq & 1) * 16 + (fr & 2) * 4;
        unsigned char* op = P.ws + WS_WI + (size_t)((kb >> 1) * 7 + cn) * 32768 + ((kb & 1) * 8 + wid) * 2048 + lane * 16;
#pragma unroll
        for (int cb = 0; cb < 2; ++cb) *(bf16x8*)(op + cb * 1024) = cat8(trread(wp + cb * 64), trread(wp + cb * 64 + 1024));
    }
}
DI void conv_copies(ldsp q, const u32x4 d, const u32x2 e) {
    const unsigned a0 = __builtin_amdgcn_alignbit(d[1], d[0], 16), a1 = __builtin_amdgcn_alignbit(d[2], d[1], 16), a2 = __builtin_amdgcn_alignbit(d[3], d[2], 16),
                   a3 = __builtin_amdgcn_alignbit(e[0], d[3], 16), a4 = __builtin_amdgcn_alignbit(e[1], e[0], 16);
    *(LAS u32x4*)(q) = (u32x4){a0, a1, a2, a3};
    *(LAS u32x4*)(q + CV_HSTR) = (u32x4){d[1], d[2], d[3], e[0]};
    *(LAS u32x4*)(q + 2 * CV_HSTR) = (u32x4){a1, a2, a3, a4};
    *(LAS u32x4*)(q + 3 * CV_HSTR) = (u32x4){d[2], d[3], e[0], e[1]};
}
DI void conv_frags(bf16x8 (&A)[8], bf16x8 (&B)[8], ldscp hp, ldscp up) {
#pragma unroll
    for (int v = 0; v < 8; ++v) {
        B[v] = ldsrd16(up + (7 - v) * 512);
        const s16x4 lo = *(const LAS s16x4*)(hp + v * 512), hi = *(const LAS s16x4*)(hp + v * 512 + 8);
        A[v] = cat8(lo, hi);
    }
}
DI void conv_mma(f32x4 (&acc)[8], const bf16x8 (&A)[8], const bf16x8 (&B)[8]) {
#pragma unroll
    for (int J = 0; J < 8; ++J)
#pragma unroll
        for (int v = 0; v <= J; ++v) acc[J] = MFMA16(A[v], B[J - v], acc[J]);
}
DI void conv_part(ldsp lds, const K1Args& P, int c0, const u32x4 sw, const unsigned dw, int wid, int lane, const Out1& o) {
    const int tid = wid * 64 + lane, fr = lane & 15, fq = lane >> 4;
    {
        const int cq = tid & 255;
        const u32x4 d = *(const LAS u32x4*)(lds + CV_RAW + tid * 16);
        u32x2 e = *(const LAS u32x2*)(lds + CV_RAW + tid * 16 + 16);
        const u32x4 z4 = {0u, 0u, 0u, 0u}; const u32x2 z2 = {0u, 0u};
        if (cq == 255) e = z2;
        ldsp q = lds + CV_H + (tid >> 8) * 4 * CV_HSTR + (cq + 2) * 16;
        conv_copies(q, d, e);
        if (cq == 0) { conv_copies(q - 16, z4, (u32x2){d[0], d[1]}); conv_copies(q - 32, z4, z2); }
        if (cq == 255) { conv_copies(q + 16, z4, z2); conv_copies(q + 32, z4, z2); }
    }
    SBAR(); st_range<4, 6>(o); SBAR();
    {
        ldscp kp = lds + K1_X + tid * 16;
        u32x4 a = *(const LAS u32x4*)(kp - 16);
        const u32x4 b = *(const LAS u32x4*)(kp), vr = *(const LAS u32x4*)(kp + 8192);
        if (tid == 0) a = (u32x4){0u, 0u, 0u, 0u};
        const unsigned kr[7] = {a[1], a[2], a[3], b[0], b[1], b[2], b[3]};
#pragma unroll
        for (int c = 0; c < 2; ++c) {
            const float w0 = bf_lo(sw[2 * c]), w1 = bf_hi(sw[2 * c]), w2 = bf_lo(sw[2 * c + 1]), w3 = bf_hi(sw[2 * c + 1]);
            float kc[7], kv[4];
#pragma unroll
            for (int i = 0; i < 7; ++i) kc[i] = c ? bf_hi(kr[i]) : bf_lo(kr[i]);
#pragma unroll
            for (int i = 0; i < 4; ++i) {
                float s = rbf(kc[i] * w3);
                s = rbf(s + rbf(kc[i + 1] * w2));
                s = rbf(s + rbf(kc[i + 2] * w1));
                s = rbf(s + rbf(kc[i + 3] * w0));
                kv[i] = s * (c ? bf_hi(vr[i]) : bf_lo(vr[i]));
            }
            u32x2 wv = {cvtpk(kv[3], kv[2]), cvtpk(kv[1], kv[0])};
            *(LAS u32x2*)(lds + CV_U + c * CV_USTR + (2044 - 4 * tid) * 2) = wv;
            SBAR(); if (c == 0) st_range<6, 8>(o); else st_range<8, 10>(o); SBAR();
        }
        if (tid < 120) { const int c = tid / 60, j = tid - c * 60; *(LAS u32x2*)(lds + CV_U + c * CV_USTR + 4096 + j * 8) = (u32x2){0u, 0u}; }
    }
    __syncthreads();
    TS(P.ws, 0, 5);
    const int ch = wid >> 2, qw = wid & 3;
    {
        const int y1 = 24 + fr - 8 * fq;
        ldscp hb = lds + CV_H + ch * 4 * CV_HSTR + (y1 & 3) * CV_HSTR + (y1 & ~3) * 2 + qw * 128;
        ldscp ub = lds + CV_U + ch * CV_USTR + (2056 - 256 * 7 - 16 * fr - 8 * fq) * 2 + qw * 128;
        f32x4 acc[8];
#pragma unroll
        for (int i = 0; i < 8; ++i) acc[i] = (f32x4){0.f, 0.f, 0.f, 0.f};
        bf16x8 A0[8], B0[8], A1[8], B1[8];
        conv_frags(A0, B0, hb, ub); SBAR();
        conv_frags(A1, B1, hb + 64, ub + 64); SBAR();
        st_range<10, 12>(o); SBAR();
        conv_mma(acc, A0, B0); SBAR();
        st_range<12, 14>(o); SBAR();
        conv_mma(acc, A1, B1); SBAR();
        st_range<14, 16>(o); SBAR();
#pragma unroll
        for (int i = 0; i < 8; ++i) *(LAS f32x4*)(lds + CV_R + wid * 8192 + i * 1024 + lane * 16) = acc[i];
    }
    __syncthreads();
    TS(P.ws, 0, 6);
    {
        const float dd = ch ? bf_hi(dw) : bf_lo(dw);
#pragma unroll
        for (int i = 0; i < 2; ++i) {
            ldscp rp = lds + CV_R + ch * 32768 + (2 * qw + i) * 1024 + lane * 16;
            const f32x4 y = ((*(const LAS f32x4*)rp + *(const LAS f32x4*)(rp + 8192)) + *(const LAS f32x4*)(rp + 16384)) + *(const LAS f32x4*)(rp + 24576);
            const int t = 256 * (2 * qw + i) + 16 * fr + 4 * fq;
            const u32x2 uw = *(const LAS u32x2*)(lds + CV_U + ch * CV_USTR + (2044 - t) * 2);
            const float kv[4] = {bf_hi(uw[1]), bf_lo(uw[1]), bf_hi(uw[0]), bf_lo(uw[0])};
            float r[4];
#pragma unroll
            for (int e = 0; e < 4; ++e) r[e] = rbf(y[e]) + rbf(kv[e] * dd);
            u32x2 w = {cvtpk(r[0], r[1]), cvtpk(r[2], r[3])};
            *(u32x2*)(P.ws + WS_Y2 + ((size_t)(c0 + ch) * SEQ + t) * 2) = w;
        }
    }
}

DI void gate_part(const K1Args& P, int row0, int wid, int lane) {
    const int lc = lane < 56 ? lane : 0;
    u32x4 gw = *(const u32x4*)(P.gate_w + lc * 8);
    float* gp = (float*)(P.ws + WS_G);
    u32x4 xv[8];
#pragma unroll
    for (int r = 0; r < 8; ++r) xv[r] = *(const u32x4*)(P.x + (size_t)(row0 + wid * 8 + r) * DM + lc * 8);
    float mine = 0.f;
#pragma unroll
    for (int r = 0; r < 8; ++r) {
        float s = 0.f;
#pragma unroll
        for (int j = 0; j < 4; ++j) { s += rbf(bf_lo(xv[r][j]) * bf_lo(gw[j])); s += rbf(bf_hi(xv[r][j]) * bf_hi(gw[j])); }
        if (lane >= 56) s = 0.f;
#pragma unroll
        for (int o = 32; o >= 1; o >>= 1) s += __shfl_xor(s, o);
        if (lane == r) mine = s;
    }
    const float z = rbf(mine);
    const float g = rbf(1.0f / (1.0f + __expf(-z)));
    if (lane < 8) gp[row0 + wid * 8 + lane] = g;
}

template <bool VT>
DI void big_tile(const G1& g, const K1Args& P, int pm, int head, int c0, int e, int wid, bf16x8 (&At)[4][2], bf16x8 (&B0)[2][2], Out1& o) {
    f32x4 acc[2][2][4][2];
    zero8(acc[0][0]); zero8(acc[0][1]); zero8(acc[1][0]); zero8(acc[1][1]);
    bf16x8 B1[2][2];
    auto lastside = [&](int s) {
        if (wid < 4) side(g, 14 + s);
        else if (s == 0) {
            const unsigned l = (unsigned)lane_id();
            if (wid == 4) touch(g.sdst, l * 128u, (const char*)P.ssm_k + (size_t)c0 * (SEQ * 2));
            else touch(g.sdst, l * (DM * 2), wo_block(P, e) + (wid & 1) * (64 * DM * 2));
        }
    };
    mainpair<VT, 0>(g, 0, acc, At, B0, B1, lastside);
    for (int t = 2; t < 14; t += 2) mainpair<VT, 1>(g, t, acc, At, B0, B1, lastside);
    mainpair<VT, 2>(g, 14, acc, At, B0, B1, lastside);
    TS(P.ws, 0, 3);
    const int lane = lane_id();
    tail_stage(g, P, c0, e, wid, lane);
    if (wid == 7) CODE_TOUCH2(K2_SYM, g.sdst, e & 7, lane);
    SBAR();
    const int wr = wid >> 2, wc = wid & 3, fr = lane & 15, fq = lane >> 4;
    if (VT) {
        o.voff = (unsigned)((pm * 4 + wr) * 32768 + wc * 4096 + (fq >> 1) * 1024 + (fq & 1) * 512 + fr * 16);
        pack_v(acc[0][0], o.pk); pack_v(acc[0][1], o.pk + 4); pack_v(acc[1][0], o.pk + 8); pack_v(acc[1][1], o.pk + 12);
    } else {
        o.voff = kq_off(pm, wr, wc, fr, fq);
        pack_kq(acc[0][0], o.pk, 1.0f); pack_kq(acc[0][1], o.pk + 4, 1.0f); pack_kq(acc[1][0], o.pk + 8, 1.0f); pack_kq(acc[1][1], o.pk + 12, 1.0f);
    }
}

__global__ __launch_bounds__(512, 2) void k1_proj(K1Args P) {
    extern __shared__ __attribute__((aligned(16))) unsigned char shm[];
    ldsp lds = (ldsp)shm;
    const int b = blockIdx.x, xcd = b & 7, j = b >> 3, li = j & 15, pm = li & 7, head = 2 * xcd + (li >> 3), vt = j >> 4;
    const int tid = threadIdx.x, wid = rfl(tid >> 6), lane = tid & 63, wr = wid >> 2, wc = wid & 3, fr = lane & 15, fq = lane >> 4;
    const int c0 = (xcd * 28 + (j < 28 ? j : 0)) * 2, e = xcd * 32 + j;
    TS(P.ws, 0, 0);
    if (wid == 7) CODE_TOUCH2(K1_SYM, (unsigned)(uintptr_t)lds + K1_X + 16384 + 7 * 256, j, lane);
    u32x4 sw = *(const CAS u32x4*)(uintptr_t)(P.shift_k + c0 * 4); unsigned dw = *(const CAS unsigned*)(uintptr_t)(P.ssm_d + c0);
    G1 g;
    const unsigned lds0 = (unsigned)(uintptr_t)lds;
    g.lds = lds; g.ldsw = lds0 + (unsigned)wid * 1024u;
    { int R, C; stage_rc(tid * 16, R, C); g.voffA_kv = (unsigned)(R * DC + C) * 2u; g.voffA_q = (unsigned)(R * DM + C) * 2u; }
    { const int kg = wid >> 1, row = (lane >> 2) & 7, ch = (lane & 3) ^ (2 * (kg & 1)), n = 32 * (2 * (wid & 1) + (lane >> 5)) + 8 * ch;
      g.voffB = (unsigned)((8 * kg + row) * NW_ + n) * 2u; }
    g.aoff = lds_byte(wr * 64 + fr, fq * 8);
    g.boff0 = fq * 2048 + wc * 512 + (fr >> 2) * 64 + (32 * (fq & 1)) + 8 * (fr & 3);
    g.boff1 = g.boff0 ^ 32;
    g.A_kv = (const char*)P.ckv + (size_t)pm * 256 * DC * 2;
    g.A_q = (const char*)P.x + (size_t)pm * 256 * DM * 2;
    g.B_kv = (const char*)(vt ? P.wuv : P.wuk) + head * 512;
    g.B_q = (const char*)P.wq + head * 512 + vt * 256;
    if (wid < 4) {
        g.sbase = (const char*)(wid < 2 ? P.ak : P.av) + (wid & 1) * (64 * DM * 2) + c0 * 2; g.sstep = 128 * DM * 2; g.sv = (unsigned)lane * (DM * 2);
        g.sadd = 0; g.sdst = lds0 + K1_X + (wid >> 1) * 8192 + (wid & 1) * 256; g.sdstep = 512;
    } else {
        if (wid == 4) { g.sbase = g.A_kv + (size_t)((head & 1) * 2 + vt) * 64 * (DC * 2); g.sstep = 128; g.sv = (unsigned)lane * (DC * 2); }
        else if (wid == 5) { g.sbase = g.B_kv + (size_t)pm * 8 * (NW_ * 2); g.sstep = 64 * NW_ * 2; g.sv = (unsigned)((lane >> 2) & 7) * (NW_ * 2) + (lane & 3) * 128; }
        else { g.sbase = (const char*)(wid == 6 ? P.ak : P.av) + (size_t)j * 4 * (DM * 2) + xcd * 112; g.sstep = 128 * DM * 2; g.sv = (unsigned)(lane & 3) * (DM * 2) + ((lane >> 2) & 1) * 108; }
        g.sadd = 4; g.sdst = lds0 + K1_X + 16384 + wid * 256; g.sdstep = 0;
    }
    PIN(g.voffA_kv); PIN(g.voffB); PIN(g.sv); PIN(g.aoff); PIN(g.boff0); PIN(g.boff1);
    stage_units(g, std::make_integer_sequence<int, 8>{});
    WAIT_V(12);
    auto warm = [&](int slot) {
        const unsigned sink = lds0 + K1_X + 16384 + wid * 256;
        if (slot > 0) { if (wid >= 4) side(g, slot - 5); }
        else if (wid < 3) touch(sink, (unsigned)lane * (DM * 2), g.A_q + (size_t)((head & 1) * 2 + vt) * 64 * (DM * 2) + (4 + wid) * 128);
        else if (wid == 3) touch(sink, (unsigned)((lane < 48 ? lane : 0) >> 1) * (NW_ * 2) + (lane & 1) * 128, g.B_q + (size_t)(256 + pm * 24) * (NW_ * 2));
    };
    asm volatile("" : "+s"(sw), "+s"(dw));
    if (b == 0 && tid < N_AFLAGS + N_CFLAGS) {
#pragma unroll
        for (int q = 0; q < 4; ++q) *(u32x4*)((unsigned*)(P.ws + WS_FLAGS) + tid * FLAG_STRIDE + 4 * q) = (u32x4){0u, 0u, 0u, 0u};
    }
    bf16x8 At[4][2], B0[2][2];
    BAR;
    if (wr == 1) BAR;
    TS(P.ws, 0, 1);
    {
        f32x4 q0[4][2], q1[4][2];
        zero8(q0); zero8(q1);
        qphases(g, q0, q1, At, B0, warm, std::make_integer_sequence<int, 14>{});
        stage_unit<27>(g); stage_unit<28>(g); SBAR();
        Out1 o;
        o.rs = __builtin_amdgcn_make_buffer_rsrc(P.ws + WS_Q + (size_t)head * MB, (short)0, (int)MB, 0x00020000);
        o.voff = kq_off(pm, wr, wc, fr, fq) + vt * 8192; o.sbj = 65536; o.shi = 16384;
        pack_kq(q0, o.pk, 0.0625f * 1.4426950408889634f); pack_kq(q1, o.pk + 4, 0.0625f * 1.4426950408889634f);
        st_range<0, 8>(o);
        SBAR();
    }
    TS(P.ws, 0, 2);
    Out1 o;
    if (vt) big_tile<true>(g, P, pm, head, c0, e, wid, At, B0, o); else big_tile<false>(g, P, pm, head, c0, e, wid, At, B0, o);
    o.rs = __builtin_amdgcn_make_buffer_rsrc(P.ws + (vt ? WS_V : WS_K) + (size_t)head * MB, (short)0, (int)MB, 0x00020000);
    o.sbj = vt ? 16384 : 8192; o.shi = vt ? 2048 : 16384;
    SBAR(); st_range<0, 4>(o); SBAR();
    WAIT_V(4);
    if (wr == 0) BAR;
    BAR;
    TS(P.ws, 0, 4);
    const int lane2 = lane_id();
    wo_image(lds, P, e, wid, lane2);
    if (j < 28) conv_part(lds, P, c0, sw, dw, wid, lane2, o);
    else { st_range<4, 16>(o); gate_part(P, (xcd * 4 + (j - 28)) * 64, wid, lane2); }
    TS(P.ws, 0, 7);
}
#undef SA
#undef SB

constexpr int AT_K = 0, AT_V = 65536, AT_LDS = 163840;
constexpr float THR = 8.0f;
constexpr int PD = 6;
constexpr int NEB = 24;
template <int... Is> using iseq = std::integer_sequence<int, Is...>;
template <int N> using make_iseq = std::make_integer_sequence<int, N>;
#define ACLOB "a0","a1","a2","a3","a4","a5","a6","a7","a8","a9","a10","a11","a12","a13","a14","a15","a16","a17","a18","a19","a20","a21","a22","a23","a24","a25","a26","a27","a28","a29","a30","a31","a32","a33","a34","a35","a36","a37","a38","a39","a40","a41","a42","a43","a44","a45","a46","a47","a48","a49","a50","a51","a52","a53","a54","a55","a56","a57","a58","a59","a60","a61","a62","a63","a64","a65","a66","a67","a68","a69","a70","a71","a72","a73","a74","a75","a76","a77","a78","a79","a80","a81","a82","a83","a84","a85","a86","a87","a88","a89","a90","a91","a92","a93","a94","a95","a96","a97","a98","a99","a100","a101","a102","a103","a104","a105","a106","a107","a108","a109","a110","a111","a112","a113","a114","a115","a116","a117","a118","a119","a120","a121","a122","a123","a124","a125","a126","a127","a128","a129","a130","a131","a132","a133","a134","a135","a136","a137","a138","a139","a140","a141","a142","a143","a144","a145","a146","a147","a148","a149","a150","a151","a152","a153","a154","a155","a156","a157","a158","a159","a160","a161","a162","a163","a164","a165","a166","a167","a168","a169","a170","a171","a172","a173","a174","a175","a176","a177","a178","a179","a180","a181","a182","a183","a184","a185","a186","a187","a188","a189","a190","a191","a192","a193","a194","a195","a196","a197","a198","a199","a200","a201","a202","a203","a204","a205","a206","a207","a208","a209","a210","a211","a212","a213","a214","a215","a216","a217","a218","a219","a220","a221","a222","a223","a224","a225","a226","a227","a228","a229","a230","a231","a232","a233","a234","a235","a236","a237","a238","a239","a240","a241","a242","a243","a244","a245","a246","a247","a248","a249","a250","a251","a252","a253","a254","a255"

constexpr int op_slot(int s) { switch (s % 5) { case 0: return 0; case 1: return 32768; case 2: return 65536; case 3: return 131072; default: return 98304; } }
static_assert(AT_V + 32768 == 98304 && AT_LDS == 163840, "lds");
struct OPJ {
    const char *wbase, *xp, *yp, *gp, *pb;
    unsigned cv, lds0, xo, yo, po; ldscp wp;
    const unsigned* fl; unsigned* pub;
    unsigned look, done;
    u32x2 xw[4]; unsigned yw[16]; float g; unsigned char* ws;
};
struct AT {
    OPJ o;
    u32x2 pk;
    f32x2 ml2;
    const char *part, *mlp;
    bool isA, early;
    unsigned* flag;
    f32x16 s[2][2];
    f32x16 cm[2];
    u32x4 p[4];
    bf16x8 fr[8];
    float m, l, rs0, rs1, mx0, mx1, alpha, aone;
    bool pend, first, drain;
    ldscp kb;
    ldscp vcur;
    unsigned goff;
    unsigned m0k, m0v;
    const char *gk, *gv;
};

DI void dma_piece(unsigned m0v, unsigned goff, const char* g, const int off) {
    switch (off) {
    case 0: asm volatile("s_mov_b32 m0, %0\n\ts_nop 0\n\tglobal_load_lds_dwordx4 %1, %2" :: "s"(m0v), "v"(goff), "s"(g) : "memory"); break;
    case 1: asm volatile("s_mov_b32 m0, %0\n\ts_nop 0\n\tglobal_load_lds_dwordx4 %1, %2 offset:1024" :: "s"(m0v), "v"(goff), "s"(g) : "memory"); break;
    case 2: asm volatile("s_mov_b32 m0, %0\n\ts_nop 0\n\tglobal_load_lds_dwordx4 %1, %2 offset:2048" :: "s"(m0v), "v"(goff), "s"(g) : "memory"); break;
    default: asm volatile("s_mov_b32 m0, %0\n\ts_nop 0\n\tglobal_load_lds_dwordx4 %1, %2 offset:3072" :: "s"(m0v), "v"(goff), "s"(g) : "memory"); break;
    }
}
template <int I> DI void dma8(unsigned m0base, unsigned goff, const char* g) { dma_piece(m0base + (I >> 2) * 4096, goff, g + (I >> 2) * 4096, I & 3); }
DI void dma_tile(unsigned m0base, unsigned goff, const char* g) {
    dma8<0>(m0base, goff, g); dma8<1>(m0base, goff, g); dma8<2>(m0base, goff, g); dma8<3>(m0base, goff, g);
    dma8<4>(m0base, goff, g); dma8<5>(m0base, goff, g); dma8<6>(m0base, goff, g); dma8<7>(m0base, goff, g);
}
DI float ex2(float x) { return __builtin_amdgcn_exp2f(x); }
DI float MX3(float a, float b, float c) { float r; asm("v_max3_f32 %0, %1, %2, %3" : "=v"(r) : "v"(a), "v"(b), "v"(c)); return r; }
DI float MX2(float a, float b) { float r; asm("v_max_f32_e32 %0, %1, %2" : "=v"(r) : "v"(a), "v"(b)); return r; }
template <int D0> DI void qk_init(f32x16& s, const bf16x8& k, const f32x16& c) {
    asm volatile("v_mfma_f32_32x32x16_bf16 %0, %1, a[%c3:%c4], %2" : "=&v"(s) : "v"(k), "v"(c), "i"(128 + 4 * D0), "i"(131 + 4 * D0)); }
template <int D0> DI void qk_acc(f32x16& s, const bf16x8& k) {
    asm volatile("v_mfma_f32_32x32x16_bf16 %0, %1, a[%c2:%c3], %0" : "+v"(s) : "v"(k), "i"(128 + 4 * D0), "i"(131 + 4 * D0)); }
template <int DB> DI void pv_acc(const bf16x8& v, const u32x4& p) {
    asm volatile("v_mfma_f32_32x32x16_bf16 a[%c2:%c3], %0, %1, a[%c2:%c3]" :: "v"(v), "v"(p), "i"(16 * DB), "i"(16 * DB + 15)); }
DI void add_bcast(f32x16& x, float aone, float b) { asm volatile("s_nop 1\n\tv_mfma_f32_32x32x2_f32 %0, %1, %2, %0" : "+v"(x) : "v"(aone), "v"(b)); }
#define NOPS16() asm volatile("s_nop 7\n\ts_nop 7" ::: "memory")
#define NOPS2() asm volatile("s_nop 1" ::: "memory")
template <int I> DI void a_zero() { asm volatile("v_accvgpr_write_b32 a[%c0], 0" :: "i"(I)); }
template <int I> DI void a_scale(float f) { float t; asm volatile("v_accvgpr_read_b32 %0, a[%c2]\n\tv_mul_f32 %0, %1, %0\n\tv_accvgpr_write_b32 a[%c2], %0" : "=&v"(t) : "v"(f), "i"(I)); }
template <int I> DI void a_qmov() { asm volatile("v_accvgpr_mov_b32 a[%c0], a[%c1]" :: "i"(128 + I), "i"(192 + I)); }
template <int I> DI float a_read() { float t; asm volatile("v_accvgpr_read_b32 %0, a[%c1]" : "=v"(t) : "i"(I)); return t; }
template <int F> DI void q_load(unsigned voff, const char* g) {
    asm volatile("global_load_dwordx4 a[%c2:%c3], %0, %1 offset:%c4" :: "v"(voff), "s"(g + ((F & 15) >> 2) * 4096), "i"(128 + 4 * F), "i"(131 + 4 * F), "i"((F & 3) * 1024) : "memory"); }
template <int K> DI void pk_store(const AT& c, const u32x4 pk) {
    asm volatile("global_store_dwordx4 %0, %1, %2 offset:%c3 sc1\n\ts_nop 1" :: "v"(c.o.cv), "v"(pk), "s"(c.part + (K >> 2) * 4096), "i"((K & 3) * 1024) : "memory"); }
template <int K> DI void p_load(const AT& c) {
    asm volatile("global_load_dwordx4 a[%c2:%c3], %0, %1 offset:%c4 sc1" :: "v"(c.o.cv), "s"(c.part + (K >> 2) * 4096), "i"(192 + 4 * K), "i"(195 + 4 * K), "i"((K & 3) * 1024) : "memory"); }
DI void ml_load(AT& c) { asm volatile("global_load_dwordx2 %0, %1, %2 sc1" : "+v"(c.ml2) : "v"((c.o.cv & 496u) >> 1), "s"(c.mlp) : "memory"); }
template <int I> DI void op_wpiece(const OPJ& o) {
    constexpr int S = I >> 3, H = (I & 4) * 1024;
    dma_piece(o.lds0 + op_slot(S) + H, o.cv, o.wbase + S * 32768 + H, I & 3);
}
template <int DB> DI void pv_new(const bf16x8& v, const u32x4& p) {
    asm volatile("v_mfma_f32_32x32x16_bf16 a[%c2:%c3], %0, %1, 0" :: "v"(v), "v"(p), "i"(16 * DB), "i"(16 * DB + 15)); }
template <int... Is> DI void a_zeros(iseq<Is...>) { (a_zero<Is>(), ...); }
template <int... Is> DI void a_scales(float f, iseq<Is...>) { (a_scale<Is>(f), ...); }
template <int... Is> DI void a_qmovs(iseq<Is...>) { (a_qmov<Is>(), ...); }
template <int B, int... Is> DI void q_loads(unsigned voff, const char* g, iseq<Is...>) { (q_load<B + Is>(voff, g), ...); }
template <int... Is> DI void p_loads(const AT& c, iseq<Is...>) { (p_load<Is>(c), ...); }
template <int B, int... Is> DI void a_reads(float (&o)[16], iseq<Is...>) { ((o[Is] = a_read<B + Is>()), ...); }

template <int KS, int R> DI bf16x8 kfrag(const AT& c) { return ldsrd16(c.kb + KS * 32768 + ((R & 1) * 16 + (R >> 1)) * 1024); }
template <int R, bool DBM = false> DI bf16x8 vfrag(ldscp v) {
    return ldsrd16(v + (DBM ? (R >> 2) * 4096 + (R & 3) * 1024 : (R & 7) * 4096 + (R >> 3) * 1024)); }
template <int Q> DI void pk_quad(AT& c) {
    const float t0 = a_read<4 * Q>(), t1 = a_read<4 * Q + 1>(), t2 = a_read<4 * Q + 2>(), t3 = a_read<4 * Q + 3>();
    if constexpr (Q & 1) pk_store<(Q >> 1)>(c, (u32x4){c.pk[0], c.pk[1], cvtpk(t0, t1), cvtpk(t2, t3)});
    else { c.pk = (u32x2){cvtpk(t0, t1), cvtpk(t2, t3)}; PIN(c.pk); }
}
template <int X, int E> DI void exp_sum(AT& c) {
    const float e = ex2(c.s[X][E >> 4][E & 15]); c.s[X][E >> 4][E & 15] = e;
    if constexpr (E & 1) c.rs1 += e; else c.rs0 += e;
}
template <int X, int W> DI void pack1(AT& c) {
    c.p[W >> 2][W & 3] = cvtpk(c.s[X][W >> 3][(2 * W) & 15], c.s[X][W >> 3][(2 * W + 1) & 15]);
}
template <int X, int MODE, int N> DI void gapA(AT& c) {
    constexpr int d0 = N >> 1, kb = N & 1, Y = X ^ 1;
    if constexpr (d0 == 0) qk_init<0>(c.s[Y][kb], c.fr[N & 7], c.cm[(MODE & 4) ? kb : 0]);
    else qk_acc<d0>(c.s[Y][kb], c.fr[N & 7]);
    constexpr int R = N + PD;
    if constexpr (R < 32) c.fr[R & 7] = kfrag<Y, R>(c); else if constexpr (MODE & 1) c.fr[R & 7] = vfrag<R - 32, (MODE & 18) == 18>(c.vcur);
    if constexpr (MODE & 1) {
        if constexpr (N < 32 - NEB) { exp_sum<X, NEB + N>(c); PIN(c.rs0); PIN(c.rs1); }
        if constexpr (N >= 10 && N < 26) { pack1<X, N - 10>(c); PIN(c.p[(N - 10) >> 2]); }
    }
    if constexpr ((MODE & 4) && N == 2) asm volatile("" :: "v"(c.cm[0]), "v"(c.cm[1]));
    if constexpr ((MODE & 18) == 16) pk_quad<N>(c);
    if constexpr ((MODE & 18) == 18 && N >= 24) pk_quad<N - 24>(c);
    if constexpr ((MODE & 2) && (N & 1)) {
        if constexpr (N < 16) dma8<(N >> 1)>(c.m0k, c.goff, c.gk); else dma8<((N - 16) >> 1)>(c.m0v, c.goff, c.gv);
    }
    SBAR();
}
template <int X> DI void decide(AT& c) {
    constexpr int Y = X ^ 1;
    float mm = MX2(c.mx0, c.mx1);
    auto rr = __builtin_amdgcn_permlane32_swap(__float_as_uint(mm), __float_as_uint(mm), false, false);
    const float rm = MX2(__uint_as_float(rr[0]), __uint_as_float(rr[1]));
    if (__builtin_expect(c.first || __any(rm > THR), 0)) {
        const float dl = c.first ? rm : MX2(rm, 0.f);
        c.m += dl; c.alpha = ex2(-dl); c.pend = !c.first;
        const float nd = -dl;
        add_bcast(c.s[Y][0], c.aone, nd); add_bcast(c.s[Y][1], c.aone, nd); add_bcast(c.cm[0], c.aone, nd);
        asm volatile("s_nop 7\n\ts_nop 7\n\ts_nop 3" : "+v"(c.s[Y][0]), "+v"(c.s[Y][1]), "+v"(c.cm[0]));
    }
}
template <int X, int I> DI void maxstep(AT& c) {
    constexpr int Y = X ^ 1, h = I >> 2, r = 4 * (I & 3);
    if constexpr (I == 0) { c.mx0 = MX2(c.s[Y][0][0], c.s[Y][0][1]); c.mx1 = MX2(c.s[Y][0][2], c.s[Y][0][3]); }
    else { c.mx0 = MX3(c.mx0, c.s[Y][h][r], c.s[Y][h][r + 1]); c.mx1 = MX3(c.mx1, c.s[Y][h][r + 2], c.s[Y][h][r + 3]); }
}
template <int X, int MODE, int N> DI void gapB(AT& c) {
    constexpr bool DBM = (MODE & 18) == 18;
    constexpr int ks = DBM ? N & 3 : N >> 3, db = DBM ? N >> 2 : N & 7, Y = X ^ 1;
    if constexpr ((MODE & 16) && ks == 0) pv_new<db>(c.fr[N & 7], c.p[ks]); else pv_acc<db>(c.fr[N & 7], c.p[ks]);
    if constexpr (DBM && N < 24) pk_quad<N + 8>(c);
    if constexpr ((MODE & 32) && N == 10) {
        if (c.isA) {
            asm volatile("s_waitcnt vmcnt(0)" ::: "memory");
            c.early = rfl(__float_as_uint(a_read<192>())) != 0u;
            __builtin_amdgcn_fence(__ATOMIC_ACQUIRE, "wavefront");
            if (c.early) ml_load(c);
        }
    }
    if constexpr ((MODE & 32) && N > 10 && N < 27) { if (c.early) p_load<N - 11>(c); }
    if constexpr ((MODE & 48) == 48 && N == 16) { asm volatile("s_waitcnt vmcnt(0)" ::: "memory"); __hip_atomic_store(c.flag, 1u, RLX_AGENT); }
    if constexpr ((MODE & 48) == 32 && N == 28) asm volatile("global_load_dword a128, %0, %1 sc1" :: "v"(0u), "s"(c.flag) : "memory");
    if constexpr (MODE & 64) op_wpiece<N>(c.o);
    constexpr int R = N + PD;
    if constexpr (R < 32) c.fr[R & 7] = vfrag<R, DBM>(c.vcur);
    if constexpr (MODE & 1) {
        if constexpr (MODE & 2) {
            if constexpr (N == 28) { c.fr[0] = kfrag<X, 0>(c); c.fr[1] = kfrag<X, 1>(c); }
            if constexpr (N == 29) { c.fr[2] = kfrag<X, 2>(c); c.fr[3] = kfrag<X, 3>(c); }
            if constexpr (N == 30) { c.fr[4] = kfrag<X, 4>(c); }
            if constexpr (N == 31) { c.fr[5] = kfrag<X, 5>(c); }
        }
        if constexpr (N == 1) { asm volatile("s_nop 7" : "+v"(c.s[Y][0]), "+v"(c.s[Y][1])); }
        if constexpr (N >= 2 && N < 6) { maxstep<X, 2 * (N - 2)>(c); maxstep<X, 2 * (N - 2) + 1>(c); PIN(c.mx0); PIN(c.mx1); }
        if constexpr (N == 6) { c.l += c.rs0 + c.rs1; c.rs0 = 0.f; c.rs1 = 0.f; PIN(c.l); }
        if constexpr (N == 7) decide<X>(c);
        if constexpr (N >= 8) { exp_sum<Y, N - 8>(c); PIN(c.rs0); PIN(c.rs1); }
    }
    SBAR();
    if constexpr ((MODE & 1) && N == 27) {
        if constexpr (MODE & 2) {
            if (X == 0 && c.drain) asm volatile("s_waitcnt vmcnt(0)" ::: "memory");
            if constexpr (MODE & 16) asm volatile("s_waitcnt vmcnt(24) lgkmcnt(0)\n\ts_barrier" ::: "memory");
            else asm volatile("s_waitcnt vmcnt(8) lgkmcnt(0)\n\ts_barrier" ::: "memory");
        } else {
            if (!c.isA) asm volatile("s_waitcnt vmcnt(0)" ::: "memory");
            asm volatile("s_waitcnt lgkmcnt(0)\n\ts_barrier" ::: "memory");
        }
        SBAR();
    }
}
template <int X, int MODE, int... Ns> DI void phaseA(AT& c, iseq<Ns...>) { (gapA<X, MODE, Ns>(c), ...); }
template <int X, int MODE, int... Ns> DI void phaseB(AT& c, iseq<Ns...>) { (gapB<X, MODE, Ns>(c), ...); }
template <int X, int B, int... Es> DI void exps_from(AT& c, iseq<Es...>) { (exp_sum<X, B + Es>(c), ...); }
template <int X, int... Ws> DI void packs(AT& c, iseq<Ws...>) { (pack1<X, Ws>(c), ...); }
template <int... Is> DI void maxsteps1(AT& c, iseq<Is...>) { (maxstep<1, Is>(c), ...); }
DI void rescale_o(AT& c) {
    if (__builtin_expect(c.pend, 0)) {
        NOPS16();
        a_scales(c.alpha, make_iseq<128>{});
        NOPS2();
        c.l *= c.alpha; c.pend = false;
    }
}
DI float halfsum(float x) { auto rr = __builtin_amdgcn_permlane32_swap(__float_as_uint(x), __float_as_uint(x), false, false); return __uint_as_float(rr[0]) + __uint_as_float(rr[1]); }
DI void build_mask(AT& c, int mrel) {
    const float NEG = -__builtin_inff(), nm = -c.m;
#pragma unroll
    for (int r = 0; r < 16; ++r) { const int kk = (r & 3) + 8 * (r >> 2); c.cm[0][r] = kk > mrel ? NEG : nm; c.cm[1][r] = kk + 32 > mrel ? NEG : nm; }
    PIN(c.cm[0]); PIN(c.cm[1]); NOPS2();
}

struct ATW {
    const char *Kh, *Vh; ldsp lds; unsigned mk, mv; int n1, vs, lane, wid, r32, hi, qrel; bool isA;
};
DI int tile_of(const ATW& w, int g) { return w.isA ? w.n1 + g : (g < w.n1 ? g : g - w.n1); }
template <int X, bool DMA> DI void setup(AT& c, const ATW& w, int g) {
    c.vcur = w.lds + AT_V + w.vs * 32768 + w.lane * 16;
    if constexpr (DMA) {
        const int v2 = w.vs == 0 ? 2 : w.vs - 1, tn = tile_of(w, g + 2);
        c.gk = w.Kh + (size_t)tn * 32768; c.gv = w.Vh + (size_t)tn * 32768; c.m0k = w.mk + X * 32768; c.m0v = w.mv + v2 * 32768;
    }
}
template <bool LAST, bool seam> DI void pair(AT& c, ATW& w, int g0) {
    setup<0, true>(c, w, g0);
    c.first = seam;
    const float m_old = c.m;
    if constexpr (seam) { a_qmovs(make_iseq<64>{}); NOPS2(); }
    int qr = w.qrel;
    if constexpr (LAST) {
        if constexpr (seam) asm volatile("; last pair, two segments" : "+v"(qr), "+v"(c.s[0][0]), "+v"(c.s[0][1]), "+v"(c.m));
        else asm volatile("; last pair" : "+v"(qr), "+v"(c.s[0][0]), "+v"(c.s[0][1]), "+v"(c.m));
        build_mask(c, qr);
    }
    SBAR();
    phaseA<0, LAST ? 7 : 3>(c, make_iseq<32>{});
    phaseB<0, 3>(c, make_iseq<32>{});
    rescale_o(c);
    if (c.drain) { __hip_atomic_store(c.flag, 1u, RLX_AGENT); c.drain = false; }
    c.first = false;
    w.vs = w.vs == 2 ? 0 : w.vs + 1;
    setup<1, !LAST>(c, w, g0 + 1);
    if constexpr (LAST || seam) asm volatile("" : "+s"(c.part));
    if constexpr (LAST) {
        asm volatile("global_load_dword a192, %0, %1 sc1" :: "v"(0u), "s"(c.flag) : "memory");
        PIN(qr); build_mask(c, qr - 64);
    }
    if constexpr (seam) {
        const float lt = halfsum(c.l);
        __hip_atomic_store((unsigned long long*)c.mlp + w.r32, ((unsigned long long)__float_as_uint(lt) << 32) | __float_as_uint(m_old), RLX_AGENT);
        c.l = 0.f;
        asm volatile("; second segment" : "+v"(c.s[1][0]), "+v"(c.s[1][1]), "+v"(c.rs0), "+v"(c.rs1), "+v"(c.l));
        SBAR();
        phaseA<1, (LAST ? 5 : 3) | 16>(c, make_iseq<32>{});
        phaseB<1, (LAST ? 33 : 3) | 16>(c, make_iseq<32>{});
        if constexpr (!LAST) c.drain = true;
    } else {
        SBAR();
        phaseA<1, LAST ? 5 : 3>(c, make_iseq<32>{});
        phaseB<1, LAST ? 33 : 3>(c, make_iseq<32>{});
    }
    rescale_o(c);
    w.vs = w.vs == 2 ? 0 : w.vs + 1;
}

constexpr int op_blk(int mb) { return (mb + 8) % 14; }
template <int R> DI bf16x8 op_wfrag(const OPJ& o) { return ldsrd16(o.wp + op_slot(R >> 5) + (R & 31) * 1024); }
template <int I> DI void op_eload(OPJ& o) {
    if constexpr (I < 12) asm volatile("global_load_ushort %0, %1, %2" : "=v"(o.yw[I]) : "v"(o.yo), "s"(o.yp + ((I >> 2) * 128 + (I & 3)) * (SEQ * 2)) : "memory");
    else if constexpr (I < 16) asm volatile("global_load_ushort %0, %1, %2" : "=v"(o.yw[I]) : "v"(o.yo & 0x3ffffu), "s"(o.yp + (384 + (I & 3)) * (SEQ * 2)) : "memory");
    else if constexpr (I < 19) asm volatile("global_load_dwordx2 %0, %1, %2 offset:%c3" : "=v"(o.xw[I - 16]) : "v"(o.xo), "s"(o.xp), "i"((I - 16) * 256) : "memory");
    else if constexpr (I == 19) asm volatile("global_load_dwordx2 %0, %1, %2 offset:768" : "=v"(o.xw[3]) : "v"(o.xo - ((o.cv >> 2) & 128u)), "s"(o.xp) : "memory");
    else asm volatile("global_load_dword %0, %1, %2" : "=v"(o.g) : "v"((o.cv & 240u) >> 2), "s"(o.gp) : "memory");
}
constexpr int op_land(int st) { return st < 2 ? 224 + 16 * st : 96 + 16 * st; }
template <int I> DI void op_pload(const OPJ& o) {
    asm volatile("global_load_dwordx4 a[%c2:%c3], %0, %1 sc1" :: "v"(o.po), "s"(o.pb + (I & 3) * 458752 + (I >> 2) * 4096),
                 "i"(op_land(I >> 2) + 4 * (I & 3)), "i"(op_land(I >> 2) + 4 * (I & 3) + 3) : "memory"); }
template <int S> DI void op_take(OPJ& o) {
    if (o.done == S && ((unsigned)__ballot(o.look > (unsigned)S) & 0xffffu) == 0xffffu) {
        op_pload<4 * S>(o); op_pload<4 * S + 1>(o); op_pload<4 * S + 2>(o); op_pload<4 * S + 3>(o); o.done = S + 1; }
}
template <int... Ss> DI void op_takes(OPJ& o, iseq<Ss...>) { __builtin_amdgcn_fence(__ATOMIC_ACQUIRE, "wavefront"); (op_take<Ss>(o), ...); }
DI void op_look(const OPJ& o) { asm volatile("global_load_dword a208, %0, %1 sc1" :: "v"((o.cv >> 2) & 60u), "s"(o.fl) : "memory"); }
template <int N> DI void op_wait(unsigned n) {
    if (N == 0 || n >= N) asm volatile("s_waitcnt vmcnt(%c0)" :: "i"(4 * N) : "memory");
    else if constexpr (N > 0) op_wait<N - 1>(n);
}
DI void op_publish(const OPJ& o, unsigned n) { asm volatile("global_store_dword %0, %1, %2 sc1" :: "v"(0u), "v"(n), "s"(o.pub) : "memory"); }
template <int Q> DI void op_quad(AT& c) {
    constexpr int A = 16 * op_blk(Q >> 2) + 4 * (Q & 3);
    const float t0 = a_read<A>(), t1 = a_read<A + 1>(), t2 = a_read<A + 2>(), t3 = a_read<A + 3>();
    if constexpr (Q & 1) pk_store<(Q >> 1)>(c, (u32x4){c.pk[0], c.pk[1], cvtpk(t0, t1), cvtpk(t2, t3)});
    else { c.pk = (u32x2){cvtpk(t0, t1), cvtpk(t2, t3)}; PIN(c.pk); }
}
template <bool MERGE, int Q> DI void ctx_quad(float f1, float f2, u32x4 (&bf)[16]) {
    const float o0 = a_read<4 * Q>(), o1 = a_read<4 * Q + 1>(), o2 = a_read<4 * Q + 2>(), o3 = a_read<4 * Q + 3>();
    if constexpr (MERGE) {
        const unsigned p0 = __float_as_uint(a_read<192 + 2 * Q>()), p1 = __float_as_uint(a_read<193 + 2 * Q>());
        bf[Q >> 1][2 * (Q & 1)] = cvtpk(o0 * f1 + bf_lo(p0) * f2, o1 * f1 + bf_hi(p0) * f2);
        bf[Q >> 1][2 * (Q & 1) + 1] = cvtpk(o2 * f1 + bf_lo(p1) * f2, o3 * f1 + bf_hi(p1) * f2);
    } else { bf[Q >> 1][2 * (Q & 1)] = cvtpk(o0 * f1, o1 * f1); bf[Q >> 1][2 * (Q & 1) + 1] = cvtpk(o2 * f1, o3 * f1); }
    if constexpr (Q & 1) PIN(bf[Q >> 1]);
}
template <bool MERGE, int G> DI void op_step(AT& c, u32x4 (&bf)[16], float f1, float f2) {
    constexpr int S = G >> 5, N = G & 31;
    if constexpr (N < 2) pv_new<op_blk(2 * S + N)>(c.fr[G & 7], bf[0]); else pv_acc<op_blk(2 * S + (N & 1))>(c.fr[G & 7], bf[N >> 1]);
    if constexpr (N == 32 - PD && S < 4) {
        constexpr int CNT[4] = {45, 41, 45, 4};
        asm volatile("s_waitcnt vmcnt(%c0)\n\ts_barrier" :: "i"(CNT[S]) : "memory");
    }
    if constexpr (N == 32 - PD && S >= 3) {
        if constexpr (S > 3) asm volatile("s_waitcnt vmcnt(5)" ::: "memory");
        op_publish(c.o, S - 1);
    }
    if constexpr (G + PD < 224) c.fr[(G + PD) & 7] = op_wfrag<G + PD>(c.o);
    if constexpr (G < 8) op_wpiece<32 + G>(c.o);
    if constexpr (G >= 5 && G < 26) op_eload<G - 5>(c.o);
    if constexpr (G < 28) ctx_quad<MERGE, G + 4>(f1, f2, bf);
    if constexpr (G >= 58 && G < 66) op_wpiece<40 + G - 58>(c.o);
    if constexpr (G >= 90 && G < 98) op_wpiece<48 + G - 90>(c.o);
    if constexpr (S >= 1 && N >= 2 && N <= 23 && (N - 2) % 3 == 0) op_quad<8 * (S - 1) + (N - 2) / 3>(c);
    SBAR();
}
template <bool MERGE, int B, int... Is> DI void op_steps(AT& c, u32x4 (&bf)[16], float f1, float f2, iseq<Is...>) { (op_step<MERGE, B + Is>(c, bf, f1, f2), ...); }
template <bool MERGE> DI void op_first(AT& c, u32x4 (&bf)[16], float f1, float f2) {
    SBAR(); NOPS16(); SBAR();
    ctx_quad<MERGE, 0>(f1, f2, bf); ctx_quad<MERGE, 1>(f1, f2, bf); ctx_quad<MERGE, 2>(f1, f2, bf); ctx_quad<MERGE, 3>(f1, f2, bf);
    SBAR();
    asm volatile("s_waitcnt vmcnt(24)\n\ts_barrier" ::: "memory");
    c.fr[0] = op_wfrag<0>(c.o); c.fr[1] = op_wfrag<1>(c.o); c.fr[2] = op_wfrag<2>(c.o); c.fr[3] = op_wfrag<3>(c.o); c.fr[4] = op_wfrag<4>(c.o); c.fr[5] = op_wfrag<5>(c.o);
    SBAR();
    op_steps<MERGE, 0>(c, bf, f1, f2, make_iseq<32>{});
}
template <int... Is> DI void op_last(AT& c, iseq<Is...>) { (op_quad<48 + Is>(c), ...); }
template <int K> DI void op_add(f32x4 (&acc)[4], const u32x4 (&sel)[2]) {
    constexpr int S = 2 * (K & 3) + (K >> 4), A = op_land(S) + 4 * ((K >> 2) & 3);
    if constexpr (S < 7) asm volatile("v_mfma_f32_16x16x32_bf16 %0, %1, a[%c2:%c3], %0" : "+v"(acc[K & 3]) : "v"(sel[S & 1]), "i"(A), "i"(A + 3)); }
template <int... Ks> DI void op_adds(f32x4 (&acc)[4], const u32x4 (&sel)[2], iseq<Ks...>) { (op_add<Ks>(acc, sel), ...); }

__global__ __launch_bounds__(256, 1) void k2_attn(unsigned char* ws, const bf16_t* x, bf16_t* out) {
    extern __shared__ __attribute__((aligned(16))) unsigned char shm[];
    ldsp lds = (ldsp)shm;
    const int tid = threadIdx.x, wid = rfl(tid >> 6), lane = tid & 63, r32 = lane & 31, hi = lane >> 5;
    const int b = blockIdx.x, xcd = b & 7, j = b >> 3, head = 2 * xcd + (j >> 4), w16 = j & 15;
    const bool isA = w16 >= 8; const int i = w16 & 7, bq = 15 - i;
    const int seg = head * 8 + i;
    ATW w;
    w.Kh = (const char*)ws + WS_K + (size_t)head * MB; w.Vh = (const char*)ws + WS_V + (size_t)head * MB;
    const char* Qh = (const char*)ws + WS_Q + (size_t)head * MB;
    w.lds = lds; w.n1 = 15 - 2 * i; w.vs = 0; w.lane = lane; w.wid = wid; w.r32 = r32; w.hi = hi; w.isA = isA; w.qrel = wid * 32 + r32 - 4 * hi;
    AT c;
    const unsigned lds0 = (unsigned)(uintptr_t)lds;
    c.part = (const char*)ws + WS_PART + (size_t)(seg * 4 + wid) * 16384; c.mlp = (const char*)ws + WS_ML + (seg * 4 + wid) * 256;
    c.flag = (unsigned*)(ws + WS_FLAGS) + seg * FLAG_STRIDE + wid;
    c.isA = isA; c.early = false; c.ml2 = (f32x2){0.f, 0.f};
    OPJ& o = c.o;
    o.cv = (unsigned)lane * 16u;
    c.goff = (unsigned)wid * 8192u + (unsigned)lane * 16u;
    c.kb = lds + AT_K + lane * 16;
    w.mk = lds0 + AT_K + wid * 8192; w.mv = lds0 + AT_V + wid * 8192;
    TS(ws, 1, 0);
    asm volatile("s_nop 4" ::: ACLOB);
    dma_tile(w.mk, c.goff, w.Kh + (size_t)tile_of(w, 0) * 32768);
    q_loads<0>((unsigned)lane * 16u, Qh + (size_t)(bq * 4 + wid) * 16384, make_iseq<16>{});
    dma_tile(w.mk + 32768, c.goff, w.Kh + (size_t)tile_of(w, 1) * 32768);
    dma_tile(w.mv, c.goff, w.Vh + (size_t)tile_of(w, 0) * 32768);
    if (wid == 3) CODE_TOUCH2(K2_SYM, lds0 + AT_V + 65536, j & 7, lane);
    const bool q2now = !isA && w.n1 <= 3;
    const char* q2p = Qh + (size_t)(i * 4 + wid) * 16384;
    if (q2now) q_loads<16>((unsigned)lane * 16u, q2p, make_iseq<16>{});
    a_zeros(make_iseq<128>{});
    c.cm[0] = f32x16{}; c.cm[1] = f32x16{}; c.m = 0.f; c.l = 0.f; c.rs0 = 0.f; c.rs1 = 0.f; c.alpha = 1.f; c.pend = false; c.drain = false;
    c.aone = hi ? 0.f : 1.f;
    PIN(c.cm[0]);
    if (q2now) asm volatile("s_waitcnt vmcnt(32)" ::: "memory"); else asm volatile("s_waitcnt vmcnt(16)" ::: "memory");
    asm volatile("s_barrier" ::: "memory");
    SBAR();
    c.fr[0] = kfrag<0, 0>(c); c.fr[1] = kfrag<0, 1>(c); c.fr[2] = kfrag<0, 2>(c); c.fr[3] = kfrag<0, 3>(c); c.fr[4] = kfrag<0, 4>(c); c.fr[5] = kfrag<0, 5>(c);
    SBAR();
    phaseA<1, 0>(c, make_iseq<32>{});
    dma_tile(w.mv + 32768, c.goff, w.Vh + (size_t)tile_of(w, 1) * 32768);
    SBAR(); asm volatile("s_nop 7\n\ts_nop 7" : "+v"(c.s[0][0]), "+v"(c.s[0][1])); SBAR();
    c.first = true;
    maxsteps1(c, make_iseq<8>{});
    decide<1>(c);
    exps_from<0, 0>(c, make_iseq<NEB>{});
    asm volatile("s_waitcnt vmcnt(8) lgkmcnt(0)\n\ts_barrier" ::: "memory");
    SBAR();
    c.fr[0] = kfrag<1, 0>(c); c.fr[1] = kfrag<1, 1>(c); c.fr[2] = kfrag<1, 2>(c); c.fr[3] = kfrag<1, 3>(c); c.fr[4] = kfrag<1, 4>(c); c.fr[5] = kfrag<1, 5>(c);
    TS(ws, 1, 1);
    const int q2at = isA || q2now ? -1 : 2;
    for (int g0 = 0, gstop = isA ? 14 : w.n1 - 1; ; g0 += 2, gstop = 14) {
        for (; g0 < gstop; g0 += 2) {
            if (g0 == q2at) q_loads<16>(c.o.cv, q2p, make_iseq<16>{});
            pair<false, false>(c, w, g0);
        }
        if (g0 >= 14) break;
        pair<false, true>(c, w, g0);
    }
    TS(ws, 1, 2);
    if (!isA && w.n1 == 15) pair<true, true>(c, w, 14); else pair<true, false>(c, w, 14);
    TS(ws, 1, 3);
    int h2 = head; asm volatile("" : "+s"(h2), "+s"(ws));
    const int rq = isA ? bq : i, fn = lane & 15, rg = lane >> 4;
    const int trow = rq * 128 + (h2 >> 1) * 16, tcol = (h2 & 1) * 32 + wid * 8;
    unsigned* flags = (unsigned*)(ws + WS_FLAGS) + (N_AFLAGS + rq * 4) * FLAG_STRIDE;
    o.wbase = (const char*)ws + WS_WI + (size_t)h2 * 229376 + wid * 8192;
    o.lds0 = lds0 + wid * 8192; o.wp = lds + lane * 16; o.fl = flags + (h2 >> 2) * FLAG_STRIDE; o.pub = flags + wid * FLAG_STRIDE + h2; o.done = 0u;
    o.xp = (const char*)x + ((size_t)trow * DM + tcol) * 2; o.xo = (unsigned)(fn * DM + (rg >> 1) * 64 + (rg & 1) * 4) * 2u;
    o.yp = (const char*)ws + WS_Y2 + ((size_t)tcol * SEQ + trow) * 2; o.yo = (unsigned)(((rg >> 1) * 64 + (rg & 1) * 4) * SEQ + fn) * 2u;
    o.gp = (const char*)ws + WS_G + trow * 4; o.ws = ws;
    o.pb = (const char*)ws + WS_OP + (size_t)(rq * 64 + (h2 >> 2)) * 28672 + ((h2 & 1) * 2 + (wid & 1)) * 1024 + ((wid >> 1) * 32 + (h2 & 2) * 8) * 16;
    o.po = (unsigned)(rg * 114688 + fn * 16);
    c.vcur = lds + AT_V + 32768 + lane * 16;
    c.fr[0] = vfrag<0>(c.vcur); c.fr[1] = vfrag<1>(c.vcur); c.fr[2] = vfrag<2>(c.vcur); c.fr[3] = vfrag<3>(c.vcur); c.fr[4] = vfrag<4>(c.vcur); c.fr[5] = vfrag<5>(c.vcur);
    SBAR();
    exps_from<0, NEB>(c, make_iseq<32 - NEB>{});
    packs<0>(c, make_iseq<16>{});
    c.l += c.rs0 + c.rs1;
    PIN(c.p[0]); PIN(c.p[1]); PIN(c.p[2]); PIN(c.p[3]);
    SBAR();
    if (isA && !c.early) {
        asm volatile("s_waitcnt vmcnt(0)" ::: "memory");
        c.early = rfl(__float_as_uint(a_read<128>())) != 0u;
        __builtin_amdgcn_fence(__ATOMIC_ACQUIRE, "wavefront");
        if (c.early) { ml_load(c); p_loads(c, make_iseq<16>{}); }
    }
    SBAR(); NOPS2(); SBAR();
    phaseB<0, 64>(c, make_iseq<32>{});
    SBAR();
    if (isA && !c.early) {
        unsigned spins = 0;
        while (rfl(__hip_atomic_load(c.flag, RLX_AGENT)) == 0u) { __builtin_amdgcn_s_sleep(1); if (++spins > (1u << 22)) break; }
        __builtin_amdgcn_fence(__ATOMIC_ACQUIRE, "wavefront");
        ml_load(c);
        p_loads(c, make_iseq<16>{});
        asm volatile("s_waitcnt vmcnt(0)" : "+v"(c.ml2) :: "memory");
    }
    const float lt = halfsum(c.l);
    TS(ws, 1, 4);
    u32x4 bf[16];
    c.part = (const char*)ws + WS_OP + (size_t)((rq * 16 + h2) * 4 + wid) * 28672;
    if (isA) {
        asm volatile("s_waitcnt vmcnt(32)" : "+v"(c.ml2) :: "memory");
        const float mn = __builtin_fmaxf(c.m, c.ml2[0]);
        const float w1 = ex2(c.m - mn), w2 = ex2(c.ml2[0] - mn);
        const float inv = 1.0f / (lt * w1 + c.ml2[1] * w2);
        op_first<true>(c, bf, w1 * inv, w2 * inv);
    } else op_first<false>(c, bf, 1.0f / lt, 0.f);
    op_steps<false, 32>(c, bf, 0.f, 0.f, make_iseq<192>{});
    NOPS16(); SBAR();
    op_last(c, make_iseq<8>{});
    op_look(o);
    TS(ws, 1, 5);
#pragma unroll
    for (int e = 0; e < 16; ++e) PIN(o.yw[e]);
    PIN(o.xw[0]); PIN(o.xw[1]); PIN(o.xw[2]); PIN(o.xw[3]); PIN(o.g);
    const float g = o.g, og = rbf(1.0f - g);
    float ta[16];
#pragma unroll
    for (int e = 0; e < 8; ++e) {
        ta[2 * e] = rbf(g * rbf(bf_lo(o.xw[e >> 1][e & 1]) * bf_lo(o.yw[2 * e]))); ta[2 * e + 1] = rbf(g * rbf(bf_hi(o.xw[e >> 1][e & 1]) * bf_lo(o.yw[2 * e + 1])));
        PIN(ta[2 * e]); PIN(ta[2 * e + 1]);
    }
    u32x4 sel[2]; f32x4 acc[4];
#pragma unroll
    for (int s2 = 0; s2 < 2; ++s2) {
#pragma unroll
        for (int e = 0; e < 4; ++e) sel[s2][e] = (fn >> 1) == 4 * s2 + e ? 0x3f80u << (16 * (fn & 1)) : 0u;
        acc[2 * s2] = f32x4{}; acc[2 * s2 + 1] = f32x4{}; PIN(sel[s2]); PIN(acc[2 * s2]); PIN(acc[2 * s2 + 1]);
    }
    SBAR();
    asm volatile("s_waitcnt vmcnt(6)" ::: "memory");
    op_publish(o, 6u);
    asm volatile("s_waitcnt vmcnt(2)" ::: "memory");
    op_publish(o, 7u);
    asm volatile("s_waitcnt vmcnt(2)\n\ts_barrier" ::: "memory");
    TS(ws, 1, 7);
    TS(ws, 2, 0);
    for (unsigned spins = 0; spins < (1u << 22); ++spins) {
        const unsigned d0 = o.done;
        const unsigned v = __float_as_uint(a_read<208>());
        o.look = fn == h2 ? 7u : v;
        PIN(o.look);
        op_look(o);
        op_takes(o, make_iseq<7>{});
        if (o.done == 7u) break;
        op_wait<6>(o.done - d0);
        if (o.done == d0) __builtin_amdgcn_s_sleep(1);
    }
    TS(ws, 2, 4);
    asm volatile("s_waitcnt vmcnt(0)" ::: "memory");
    TS(ws, 2, 1);
    op_adds(acc, sel, make_iseq<32>{});
    asm volatile("s_nop 7\n\ts_nop 7" : "+v"(acc[0]), "+v"(acc[1]), "+v"(acc[2]), "+v"(acc[3]));
#pragma unroll
    for (int f = 0; f < 4; ++f) {
        float r[4];
#pragma unroll
        for (int e = 0; e < 4; ++e) r[e] = ta[4 * f + e] + rbf(og * rbf(acc[f][e]));
        if (f < 3 || rg < 2) *(u32x2*)((char*)out + ((size_t)trow * DM + tcol) * 2 + o.xo + f * 256) = (u32x2){cvtpk(r[0], r[1]), cvtpk(r[2], r[3])};
    }
    TS(ws, 2, 7);
}

extern "C" __device__ __attribute__((used, noinline)) void text_end_mark() { asm volatile("s_nop 0"); }

extern "C" void kernel_launch(void* const* d_in, const int* in_sizes, int n_in, void* d_out, int out_size, void* d_ws, size_t ws_size,
                              hipStream_t stream) {
    (void)in_sizes; (void)n_in; (void)out_size; (void)ws_size;
    (void)hipFuncSetAttribute((const void*)k1_proj, hipFuncAttributeMaxDynamicSharedMemorySize, K1_LDS);
    (void)hipFuncSetAttribute((const void*)k2_attn, hipFuncAttributeMaxDynamicSharedMemorySize, AT_LDS);
    K1Args P;
    P.x = (const bf16_t*)d_in[0]; P.ak = (const bf16_t*)d_in[1]; P.av = (const bf16_t*)d_in[2]; P.shift_k = (const bf16_t*)d_in[3];
    P.ssm_k = (const bf16_t*)d_in[4]; P.ssm_d = (const bf16_t*)d_in[5]; P.ckv = (const bf16_t*)d_in[7]; P.wq = (const bf16_t*)d_in[8];
    P.wuk = (const bf16_t*)d_in[9]; P.wuv = (const bf16_t*)d_in[10]; P.gate_w = (const bf16_t*)d_in[12]; P.wo = (const bf16_t*)d_in[11]; P.ws = (unsigned char*)d_ws;
    k1_proj<<<256, 512, K1_LDS, stream>>>(P);
    k2_attn<<<256, 256, AT_LDS, stream>>>((unsigned char*)d_ws, (const bf16_t*)d_in[0], (bf16_t*)d_out);
}
```

```cpp
#include <hip/hip_runtime.h>
#include <hip/hip_bf16.h>
#include <stdint.h>
#include <utility>

#define LAS __attribute__((address_space(3)))
#define GAS __attribute__((address_space(1)))
#define CAS __attribute__((address_space(4)))
#define DI __device__ __forceinline__
typedef unsigned short bf16_t;
typedef short bf16x8 __attribute__((ext_vector_type(8)));
typedef short s16x4 __attribute__((ext_vector_type(4)));
typedef float f32x2 __attribute__((ext_vector_type(2)));
typedef float f32x4 __attribute__((ext_vector_type(4)));
typedef float f32x16 __attribute__((ext_vector_type(16)));
typedef unsigned u32x2 __attribute__((ext_vector_type(2)));
typedef unsigned u32x4 __attribute__((ext_vector_type(4)));
typedef __bf16 bfx2 __attribute__((ext_vector_type(2)));
typedef LAS unsigned char* ldsp;
typedef const LAS unsigned char* ldscp;

constexpr int SEQ = 2048, DM = 448, NH = 16, HD = 256, DC = 1024, NW_ = 4096;

constexpr size_t MB = 1024 * 1024;
constexpr size_t WS_Q = 0, WS_K = 16 * MB, WS_V = 32 * MB, WS_PART = 64 * MB, WS_ML = 80 * MB,
                 WS_Y2 = 81 * MB, WS_G = 84 * MB, WS_FLAGS = 100 * MB, WS_WI = 102 * MB, WS_OP = 128 * MB;
constexpr int FLAG_STRIDE = 64;
constexpr int N_AFLAGS = 128, N_CFLAGS = 64;

#define TS(wsp, kern, slot) do { } while (0)
#define TSI(wsp, idx) do { } while (0)
#define SBAR() __builtin_amdgcn_sched_barrier(0)
#define PIN(x) asm volatile("" : "+v"(x))
#define RLX_AGENT __ATOMIC_RELAXED, __HIP_MEMORY_SCOPE_AGENT

DI unsigned cvtpk(float lo, float hi) { f32x2 v = {lo, hi}; bfx2 b = __builtin_convertvector(v, bfx2); return __builtin_bit_cast(unsigned, b); }
DI float bf_lo(unsigned w) { return __uint_as_float(w << 16); }
DI float bf_hi(unsigned w) { return __uint_as_float(w & 0xffff0000u); }
DI float rbf(float x) { return bf_lo(cvtpk(x, 0.f)); }
DI unsigned rfl(unsigned x) { return __builtin_amdgcn_readfirstlane(x); }
DI s16x4 trread(ldscp p) { return __builtin_amdgcn_ds_read_tr16_b64_v4i16((LAS s16x4*)p); }
DI bf16x8 cat8(s16x4 lo, s16x4 hi) { return __builtin_shufflevector(lo, hi, 0, 1, 2, 3, 4, 5, 6, 7); }
DI bf16x8 ldsrd16(ldscp p) { return *(const LAS bf16x8*)p; }
#define MFMA16(a, b, c) __builtin_amdgcn_mfma_f32_16x16x32_bf16((a), (b), (c), 0, 0, 0)
#define MFMA32(a, b, c) __builtin_amdgcn_mfma_f32_32x32x16_bf16((a), (b), (c), 0, 0, 0)

DI void touch(unsigned sink, unsigned voff, const char* g) {
    asm volatile("s_mov_b32 m0, %0\n\ts_nop 0\n\tglobal_load_lds_dword %1, %2" :: "s"(sink), "v"(voff), "s"(g) : "memory");
}
#define CODE_TOUCH(SYM, END, sink, chunk, lane) do { const char* _p; unsigned _n; \
        asm volatile("s_getpc_b64 s[90:91]\n\ts_add_u32 s90, s90, " SYM "@rel32@lo+4\n\ts_addc_u32 s91, s91, " SYM "@rel32@hi+12\n\ts_mov_b64 %0, s[90:91]\n\ts_mov_b32 %1, " END "-" SYM \
                     : "=s"(_p), "=s"(_n) :: "s90", "s91", "scc"); \
        const unsigned _o = ((unsigned)(chunk) * 64u + (unsigned)(lane)) * 128u; \
        if ((unsigned)(chunk) * 8192u < _n && _n < 262144u) touch((sink), _o < _n - 4u ? _o : _n - 4u, _p); } while (0)
#define K1_SYM "_Z7k1_proj6K1Args", "_Z7k2_attnPhPKtPt"
#define K2_SYM "_Z7k2_attnPhPKtPt", "text_end_mark"
#define CODE_TOUCH2(S, sink, chunk, lane) CODE_TOUCH(S, sink, chunk, lane)

constexpr int HTB = 128 * 64 * 2;
constexpr int K1_X = 8 * HTB;
constexpr int K1_LDS = K1_X + 16384 + 2048;

DI int lds_byte(int r, int c) { const int st = (r >> 4) * 2 + (c >> 5), rr = r & 15, cc = c & 31, ob = rr * 64 + cc * 2; return st * 1024 + (ob ^ (((ob >> 9) & 1) << 5)); }
DI void stage_rc(int b, int& R, int& C) { const int st = b / 1024, sb = b % 1024, swz = sb ^ (((sb >> 9) & 1) << 5); R = (st >> 1) * 16 + swz / 64; C = (st & 1) * 32 + (swz % 64) / 2; }

struct K1Args {
    const bf16_t *x, *ak, *av, *shift_k, *ssm_k, *ssm_d, *ckv, *wq, *wuk, *wuv, *gate_w, *wo;
    unsigned char* ws;
};

#define SA(b, h) (((b) * 2 + (h)) * HTB)
#define SB(b, h) ((4 + (b) * 2 + (h)) * HTB)
constexpr int ring_slot(int u) {
    switch (u & 7) { case 5: return SB(0, 0); case 6: return SA(0, 0); case 7: return SB(0, 1); case 0: return SA(0, 1);
                     case 1: return SB(1, 0); case 2: return SA(1, 0); case 3: return SB(1, 1); default: return SA(1, 1); }
}
struct G1 {
    ldsp lds; unsigned ldsw  , voffA_kv, voffA_q, voffB, sv; int aoff, boff0, boff1;
    const char *A_kv, *A_q, *B_kv, *B_q, *sbase; unsigned sstep, sdst, sdstep; int sadd;
};
DI int lane_id() { int l; asm volatile("v_mbcnt_lo_u32_b32 %0, -1, 0\n\tv_mbcnt_hi_u32_b32 %0, -1, %0" : "=v"(l)); return l; }
template <int OFF> DI void dma16(unsigned lbase, unsigned voff, const char* g) {
    asm volatile("s_add_i32 m0, %0, %c3\n\ts_nop 0\n\tglobal_load_lds_dwordx4 %1, %2" :: "s"(lbase), "v"(voff), "s"(g), "i"(OFF) : "memory", "scc");
}
template <int BUF, int STRIDE, int H> DI void stage_a(const G1& g, const char* base, unsigned voff) {
    dma16<BUF>(g.ldsw, voff, base + H * 128 * STRIDE); dma16<BUF + 8192>(g.ldsw, voff, base + (H * 128 + 64) * STRIDE);
}
template <int BUF, int H> DI void stage_b(const G1& g, const char* base) {
    dma16<BUF>(g.ldsw, g.voffB, base + H * 256); dma16<BUF + 8192>(g.ldsw, g.voffB, base + H * 256 + 32 * NW_ * 2);
}
template <int U> DI void stage_unit(const G1& g) {
    if constexpr (U < 21) {
        constexpr int t = U / 3, r = U % 3;
        if constexpr (r == 0) stage_b<ring_slot(U), 0>(g, g.B_q + (size_t)t * (64 * NW_ * 2));
        else stage_a<ring_slot(U), DM * 2, r - 1>(g, g.A_q + t * 128, g.voffA_q);
    } else {
        constexpr int t = (U - 21) >> 2, r = (U - 21) & 3;
        if constexpr ((r & 1) == 0) stage_b<ring_slot(U), (r >> 1)>(g, g.B_kv + (size_t)t * (64 * NW_ * 2));
        else stage_a<ring_slot(U), DC * 2, (r >> 1)>(g, g.A_kv + t * 128, g.voffA_kv);
    }
}
DI void side(const G1& g, int t) {
    const int tt = t + g.sadd > 15 ? 15 : t + g.sadd;
    asm volatile("s_mov_b32 m0, %0\n\ts_nop 0\n\tglobal_load_lds_dword %1, %2" :: "s"(g.sdst + (unsigned)t * g.sdstep), "v"(g.sv), "s"(g.sbase + rfl((unsigned)tt * g.sstep)) : "memory");
}
DI void lda(bf16x8 (&dst)[4][2], const G1& g, int buf) {
#pragma unroll
    for (int m = 0; m < 4; ++m)
#pragma unroll
        for (int k = 0; k < 2; ++k) dst[m][k] = ldsrd16(g.lds + buf + g.aoff + m * 2048 + k * 1024);
}
DI void ldb(bf16x8 (&dst)[2][2], const G1& g, int buf) {
#pragma unroll
    for (int n = 0; n < 2; ++n)
#pragma unroll
        for (int k = 0; k < 2; ++k) { ldscp p = g.lds + buf + (n ? g.boff1 : g.boff0) + k * 8192; dst[n][k] = cat8(trread(p), trread(p + 256)); }
}
template <bool VT> DI void mma(f32x4 (&acc)[4][2], const bf16x8 (&At)[4][2], const bf16x8 (&Bt)[2][2]) {
    __builtin_amdgcn_s_setprio(1);
#pragma unroll
    for (int m = 0; m < 4; ++m)
#pragma unroll
        for (int n = 0; n < 2; ++n)
#pragma unroll
            for (int k = 0; k < 2; ++k) acc[m][n] = VT ? MFMA16(At[m][k], Bt[n][k], acc[m][n]) : MFMA16(Bt[n][k], At[m][k], acc[m][n]);
    __builtin_amdgcn_s_setprio(0);
}
#define WAIT_V(n) asm volatile("s_waitcnt vmcnt(" #n ")" ::: "memory")
#define WAIT_L(n) asm volatile("s_waitcnt lgkmcnt(" #n ")" ::: "memory")
#define BAR __builtin_amdgcn_s_barrier()
DI void zero8(f32x4 (&a)[4][2]) {
#pragma unroll
    for (int m = 0; m < 4; ++m) { a[m][0] = (f32x4){0.f, 0.f, 0.f, 0.f}; a[m][1] = (f32x4){0.f, 0.f, 0.f, 0.f}; }
}

DI void pack_kq(const f32x4 (&acc)[4][2], u32x4* pk, float scale) {
#pragma unroll
    for (int m = 0; m < 4; ++m) {
        const f32x4 a0 = acc[m][0] * scale, a1 = acc[m][1] * scale;
        const unsigned w0 = cvtpk(a0[0], a0[1]), w1 = cvtpk(a0[2], a0[3]), x0 = cvtpk(a1[0], a1[1]), x1 = cvtpk(a1[2], a1[3]);
        auto r0 = __builtin_amdgcn_permlane16_swap(w0, x0, false, false);
        auto r1 = __builtin_amdgcn_permlane16_swap(w1, x1, false, false);
        pk[m] = (u32x4){r0[0], r1[0], r0[1], r1[1]};
    }
}
DI unsigned kq_off(int pm, int wr, int wc, int fr, int fq) {
    return (unsigned)((pm * 8 + wr * 2) * 16384 + fr * 16 + (2 * wc + (fq & 1)) * 1024 + (fq >> 1) * 512);
}
DI void pack_v(const f32x4 (&acc)[4][2], u32x4* pk) {
#pragma unroll
    for (int n = 0; n < 2; ++n)
#pragma unroll
        for (int mm = 0; mm < 2; ++mm) {
            const f32x4 a0 = acc[2 * mm][n], a1 = acc[2 * mm + 1][n];
            const unsigned w0 = cvtpk(a0[0], a0[1]), w1 = cvtpk(a0[2], a0[3]), x0 = cvtpk(a1[0], a1[1]), x1 = cvtpk(a1[2], a1[3]);
            auto r0 = __builtin_amdgcn_permlane32_swap(w0, x0, false, false);
            auto r1 = __builtin_amdgcn_permlane32_swap(w1, x1, false, false);
            pk[2 * mm + n] = (u32x4){r0[0], r1[0], r0[1], r1[1]};
        }
}
constexpr int ST_AUX = 16;
struct Out1 { u32x4 pk[16]; __amdgpu_buffer_rsrc_t rs; unsigned voff; int sbj, shi; };
template <int I> DI void st_piece(const Out1& o) {
    __builtin_amdgcn_raw_buffer_store_b128(o.pk[I], o.rs, o.voff + (I & 1) * 256, (I >> 3) * 65536 + ((I >> 2) & 1) * o.sbj + ((I >> 1) & 1) * o.shi, ST_AUX);
}
template <int A, int B> DI void st_range(const Out1& o) { if constexpr (A < B) { st_piece<A>(o); st_range<A + 1, B>(o); } }

template <int N, class W> DI void qphase(const G1& g, f32x4 (&a0)[4][2], f32x4 (&a1)[4][2], bf16x8 (&At)[4][2], bf16x8 (&B0)[2][2], W& warm) {
    constexpr int t = N >> 1;
    if constexpr ((N & 1) == 0) {
        ldb(B0, g, ring_slot(3 * t)); SBAR(); lda(At, g, ring_slot(3 * t + 1));
        if constexpr (t < 5) warm(t);
        if constexpr (N >= 2) stage_unit<3 * t + 6>(g);
        if constexpr (N == 0) WAIT_V(10); else WAIT_V(8);
        WAIT_L(8); BAR; WAIT_L(0); mma<false>(a0, At, B0); BAR; SBAR();
    } else {
        lda(At, g, ring_slot(3 * t + 2));
        if constexpr (t >= 1) stage_unit<3 * t + 7>(g);
        stage_unit<3 * t + 8>(g);
        WAIT_V(8);
        if constexpr (N == 13) WAIT_L(0);
        BAR; WAIT_L(0); mma<false>(a1, At, B0); BAR; SBAR();
    }
}
template <class W, int... Ns> DI void qphases(const G1& g, f32x4 (&a0)[4][2], f32x4 (&a1)[4][2], bf16x8 (&At)[4][2], bf16x8 (&B0)[2][2], W& warm, std::integer_sequence<int, Ns...>) {
    (qphase<Ns>(g, a0, a1, At, B0, warm), ...);
}
template <int... Us> DI void stage_units(const G1& g, std::integer_sequence<int, Us...>) { (stage_unit<Us>(g), ...); }

template <bool VT, int POS, class W>
DI void mainpair(const G1& g, int t, f32x4 (&acc)[2][2][4][2], bf16x8 (&At)[4][2], bf16x8 (&B0)[2][2], bf16x8 (&B1)[2][2], W& lastside) {
    const char* a1 = g.A_kv + (t + 1) * 128; const char* a2 = a1 + 128; const char* a3 = a1 + 256;
    const char* b2 = g.B_kv + (size_t)(t + 2) * (64 * NW_ * 2); const char* b3 = b2 + 64 * NW_ * 2;
    ldb(B0, g, SB(0, 0)); SBAR(); lda(At, g, SA(0, 0));
    if constexpr (POS != 0) stage_a<SA(1, 1), DC * 2, 1>(g, a1, g.voffA_kv); else WAIT_V(18);
    WAIT_L(8); BAR; WAIT_L(0); mma<VT>(acc[0][0], At, B0); BAR; SBAR();
    ldb(B1, g, SB(0, 1));
    if constexpr (POS == 2) lastside(0); else side(g, t);
    if constexpr (POS != 2) stage_b<SB(0, 0), 0>(g, b2);
    if constexpr (POS == 0) WAIT_V(19);
    BAR; WAIT_L(0); mma<VT>(acc[0][1], At, B1); BAR;
    lda(At, g, SA(0, 1));
    if constexpr (POS != 2) stage_a<SA(0, 0), DC * 2, 0>(g, a2, g.voffA_kv);
    BAR; WAIT_L(0); mma<VT>(acc[1][0], At, B0); BAR; SBAR();
    if constexpr (POS != 2) stage_b<SB(0, 1), 1>(g, b2);
    if constexpr (POS == 0) WAIT_V(15); else if constexpr (POS == 1) WAIT_V(7); else WAIT_V(1);
    BAR; mma<VT>(acc[1][1], At, B1); BAR;
    ldb(B0, g, SB(1, 0)); SBAR(); lda(At, g, SA(1, 0));
    if constexpr (POS != 2) stage_a<SA(0, 1), DC * 2, 1>(g, a2, g.voffA_kv);
    WAIT_L(8); BAR; WAIT_L(0); mma<VT>(acc[0][0], At, B0); BAR; SBAR();
    ldb(B1, g, SB(1, 1));
    if constexpr (POS == 2) lastside(1); else side(g, t + 1);
    if constexpr (POS != 2) stage_b<SB(1, 0), 0>(g, b3);
    BAR; WAIT_L(0); mma<VT>(acc[0][1], At, B1); BAR;
    lda(At, g, SA(1, 1));
    if constexpr (POS != 2) stage_a<SA(1, 0), DC * 2, 0>(g, a3, g.voffA_kv);
    BAR; WAIT_L(0); mma<VT>(acc[1][0], At, B0); BAR; SBAR();
    if constexpr (POS != 2) { stage_b<SB(1, 1), 1>(g, b3); WAIT_V(7); }
    BAR; mma<VT>(acc[1][1], At, B1); BAR;
}

constexpr int CV_RAW = SA(0, 0);
constexpr int K1_WO = SA(0, 1);
constexpr int CV_HSTR = 4256;
constexpr int CV_H = 2 * HTB;
constexpr int CV_USTR = 4608;
constexpr int CV_U = CV_H + 8 * CV_HSTR;
constexpr int CV_R = CV_U + 2 * CV_USTR;
static_assert(CV_R + 65536 <= K1_LDS && CV_R <= K1_X, "lds");

DI const char* wo_block(const K1Args& P, int e) { const int eb = e < 224 ? e : 0, kb = eb / 7, cn = eb - kb * 7; return (const char*)P.wo + ((size_t)kb * 128 * DM + cn * 64) * 2; }
DI void tail_stage(const G1& g, const K1Args& P, int c0, int e, int wid, int lane) {
    dma16<CV_RAW>(g.ldsw, (unsigned)lane * 16u, (const char*)P.ssm_k + (size_t)c0 * (SEQ * 2) + wid * 1024);
    const char* wg = wo_block(P, e);
    const unsigned vo = (unsigned)((wid * 8 + (lane >> 3)) * DM + (lane & 7) * 8) * 2u;
    dma16<K1_WO>(g.ldsw, vo, wg); dma16<K1_WO + 8192>(g.ldsw, vo, wg + 64 * DM * 2);
}
DI void wo_image(ldsp lds, const K1Args& P, int e, int wid, int lane) {
    const int fr = lane & 15, fq = lane >> 4, kb = e / 7, cn = e - kb * 7;
    if (e < 224) {
        ldscp wp = lds + K1_WO + (wid * 16 + 4 * (fq >> 1) + (fr >> 2)) * 128 + (fr & 1) * 32 + (fq & 1) * 16 + (fr & 2) * 4;
        unsigned char* op = P.ws + WS_WI + (size_t)((kb >> 1) * 7 + cn) * 32768 + ((kb & 1) * 8 + wid) * 2048 + lane * 16;
#pragma unroll
        for (int cb = 0; cb < 2; ++cb) *(bf16x8*)(op + cb * 1024) = cat8(trread(wp + cb * 64), trread(wp + cb * 64 + 1024));
    }
}
DI void conv_copies(ldsp q, const u32x4 d, const u32x2 e) {
    const unsigned a0 = __builtin_amdgcn_alignbit(d[1], d[0], 16), a1 = __builtin_amdgcn_alignbit(d[2], d[1], 16), a2 = __builtin_amdgcn_alignbit(d[3], d[2], 16),
                   a3 = __builtin_amdgcn_alignbit(e[0], d[3], 16), a4 = __builtin_amdgcn_alignbit(e[1], e[0], 16);
    *(LAS u32x4*)(q) = (u32x4){a0, a1, a2, a3};
    *(LAS u32x4*)(q + CV_HSTR) = (u32x4){d[1], d[2], d[3], e[0]};
    *(LAS u32x4*)(q + 2 * CV_HSTR) = (u32x4){a1, a2, a3, a4};
    *(LAS u32x4*)(q + 3 * CV_HSTR) = (u32x4){d[2], d[3], e[0], e[1]};
}
DI void conv_frags(bf16x8 (&A)[8], bf16x8 (&B)[8], ldscp hp, ldscp up) {
#pragma unroll
    for (int v = 0; v < 8; ++v) {
        B[v] = ldsrd16(up + (7 - v) * 512);
        const s16x4 lo = *(const LAS s16x4*)(hp + v * 512), hi = *(const LAS s16x4*)(hp + v * 512 + 8);
        A[v] = cat8(lo, hi);
    }
}
DI void conv_mma(f32x4 (&acc)[8], const bf16x8 (&A)[8], const bf16x8 (&B)[8]) {
#pragma unroll
    for (int J = 0; J < 8; ++J)
#pragma unroll
        for (int v = 0; v <= J; ++v) acc[J] = MFMA16(A[v], B[J - v], acc[J]);
}
DI void conv_part(ldsp lds, const K1Args& P, int c0, const u32x4 sw, const unsigned dw, int wid, int lane, const Out1& o) {
    const int tid = wid * 64 + lane, fr = lane & 15, fq = lane >> 4;
    {
        const int cq = tid & 255;
        const u32x4 d = *(const LAS u32x4*)(lds + CV_RAW + tid * 16);
        u32x2 e = *(const LAS u32x2*)(lds + CV_RAW + tid * 16 + 16);
        const u32x4 z4 = {0u, 0u, 0u, 0u}; const u32x2 z2 = {0u, 0u};
        if (cq == 255) e = z2;
        ldsp q = lds + CV_H + (tid >> 8) * 4 * CV_HSTR + (cq + 2) * 16;
        conv_copies(q, d, e);
        if (cq == 0) { conv_copies(q - 16, z4, (u32x2){d[0], d[1]}); conv_copies(q - 32, z4, z2); }
        if (cq == 255) { conv_copies(q + 16, z4, z2); conv_copies(q + 32, z4, z2); }
    }
    SBAR(); st_range<4, 6>(o); SBAR();
    {
        ldscp kp = lds + K1_X + tid * 16;
        u32x4 a = *(const LAS u32x4*)(kp - 16);
        const u32x4 b = *(const LAS u32x4*)(kp), vr = *(const LAS u32x4*)(kp + 8192);
        if (tid == 0) a = (u32x4){0u, 0u, 0u, 0u};
        const unsigned kr[7] = {a[1], a[2], a[3], b[0], b[1], b[2], b[3]};
#pragma unroll
        for (int c = 0; c < 2; ++c) {
            const float w0 = bf_lo(sw[2 * c]), w1 = bf_hi(sw[2 * c]), w2 = bf_lo(sw[2 * c + 1]), w3 = bf_hi(sw[2 * c + 1]);
            float kc[7], kv[4];
#pragma unroll
            for (int i = 0; i < 7; ++i) kc[i] = c ? bf_hi(kr[i]) : bf_lo(kr[i]);
#pragma unroll
            for (int i = 0; i < 4; ++i) {
                float s = rbf(kc[i] * w3);
                s = rbf(s + rbf(kc[i + 1] * w2));
                s = rbf(s + rbf(kc[i + 2] * w1));
                s = rbf(s + rbf(kc[i + 3] * w0));
                kv[i] = s * (c ? bf_hi(vr[i]) : bf_lo(vr[i]));
            }
            u32x2 wv = {cvtpk(kv[3], kv[2]), cvtpk(kv[1], kv[0])};
            *(LAS u32x2*)(lds + CV_U + c * CV_USTR + (2044 - 4 * tid) * 2) = wv;
            SBAR(); if (c == 0) st_range<6, 8>(o); else st_range<8, 10>(o); SBAR();
        }
        if (tid < 120) { const int c = tid / 60, j = tid - c * 60; *(LAS u32x2*)(lds + CV_U + c * CV_USTR + 4096 + j * 8) = (u32x2){0u, 0u}; }
    }
    __syncthreads();
    TS(P.ws, 0, 5);
    const int ch = wid >> 2, qw = wid & 3;
    {
        const int y1 = 24 + fr - 8 * fq;
        ldscp hb = lds + CV_H + ch * 4 * CV_HSTR + (y1 & 3) * CV_HSTR + (y1 & ~3) * 2 + qw * 128;
        ldscp ub = lds + CV_U + ch * CV_USTR + (2056 - 256 * 7 - 16 * fr - 8 * fq) * 2 + qw * 128;
        f32x4 acc[8];
#pragma unroll
        for (int i = 0; i < 8; ++i) acc[i] = (f32x4){0.f, 0.f, 0.f, 0.f};
        bf16x8 A0[8], B0[8], A1[8], B1[8];
        conv_frags(A0, B0, hb, ub); SBAR();
        conv_frags(A1, B1, hb + 64, ub + 64); SBAR();
        st_range<10, 12>(o); SBAR();
        conv_mma(acc, A0, B0); SBAR();
        st_range<12, 14>(o); SBAR();
        conv_mma(acc, A1, B1); SBAR();
        st_range<14, 16>(o); SBAR();
#pragma unroll
        for (int i = 0; i < 8; ++i) *(LAS f32x4*)(lds + CV_R + wid * 8192 + i * 1024 + lane * 16) = acc[i];
    }
    __syncthreads();
    TS(P.ws, 0, 6);
    {
        const float dd = ch ? bf_hi(dw) : bf_lo(dw);
#pragma unroll
        for (int i = 0; i < 2; ++i) {
            ldscp rp = lds + CV_R + ch * 32768 + (2 * qw + i) * 1024 + lane * 16;
            const f32x4 y = ((*(const LAS f32x4*)rp + *(const LAS f32x4*)(rp + 8192)) + *(const LAS f32x4*)(rp + 16384)) + *(const LAS f32x4*)(rp + 24576);
            const int t = 256 * (2 * qw + i) + 16 * fr + 4 * fq;
            const u32x2 uw = *(const LAS u32x2*)(lds + CV_U + ch * CV_USTR + (2044 - t) * 2);
            const float kv[4] = {bf_hi(uw[1]), bf_lo(uw[1]), bf_hi(uw[0]), bf_lo(uw[0])};
            float r[4];
#pragma unroll
            for (int e = 0; e < 4; ++e) r[e] = rbf(y[e]) + rbf(kv[e] * dd);
            u32x2 w = {cvtpk(r[0], r[1]), cvtpk(r[2], r[3])};
            *(u32x2*)(P.ws + WS_Y2 + ((size_t)(c0 + ch) * SEQ + t) * 2) = w;
        }
    }
}

DI void gate_part(const K1Args& P, int row0, int wid, int lane) {
    const int lc = lane < 56 ? lane : 0;
    u32x4 gw = *(const u32x4*)(P.gate_w + lc * 8);
    float* gp = (float*)(P.ws + WS_G);
    u32x4 xv[8];
#pragma unroll
    for (int r = 0; r < 8; ++r) xv[r] = *(const u32x4*)(P.x + (size_t)(row0 + wid * 8 + r) * DM + lc * 8);
    float mine = 0.f;
#pragma unroll
    for (int r = 0; r < 8; ++r) {
        float s = 0.f;
#pragma unroll
        for (int j = 0; j < 4; ++j) { s += rbf(bf_lo(xv[r][j]) * bf_lo(gw[j])); s += rbf(bf_hi(xv[r][j]) * bf_hi(gw[j])); }
        if (lane >= 56) s = 0.f;
#pragma unroll
        for (int o = 32; o >= 1; o >>= 1) s += __shfl_xor(s, o);
        if (lane == r) mine = s;
    }
    const float z = rbf(mine);
    const float g = rbf(1.0f / (1.0f + __expf(-z)));
    if (lane < 8) gp[row0 + wid * 8 + lane] = g;
}

template <bool VT>
DI void big_tile(const G1& g, const K1Args& P, int pm, int head, int c0, int e, int wid, bf16x8 (&At)[4][2], bf16x8 (&B0)[2][2], Out1& o) {
    f32x4 acc[2][2][4][2];
    zero8(acc[0][0]); zero8(acc[0][1]); zero8(acc[1][0]); zero8(acc[1][1]);
    bf16x8 B1[2][2];
    auto lastside = [&](int s) {
        if (wid < 4) side(g, 14 + s);
        else if (s == 0) {
            const unsigned l = (unsigned)lane_id();
            if (wid == 4) touch(g.sdst, l * 128u, (const char*)P.ssm_k + (size_t)c0 * (SEQ * 2));
            else touch(g.sdst, l * (DM * 2), wo_block(P, e) + (wid & 1) * (64 * DM * 2));
        }
    };
    mainpair<VT, 0>(g, 0, acc, At, B0, B1, lastside);
    for (int t = 2; t < 14; t += 2) mainpair<VT, 1>(g, t, acc, At, B0, B1, lastside);
    mainpair<VT, 2>(g, 14, acc, At, B0, B1, lastside);
    TS(P.ws, 0, 3);
    const int lane = lane_id();
    tail_stage(g, P, c0, e, wid, lane);
    if (wid == 7) CODE_TOUCH2(K2_SYM, g.sdst, e & 15, lane);
    SBAR();
    const int wr = wid >> 2, wc = wid & 3, fr = lane & 15, fq = lane >> 4;
    if (VT) {
        o.voff = (unsigned)((pm * 4 + wr) * 32768 + wc * 4096 + (fq >> 1) * 1024 + (fq & 1) * 512 + fr * 16);
        pack_v(acc[0][0], o.pk); pack_v(acc[0][1], o.pk + 4); pack_v(acc[1][0], o.pk + 8); pack_v(acc[1][1], o.pk + 12);
    } else {
        o.voff = kq_off(pm, wr, wc, fr, fq);
        pack_kq(acc[0][0], o.pk, 1.0f); pack_kq(acc[0][1], o.pk + 4, 1.0f); pack_kq(acc[1][0], o.pk + 8, 1.0f); pack_kq(acc[1][1], o.pk + 12, 1.0f);
    }
}

__global__ __launch_bounds__(512, 2) void k1_proj(K1Args P) {
    extern __shared__ __attribute__((aligned(16))) unsigned char shm[];
    ldsp lds = (ldsp)shm;
    const int b = blockIdx.x, xcd = b & 7, j = b >> 3, li = j & 15, pm = li & 7, head = 2 * xcd + (li >> 3), vt = j >> 4;
    const int tid = threadIdx.x, wid = rfl(tid >> 6), lane = tid & 63, wr = wid >> 2, wc = wid & 3, fr = lane & 15, fq = lane >> 4;
    const int c0 = (xcd * 28 + (j < 28 ? j : 0)) * 2, e = xcd * 32 + j;
    TS(P.ws, 0, 0);
    if (wid == 7) CODE_TOUCH2(K1_SYM, (unsigned)(uintptr_t)lds + K1_X + 16384 + 7 * 256, j, lane);
    u32x4 sw = *(const CAS u32x4*)(uintptr_t)(P.shift_k + c0 * 4); unsigned dw = *(const CAS unsigned*)(uintptr_t)(P.ssm_d + c0);
    G1 g;
    const unsigned lds0 = (unsigned)(uintptr_t)lds;
    g.lds = lds; g.ldsw = lds0 + (unsigned)wid * 1024u;
    { int R, C; stage_rc(tid * 16, R, C); g.voffA_kv = (unsigned)(R * DC + C) * 2u; g.voffA_q = (unsigned)(R * DM + C) * 2u; }
    { const int kg = wid >> 1, row = (lane >> 2) & 7, ch = (lane & 3) ^ (2 * (kg & 1)), n = 32 * (2 * (wid & 1) + (lane >> 5)) + 8 * ch;
      g.voffB = (unsigned)((8 * kg + row) * NW_ + n) * 2u; }
    g.aoff = lds_byte(wr * 64 + fr, fq * 8);
    g.boff0 = fq * 2048 + wc * 512 + (fr >> 2) * 64 + (32 * (fq & 1)) + 8 * (fr & 3);
    g.boff1 = g.boff0 ^ 32;
    g.A_kv = (const char*)P.ckv + (size_t)pm * 256 * DC * 2;
    g.A_q = (const char*)P.x + (size_t)pm * 256 * DM * 2;
    g.B_kv = (const char*)(vt ? P.wuv : P.wuk) + head * 512;
    g.B_q = (const char*)P.wq + head * 512 + vt * 256;
    if (wid < 4) {
        g.sbase = (const char*)(wid < 2 ? P.ak : P.av) + (wid & 1) * (64 * DM * 2) + c0 * 2; g.sstep = 128 * DM * 2; g.sv = (unsigned)lane * (DM * 2);
        g.sadd = 0; g.sdst = lds0 + K1_X + (wid >> 1) * 8192 + (wid & 1) * 256; g.sdstep = 512;
    } else {
        if (wid == 4) { g.sbase = g.A_kv + (size_t)((head & 1) * 2 + vt) * 64 * (DC * 2); g.sstep = 128; g.sv = (unsigned)lane * (DC * 2); }
        else if (wid == 5) { g.sbase = g.B_kv + (size_t)pm * 8 * (NW_ * 2); g.sstep = 64 * NW_ * 2; g.sv = (unsigned)((lane >> 2) & 7) * (NW_ * 2) + (lane & 3) * 128; }
        else { g.sbase = (const char*)(wid == 6 ? P.ak : P.av) + (size_t)j * 4 * (DM * 2) + xcd * 112; g.sstep = 128 * DM * 2; g.sv = (unsigned)(lane & 3) * (DM * 2) + ((lane >> 2) & 1) * 108; }
        g.sadd = 4; g.sdst = lds0 + K1_X + 16384 + wid * 256; g.sdstep = 0;
    }
    PIN(g.voffA_kv); PIN(g.voffB); PIN(g.sv); PIN(g.aoff); PIN(g.boff0); PIN(g.boff1);
    stage_units(g, std::make_integer_sequence<int, 8>{});
    WAIT_V(12);
    auto warm = [&](int slot) {
        const unsigned sink = lds0 + K1_X + 16384 + wid * 256;
        if (slot > 0) { if (wid >= 4) side(g, slot - 5); }
        else if (wid < 3) touch(sink, (unsigned)lane * (DM * 2), g.A_q + (size_t)((head & 1) * 2 + vt) * 64 * (DM * 2) + (4 + wid) * 128);
        else if (wid == 3) touch(sink, (unsigned)((lane < 48 ? lane : 0) >> 1) * (NW_ * 2) + (lane & 1) * 128, g.B_q + (size_t)(256 + pm * 24) * (NW_ * 2));
    };
    asm volatile("" : "+s"(sw), "+s"(dw));
    if (b == 0 && tid < N_AFLAGS + N_CFLAGS) {
#pragma unroll
        for (int q = 0; q < 4; ++q) *(u32x4*)((unsigned*)(P.ws + WS_FLAGS) + tid * FLAG_STRIDE + 4 * q) = (u32x4){0u, 0u, 0u, 0u};
    }
    bf16x8 At[4][2], B0[2][2];
    BAR;
    if (wr == 1) BAR;
    TS(P.ws, 0, 1);
    {
        f32x4 q0[4][2], q1[4][2];
        zero8(q0); zero8(q1);
        qphases(g, q0, q1, At, B0, warm, std::make_integer_sequence<int, 14>{});
        stage_unit<27>(g); stage_unit<28>(g); SBAR();
        Out1 o;
        o.rs = __builtin_amdgcn_make_buffer_rsrc(P.ws + WS_Q + (size_t)head * MB, (short)0, (int)MB, 0x00020000);
        o.voff = kq_off(pm, wr, wc, fr, fq) + vt * 8192; o.sbj = 65536; o.shi = 16384;
        pack_kq(q0, o.pk, 0.0625f * 1.4426950408889634f); pack_kq(q1, o.pk + 4, 0.0625f * 1.4426950408889634f);
        st_range<0, 8>(o);
        SBAR();
    }
    TS(P.ws, 0, 2);
    Out1 o;
    if (vt) big_tile<true>(g, P, pm, head, c0, e, wid, At, B0, o); else big_tile<false>(g, P, pm, head, c0, e, wid, At, B0, o);
    o.rs = __builtin_amdgcn_make_buffer_rsrc(P.ws + (vt ? WS_V : WS_K) + (size_t)head * MB, (short)0, (int)MB, 0x00020000);
    o.sbj = vt ? 16384 : 8192; o.shi = vt ? 2048 : 16384;
    SBAR(); st_range<0, 4>(o); SBAR();
    WAIT_V(4);
    if (wr == 0) BAR;
    BAR;
    TS(P.ws, 0, 4);
    const int lane2 = lane_id();
    wo_image(lds, P, e, wid, lane2);
    if (j < 28) conv_part(lds, P, c0, sw, dw, wid, lane2, o);
    else { st_range<4, 16>(o); gate_part(P, (xcd * 4 + (j - 28)) * 64, wid, lane2); }
    TS(P.ws, 0, 7);
}
#undef SA
#undef SB

constexpr int AT_K = 0, AT_V = 65536, AT_LDS = 163840;
constexpr float THR = 8.0f;
constexpr int PD = 6;
constexpr int NEB = 24;
template <int... Is> using iseq = std::integer_sequence<int, Is...>;
template <int N> using make_iseq = std::make_integer_sequence<int, N>;
#define ACLOB "a0","a1","a2","a3","a4","a5","a6","a7","a8","a9","a10","a11","a12","a13","a14","a15","a16","a17","a18","a19","a20","a21","a22","a23","a24","a25","a26","a27","a28","a29","a30","a31","a32","a33","a34","a35","a36","a37","a38","a39","a40","a41","a42","a43","a44","a45","a46","a47","a48","a49","a50","a51","a52","a53","a54","a55","a56","a57","a58","a59","a60","a61","a62","a63","a64","a65","a66","a67","a68","a69","a70","a71","a72","a73","a74","a75","a76","a77","a78","a79","a80","a81","a82","a83","a84","a85","a86","a87","a88","a89","a90","a91","a92","a93","a94","a95","a96","a97","a98","a99","a100","a101","a102","a103","a104","a105","a106","a107","a108","a109","a110","a111","a112","a113","a114","a115","a116","a117","a118","a119","a120","a121","a122","a123","a124","a125","a126","a127","a128","a129","a130","a131","a132","a133","a134","a135","a136","a137","a138","a139","a140","a141","a142","a143","a144","a145","a146","a147","a148","a149","a150","a151","a152","a153","a154","a155","a156","a157","a158","a159","a160","a161","a162","a163","a164","a165","a166","a167","a168","a169","a170","a171","a172","a173","a174","a175","a176","a177","a178","a179","a180","a181","a182","a183","a184","a185","a186","a187","a188","a189","a190","a191","a192","a193","a194","a195","a196","a197","a198","a199","a200","a201","a202","a203","a204","a205","a206","a207","a208","a209","a210","a211","a212","a213","a214","a215","a216","a217","a218","a219","a220","a221","a222","a223","a224","a225","a226","a227","a228","a229","a230","a231","a232","a233","a234","a235","a236","a237","a238","a239","a240","a241","a242","a243","a244","a245","a246","a247","a248","a249","a250","a251","a252","a253","a254","a255"

constexpr int op_slot(int s) { switch (s % 5) { case 0: return 0; case 1: return 32768; case 2: return 65536; case 3: return 131072; default: return 98304; } }
static_assert(AT_V + 32768 == 98304 && AT_LDS == 163840, "lds");
struct OPJ {
    const char *wbase, *xp, *yp, *gp, *pb;
    unsigned cv, lds0, xo, yo, po; ldscp wp;
    const unsigned* fl; unsigned* pub;
    unsigned look, done;
    u32x2 xw[4]; unsigned yw[16]; float g; unsigned char* ws;
};
struct AT {
    OPJ o;
    u32x2 pk;
    f32x2 ml2;
    const char *part, *mlp;
    bool isA, early;
    unsigned* flag;
    f32x16 s[2][2];
    f32x16 cm[2];
    u32x4 p[4];
    bf16x8 fr[8];
    float m, l, rs0, rs1, mx0, mx1, alpha, aone;
    bool pend, first, drain;
    ldscp kb;
    ldscp vcur;
    unsigned goff;
    unsigned m0k, m0v;
    const char *gk, *gv;
};

DI void dma_piece(unsigned m0v, unsigned goff, const char* g, const int off) {
    switch (off) {
    case 0: asm volatile("s_mov_b32 m0, %0\n\ts_nop 0\n\tglobal_load_lds_dwordx4 %1, %2" :: "s"(m0v), "v"(goff), "s"(g) : "memory"); break;
    case 1: asm volatile("s_mov_b32 m0, %0\n\ts_nop 0\n\tglobal_load_lds_dwordx4 %1, %2 offset:1024" :: "s"(m0v), "v"(goff), "s"(g) : "memory"); break;
    case 2: asm volatile("s_mov_b32 m0, %0\n\ts_nop 0\n\tglobal_load_lds_dwordx4 %1, %2 offset:2048" :: "s"(m0v), "v"(goff), "s"(g) : "memory"); break;
    default: asm volatile("s_mov_b32 m0, %0\n\ts_nop 0\n\tglobal_load_lds_dwordx4 %1, %2 offset:3072" :: "s"(m0v), "v"(goff), "s"(g) : "memory"); break;
    }
}
template <int I> DI void dma8(unsigned m0base, unsigned goff, const char* g) { dma_piece(m0base + (I >> 2) * 4096, goff, g + (I >> 2) * 4096, I & 3); }
DI void dma_tile(unsigned m0base, unsigned goff, const char* g) {
    dma8<0>(m0base, goff, g); dma8<1>(m0base, goff, g); dma8<2>(m0base, goff, g); dma8<3>(m0base, goff, g);
    dma8<4>(m0base, goff, g); dma8<5>(m0base, goff, g); dma8<6>(m0base, goff, g); dma8<7>(m0base, goff, g);
}
DI float ex2(float x) { return __builtin_amdgcn_exp2f(x); }
DI float MX3(float a, float b, float c) { float r; asm("v_max3_f32 %0, %1, %2, %3" : "=v"(r) : "v"(a), "v"(b), "v"(c)); return r; }
DI float MX2(float a, float b) { float r; asm("v_max_f32_e32 %0, %1, %2" : "=v"(r) : "v"(a), "v"(b)); return r; }
template <int D0> DI void qk_init(f32x16& s, const bf16x8& k, const f32x16& c) {
    asm volatile("v_mfma_f32_32x32x16_bf16 %0, %1, a[%c3:%c4], %2" : "=&v"(s) : "v"(k), "v"(c), "i"(128 + 4 * D0), "i"(131 + 4 * D0)); }
template <int D0> DI void qk_acc(f32x16& s, const bf16x8& k) {
    asm volatile("v_mfma_f32_32x32x16_bf16 %0, %1, a[%c2:%c3], %0" : "+v"(s) : "v"(k), "i"(128 + 4 * D0), "i"(131 + 4 * D0)); }
template <int DB> DI void pv_acc(const bf16x8& v, const u32x4& p) {
    asm volatile("v_mfma_f32_32x32x16_bf16 a[%c2:%c3], %0, %1, a[%c2:%c3]" :: "v"(v), "v"(p), "i"(16 * DB), "i"(16 * DB + 15)); }
DI void add_bcast(f32x16& x, float aone, float b) { asm volatile("s_nop 1\n\tv_mfma_f32_32x32x2_f32 %0, %1, %2, %0" : "+v"(x) : "v"(aone), "v"(b)); }
#define NOPS16() asm volatile("s_nop 7\n\ts_nop 7" ::: "memory")
#define NOPS2() asm volatile("s_nop 1" ::: "memory")
template <int I> DI void a_zero() { asm volatile("v_accvgpr_write_b32 a[%c0], 0" :: "i"(I)); }
template <int I> DI void a_scale(float f) { float t; asm volatile("v_accvgpr_read_b32 %0, a[%c2]\n\tv_mul_f32 %0, %1, %0\n\tv_accvgpr_write_b32 a[%c2], %0" : "=&v"(t) : "v"(f), "i"(I)); }
template <int I> DI void a_qmov() { asm volatile("v_accvgpr_mov_b32 a[%c0], a[%c1]" :: "i"(128 + I), "i"(192 + I)); }
template <int I> DI float a_read() { float t; asm volatile("v_accvgpr_read_b32 %0, a[%c1]" : "=v"(t) : "i"(I)); return t; }
template <int F> DI void q_load(unsigned voff, const char* g) {
    asm volatile("global_load_dwordx4 a[%c2:%c3], %0, %1 offset:%c4" :: "v"(voff), "s"(g + ((F & 15) >> 2) * 4096), "i"(128 + 4 * F), "i"(131 + 4 * F), "i"((F & 3) * 1024) : "memory"); }
template <int K> DI void pk_store(const AT& c, const u32x4 pk) {
    asm volatile("global_store_dwordx4 %0, %1, %2 offset:%c3 sc1\n\ts_nop 1" :: "v"(c.o.cv), "v"(pk), "s"(c.part + (K >> 2) * 4096), "i"((K & 3) * 1024) : "memory"); }
template <int K> DI void p_load(const AT& c) {
    asm volatile("global_load_dwordx4 a[%c2:%c3], %0, %1 offset:%c4 sc1" :: "v"(c.o.cv), "s"(c.part + (K >> 2) * 4096), "i"(192 + 4 * K), "i"(195 + 4 * K), "i"((K & 3) * 1024) : "memory"); }
DI void ml_load(AT& c) { asm volatile("global_load_dwordx2 %0, %1, %2 sc1" : "+v"(c.ml2) : "v"((c.o.cv & 496u) >> 1), "s"(c.mlp) : "memory"); }
template <int I> DI void op_wpiece(const OPJ& o) {
    constexpr int S = I >> 3, H = (I & 4) * 1024;
    dma_piece(o.lds0 + op_slot(S) + H, o.cv, o.wbase + S * 32768 + H, I & 3);
}
template <int DB> DI void pv_new(const bf16x8& v, const u32x4& p) {
    asm volatile("v_mfma_f32_32x32x16_bf16 a[%c2:%c3], %0, %1, 0" :: "v"(v), "v"(p), "i"(16 * DB), "i"(16 * DB + 15)); }
template <int... Is> DI void a_zeros(iseq<Is...>) { (a_zero<Is>(), ...); }
template <int... Is> DI void a_scales(float f, iseq<Is...>) { (a_scale<Is>(f), ...); }
template <int... Is> DI void a_qmovs(iseq<Is...>) { (a_qmov<Is>(), ...); }
template <int B, int... Is> DI void q_loads(unsigned voff, const char* g, iseq<Is...>) { (q_load<B + Is>(voff, g), ...); }
template <int... Is> DI void p_loads(const AT& c, iseq<Is...>) { (p_load<Is>(c), ...); }
template <int B, int... Is> DI void a_reads(float (&o)[16], iseq<Is...>) { ((o[Is] = a_read<B + Is>()), ...); }

template <int KS, int R> DI bf16x8 kfrag(const AT& c) { return ldsrd16(c.kb + KS * 32768 + ((R & 1) * 16 + (R >> 1)) * 1024); }
template <int R, bool DBM = false> DI bf16x8 vfrag(ldscp v) {
    return ldsrd16(v + (DBM ? (R >> 2) * 4096 + (R & 3) * 1024 : (R & 7) * 4096 + (R >> 3) * 1024)); }
template <int Q> DI void pk_quad(AT& c) {
    const float t0 = a_read<4 * Q>(), t1 = a_read<4 * Q + 1>(), t2 = a_read<4 * Q + 2>(), t3 = a_read<4 * Q + 3>();
    if constexpr (Q & 1) pk_store<(Q >> 1)>(c, (u32x4){c.pk[0], c.pk[1], cvtpk(t0, t1), cvtpk(t2, t3)});
    else { c.pk = (u32x2){cvtpk(t0, t1), cvtpk(t2, t3)}; PIN(c.pk); }
}
template <int X, int E> DI void exp_sum(AT& c) {
    const float e = ex2(c.s[X][E >> 4][E & 15]); c.s[X][E >> 4][E & 15] = e;
    if constexpr (E & 1) c.rs1 += e; else c.rs0 += e;
}
template <int X, int W> DI void pack1(AT& c) {
    c.p[W >> 2][W & 3] = cvtpk(c.s[X][W >> 3][(2 * W) & 15], c.s[X][W >> 3][(2 * W + 1) & 15]);
}
template <int X, int MODE, int N> DI void gapA(AT& c) {
    constexpr int d0 = N >> 1, kb = N & 1, Y = X ^ 1;
    if constexpr (d0 == 0) qk_init<0>(c.s[Y][kb], c.fr[N & 7], c.cm[(MODE & 4) ? kb : 0]);
    else qk_acc<d0>(c.s[Y][kb], c.fr[N & 7]);
    constexpr int R = N + PD;
    if constexpr (R < 32) c.fr[R & 7] = kfrag<Y, R>(c); else if constexpr (MODE & 1) c.fr[R & 7] = vfrag<R - 32, (MODE & 18) == 18>(c.vcur);
    if constexpr (MODE & 1) {
        if constexpr (N < 32 - NEB) { exp_sum<X, NEB + N>(c); PIN(c.rs0); PIN(c.rs1); }
        if constexpr (N >= 10 && N < 26) { pack1<X, N - 10>(c); PIN(c.p[(N - 10) >> 2]); }
    }
    if constexpr ((MODE & 4) && N == 2) asm volatile("" :: "v"(c.cm[0]), "v"(c.cm[1]));
    if constexpr ((MODE & 18) == 16) pk_quad<N>(c);
    if constexpr ((MODE & 18) == 18 && N >= 24) pk_quad<N - 24>(c);
    if constexpr ((MODE & 2) && (N & 1)) {
        if constexpr (N < 16) dma8<(N >> 1)>(c.m0k, c.goff, c.gk); else dma8<((N - 16) >> 1)>(c.m0v, c.goff, c.gv);
    }
    SBAR();
}
template <int X> DI void decide(AT& c) {
    constexpr int Y = X ^ 1;
    float mm = MX2(c.mx0, c.mx1);
    auto rr = __builtin_amdgcn_permlane32_swap(__float_as_uint(mm), __float_as_uint(mm), false, false);
    const float rm = MX2(__uint_as_float(rr[0]), __uint_as_float(rr[1]));
    if (__builtin_expect(c.first || __any(rm > THR), 0)) {
        const float dl = c.first ? rm : MX2(rm, 0.f);
        c.m += dl; c.alpha = ex2(-dl); c.pend = !c.first;
        const float nd = -dl;
        add_bcast(c.s[Y][0], c.aone, nd); add_bcast(c.s[Y][1], c.aone, nd); add_bcast(c.cm[0], c.aone, nd);
        asm volatile("s_nop 7\n\ts_nop 7\n\ts_nop 3" : "+v"(c.s[Y][0]), "+v"(c.s[Y][1]), "+v"(c.cm[0]));
    }
}
template <int X, int I> DI void maxstep(AT& c) {
    constexpr int Y = X ^ 1, h = I >> 2, r = 4 * (I & 3);
    if constexpr (I == 0) { c.mx0 = MX2(c.s[Y][0][0], c.s[Y][0][1]); c.mx1 = MX2(c.s[Y][0][2], c.s[Y][0][3]); }
    else { c.mx0 = MX3(c.mx0, c.s[Y][h][r], c.s[Y][h][r + 1]); c.mx1 = MX3(c.mx1, c.s[Y][h][r + 2], c.s[Y][h][r + 3]); }
}
template <int X, int MODE, int N> DI void gapB(AT& c) {
    constexpr bool DBM = (MODE & 18) == 18;
    constexpr int ks = DBM ? N & 3 : N >> 3, db = DBM ? N >> 2 : N & 7, Y = X ^ 1;
    if constexpr ((MODE & 16) && ks == 0) pv_new<db>(c.fr[N & 7], c.p[ks]); else pv_acc<db>(c.fr[N & 7], c.p[ks]);
    if constexpr (DBM && N < 24) pk_quad<N + 8>(c);
    if constexpr ((MODE & 32) && N == 10) {
        if (c.isA) {
            asm volatile("s_waitcnt vmcnt(0)" ::: "memory");
            c.early = rfl(__float_as_uint(a_read<192>())) != 0u;
            __builtin_amdgcn_fence(__ATOMIC_ACQUIRE, "wavefront");
            if (c.early) ml_load(c);
        }
    }
    if constexpr ((MODE & 32) && N > 10 && N < 27) { if (c.early) p_load<N - 11>(c); }
    if constexpr ((MODE & 48) == 48 && N == 16) { asm volatile("s_waitcnt vmcnt(0)" ::: "memory"); __hip_atomic_store(c.flag, 1u, RLX_AGENT); }
    if constexpr ((MODE & 48) == 32 && N == 28) asm volatile("global_load_dword a128, %0, %1 sc1" :: "v"(0u), "s"(c.flag) : "memory");
    if constexpr (MODE & 64) op_wpiece<N>(c.o);
    constexpr int R = N + PD;
    if constexpr (R < 32) c.fr[R & 7] = vfrag<R, DBM>(c.vcur);
    if constexpr (MODE & 1) {
        if constexpr (MODE & 2) {
            if constexpr (N == 28) { c.fr[0] = kfrag<X, 0>(c); c.fr[1] = kfrag<X, 1>(c); }
            if constexpr (N == 29) { c.fr[2] = kfrag<X, 2>(c); c.fr[3] = kfrag<X, 3>(c); }
            if constexpr (N == 30) { c.fr[4] = kfrag<X, 4>(c); }
            if constexpr (N == 31) { c.fr[5] = kfrag<X, 5>(c); }
        }
        if constexpr (N == 1) { asm volatile("s_nop 7" : "+v"(c.s[Y][0]), "+v"(c.s[Y][1])); }
        if constexpr (N >= 2 && N < 6) { maxstep<X, 2 * (N - 2)>(c); maxstep<X, 2 * (N - 2) + 1>(c); PIN(c.mx0); PIN(c.mx1); }
        if constexpr (N == 6) { c.l += c.rs0 + c.rs1; c.rs0 = 0.f; c.rs1 = 0.f; PIN(c.l); }
        if constexpr (N == 7) decide<X>(c);
        if constexpr (N >= 8) { exp_sum<Y, N - 8>(c); PIN(c.rs0); PIN(c.rs1); }
    }
    SBAR();
    if constexpr ((MODE & 1) && N == 27) {
        if constexpr (MODE & 2) {
            if (X == 0 && c.drain) asm volatile("s_waitcnt vmcnt(0)" ::: "memory");
            if constexpr (MODE & 16) asm volatile("s_waitcnt vmcnt(24) lgkmcnt(0)\n\ts_barrier" ::: "memory");
            else asm volatile("s_waitcnt vmcnt(8) lgkmcnt(0)\n\ts_barrier" ::: "memory");
        } else {
            if (!c.isA) asm volatile("s_waitcnt vmcnt(0)" ::: "memory");
            asm volatile("s_waitcnt lgkmcnt(0)\n\ts_barrier" ::: "memory");
        }
        SBAR();
    }
}
template <int X, int MODE, int... Ns> DI void phaseA(AT& c, iseq<Ns...>) { (gapA<X, MODE, Ns>(c), ...); }
template <int X, int MODE, int... Ns> DI void phaseB(AT& c, iseq<Ns...>) { (gapB<X, MODE, Ns>(c), ...); }
template <int X, int B, int... Es> DI void exps_from(AT& c, iseq<Es...>) { (exp_sum<X, B + Es>(c), ...); }
template <int X, int... Ws> DI void packs(AT& c, iseq<Ws...>) { (pack1<X, Ws>(c), ...); }
template <int... Is> DI void maxsteps1(AT& c, iseq<Is...>) { (maxstep<1, Is>(c), ...); }
DI void rescale_o(AT& c) {
    if (__builtin_expect(c.pend, 0)) {
        NOPS16();
        a_scales(c.alpha, make_iseq<128>{});
        NOPS2();
        c.l *= c.alpha; c.pend = false;
    }
}
DI float halfsum(float x) { auto rr = __builtin_amdgcn_permlane32_swap(__float_as_uint(x), __float_as_uint(x), false, false); return __uint_as_float(rr[0]) + __uint_as_float(rr[1]); }
DI void build_mask(AT& c, int mrel) {
    const float NEG = -__builtin_inff(), nm = -c.m;
#pragma unroll
    for (int r = 0; r < 16; ++r) { const int kk = (r & 3) + 8 * (r >> 2); c.cm[0][r] = kk > mrel ? NEG : nm; c.cm[1][r] = kk + 32 > mrel ? NEG : nm; }
    PIN(c.cm[0]); PIN(c.cm[1]); NOPS2();
}

struct ATW {
    const char *Kh, *Vh; ldsp lds; unsigned mk, mv; int n1, vs, lane, wid, r32, hi, qrel; bool isA;
};
DI int tile_of(const ATW& w, int g) { return w.isA ? w.n1 + g : (g < w.n1 ? g : g - w.n1); }
template <int X, bool DMA> DI void setup(AT& c, const ATW& w, int g) {
    c.vcur = w.lds + AT_V + w.vs * 32768 + w.lane * 16;
    if constexpr (DMA) {
        const int v2 = w.vs == 0 ? 2 : w.vs - 1, tn = tile_of(w, g + 2);
        c.gk = w.Kh + (size_t)tn * 32768; c.gv = w.Vh + (size_t)tn * 32768; c.m0k = w.mk + X * 32768; c.m0v = w.mv + v2 * 32768;
    }
}
template <bool LAST, bool seam> DI void pair(AT& c, ATW& w, int g0) {
    setup<0, true>(c, w, g0);
    c.first = seam;
    const float m_old = c.m;
    if constexpr (seam) { a_qmovs(make_iseq<64>{}); NOPS2(); }
    int qr = w.qrel;
    if constexpr (LAST) {
        if constexpr (seam) asm volatile("; last pair, two segments" : "+v"(qr), "+v"(c.s[0][0]), "+v"(c.s[0][1]), "+v"(c.m));
        else asm volatile("; last pair" : "+v"(qr), "+v"(c.s[0][0]), "+v"(c.s[0][1]), "+v"(c.m));
        build_mask(c, qr);
    }
    SBAR();
    phaseA<0, LAST ? 7 : 3>(c, make_iseq<32>{});
    phaseB<0, 3>(c, make_iseq<32>{});
    rescale_o(c);
    if (c.drain) { __hip_atomic_store(c.flag, 1u, RLX_AGENT); c.drain = false; }
    c.first = false;
    w.vs = w.vs == 2 ? 0 : w.vs + 1;
    setup<1, !LAST>(c, w, g0 + 1);
    if constexpr (LAST || seam) asm volatile("" : "+s"(c.part));
    if constexpr (LAST) {
        asm volatile("global_load_dword a192, %0, %1 sc1" :: "v"(0u), "s"(c.flag) : "memory");
        PIN(qr); build_mask(c, qr - 64);
    }
    if constexpr (seam) {
        const float lt = halfsum(c.l);
        __hip_atomic_store((unsigned long long*)c.mlp + w.r32, ((unsigned long long)__float_as_uint(lt) << 32) | __float_as_uint(m_old), RLX_AGENT);
        c.l = 0.f;
        asm volatile("; second segment" : "+v"(c.s[1][0]), "+v"(c.s[1][1]), "+v"(c.rs0), "+v"(c.rs1), "+v"(c.l));
        SBAR();
        phaseA<1, (LAST ? 5 : 3) | 16>(c, make_iseq<32>{});
        phaseB<1, (LAST ? 33 : 3) | 16>(c, make_iseq<32>{});
        if constexpr (!LAST) c.drain = true;
    } else {
        SBAR();
        phaseA<1, LAST ? 5 : 3>(c, make_iseq<32>{});
        phaseB<1, LAST ? 33 : 3>(c, make_iseq<32>{});
    }
    rescale_o(c);
    w.vs = w.vs == 2 ? 0 : w.vs + 1;
}

constexpr int op_blk(int mb) { return (mb + 8) % 14; }
template <int R> DI bf16x8 op_wfrag(const OPJ& o) { return ldsrd16(o.wp + op_slot(R >> 5) + (R & 31) * 1024); }
template <int I> DI void op_eload(OPJ& o) {
    if constexpr (I < 12) asm volatile("global_load_ushort %0, %1, %2" : "=v"(o.yw[I]) : "v"(o.yo), "s"(o.yp + ((I >> 2) * 128 + (I & 3)) * (SEQ * 2)) : "memory");
    else if constexpr (I < 16) asm volatile("global_load_ushort %0, %1, %2" : "=v"(o.yw[I]) : "v"(o.yo & 0x3ffffu), "s"(o.yp + (384 + (I & 3)) * (SEQ * 2)) : "memory");
    else if constexpr (I < 19) asm volatile("global_load_dwordx2 %0, %1, %2 offset:%c3" : "=v"(o.xw[I - 16]) : "v"(o.xo), "s"(o.xp), "i"((I - 16) * 256) : "memory");
    else if constexpr (I == 19) asm volatile("global_load_dwordx2 %0, %1, %2 offset:768" : "=v"(o.xw[3]) : "v"(o.xo - ((o.cv >> 2) & 128u)), "s"(o.xp) : "memory");
    else asm volatile("global_load_dword %0, %1, %2" : "=v"(o.g) : "v"((o.cv & 240u) >> 2), "s"(o.gp) : "memory");
}
constexpr int op_land(int st) { return st < 2 ? 224 + 16 * st : 96 + 16 * st; }
template <int I> DI void op_pload(const OPJ& o) {
    asm volatile("global_load_dwordx4 a[%c2:%c3], %0, %1 sc1" :: "v"(o.po), "s"(o.pb + (I & 3) * 458752 + (I >> 2) * 4096),
                 "i"(op_land(I >> 2) + 4 * (I & 3)), "i"(op_land(I >> 2) + 4 * (I & 3) + 3) : "memory"); }
template <int S> DI void op_take(OPJ& o) {
    if (o.done == S && ((unsigned)__ballot(o.look > (unsigned)S) & 0xffffu) == 0xffffu) {
        op_pload<4 * S>(o); op_pload<4 * S + 1>(o); op_pload<4 * S + 2>(o); op_pload<4 * S + 3>(o); o.done = S + 1; }
}
template <int... Ss> DI void op_takes(OPJ& o, iseq<Ss...>) { __builtin_amdgcn_fence(__ATOMIC_ACQUIRE, "wavefront"); (op_take<Ss>(o), ...); }
DI void op_look(const OPJ& o) { asm volatile("global_load_dword a208, %0, %1 sc1" :: "v"((o.cv >> 2) & 60u), "s"(o.fl) : "memory"); }
template <int N> DI void op_wait(unsigned n) {
    if (N == 0 || n >= N) asm volatile("s_waitcnt vmcnt(%c0)" :: "i"(4 * N) : "memory");
    else if constexpr (N > 0) op_wait<N - 1>(n);
}
DI void op_publish(const OPJ& o, unsigned n) { asm volatile("global_store_dword %0, %1, %2 sc1" :: "v"(0u), "v"(n), "s"(o.pub) : "memory"); }
template <int Q> DI void op_quad(AT& c) {
    constexpr int A = 16 * op_blk(Q >> 2) + 4 * (Q & 3);
    const float t0 = a_read<A>(), t1 = a_read<A + 1>(), t2 = a_read<A + 2>(), t3 = a_read<A + 3>();
    if constexpr (Q & 1) pk_store<(Q >> 1)>(c, (u32x4){c.pk[0], c.pk[1], cvtpk(t0, t1), cvtpk(t2, t3)});
    else { c.pk = (u32x2){cvtpk(t0, t1), cvtpk(t2, t3)}; PIN(c.pk); }
}
template <bool MERGE, int Q> DI void ctx_quad(float f1, float f2, u32x4 (&bf)[16]) {
    const float o0 = a_read<4 * Q>(), o1 = a_read<4 * Q + 1>(), o2 = a_read<4 * Q + 2>(), o3 = a_read<4 * Q + 3>();
    if constexpr (MERGE) {
        const unsigned p0 = __float_as_uint(a_read<192 + 2 * Q>()), p1 = __float_as_uint(a_read<193 + 2 * Q>());
        bf[Q >> 1][2 * (Q & 1)] = cvtpk(o0 * f1 + bf_lo(p0) * f2, o1 * f1 + bf_hi(p0) * f2);
        bf[Q >> 1][2 * (Q & 1) + 1] = cvtpk(o2 * f1 + bf_lo(p1) * f2, o3 * f1 + bf_hi(p1) * f2);
    } else { bf[Q >> 1][2 * (Q & 1)] = cvtpk(o0 * f1, o1 * f1); bf[Q >> 1][2 * (Q & 1) + 1] = cvtpk(o2 * f1, o3 * f1); }
    if constexpr (Q & 1) PIN(bf[Q >> 1]);
}
template <bool MERGE, int QO, int G> DI void op_step(AT& c, u32x4 (&bf)[16], float f1, float f2) {
    constexpr int S = G >> 5, N = G & 31;
    if constexpr (N < 2) pv_new<op_blk(2 * S + N)>(c.fr[G & 7], bf[0]); else pv_acc<op_blk(2 * S + (N & 1))>(c.fr[G & 7], bf[N >> 1]);
    if constexpr (N == 32 - PD && S < 4) {
        constexpr int CNT[4] = {45, 41, 45, 4};
        asm volatile("s_waitcnt vmcnt(%c0)\n\ts_barrier" :: "i"(CNT[S]) : "memory");
    }
    if constexpr (N == 32 - PD && S >= 3) {
        if constexpr (S > 3) asm volatile("s_waitcnt vmcnt(5)" ::: "memory");
        op_publish(c.o, S - 1);
    }
    if constexpr (G + PD < 224) c.fr[(G + PD) & 7] = op_wfrag<G + PD>(c.o);
    if constexpr (G < 8) op_wpiece<32 + G>(c.o);
    if constexpr (G >= 5 && G < 26) op_eload<G - 5>(c.o);
    if constexpr (G + QO < 32) ctx_quad<MERGE, G + QO>(f1, f2, bf);
    if constexpr (G >= 58 && G < 66) op_wpiece<40 + G - 58>(c.o);
    if constexpr (G >= 90 && G < 98) op_wpiece<48 + G - 90>(c.o);
    if constexpr (S >= 1 && N >= 2 && N <= 23 && (N - 2) % 3 == 0) op_quad<8 * (S - 1) + (N - 2) / 3>(c);
    SBAR();
}
template <bool MERGE, int QO, int B, int... Is> DI void op_steps(AT& c, u32x4 (&bf)[16], float f1, float f2, iseq<Is...>) { (op_step<MERGE, QO, B + Is>(c, bf, f1, f2), ...); }
template <bool MERGE, int QO> DI void op_first(AT& c, u32x4 (&bf)[16], float f1, float f2) {
    SBAR();
    asm volatile("s_waitcnt vmcnt(24)\n\ts_barrier" ::: "memory");
    c.fr[0] = op_wfrag<0>(c.o); c.fr[1] = op_wfrag<1>(c.o); c.fr[2] = op_wfrag<2>(c.o); c.fr[3] = op_wfrag<3>(c.o); c.fr[4] = op_wfrag<4>(c.o); c.fr[5] = op_wfrag<5>(c.o);
    SBAR();
    op_steps<MERGE, QO, 0>(c, bf, f1, f2, make_iseq<32>{});
}
DI void merge_factors(const AT& c, float lt, float& f1, float& f2) {
    const float mn = __builtin_fmaxf(c.m, c.ml2[0]);
    const float w1 = ex2(c.m - mn), w2 = ex2(c.ml2[0] - mn);
    const float inv = 1.0f / (lt * w1 + c.ml2[1] * w2);
    f1 = w1 * inv; f2 = w2 * inv;
}
template <bool MERGE, int N> DI void tail_gap(AT& c, u32x4 (&bf)[16], float lt, float& f1, float& f2) {
    pv_acc<(N >> 2)>(c.fr[N & 7], c.p[N & 3]);
    op_wpiece<N>(c.o);
    if constexpr (N + PD < 32) c.fr[(N + PD) & 7] = vfrag<N + PD, true>(c.vcur);
    if constexpr (MERGE && N == 4) { asm volatile("s_waitcnt vmcnt(22)" : "+v"(c.ml2) :: "memory"); merge_factors(c, lt, f1, f2); PIN(f1); PIN(f2); }
    if constexpr (MERGE && N >= 6 && !(N & 1)) asm volatile("s_waitcnt vmcnt(%c0)" :: "i"(23 + (N - 6) / 2) : "memory");
    if constexpr (N >= 6) ctx_quad<MERGE, N - 6>(f1, f2, bf);
    SBAR();
}
template <bool MERGE, int... Ns> DI void tail_fast(AT& c, u32x4 (&bf)[16], float lt, iseq<Ns...>) {
    float f1 = MERGE ? 0.f : 1.0f / lt, f2 = 0.f;
    c.fr[0] = vfrag<0, true>(c.vcur); c.fr[1] = vfrag<1, true>(c.vcur); c.fr[2] = vfrag<2, true>(c.vcur);
    c.fr[3] = vfrag<3, true>(c.vcur); c.fr[4] = vfrag<4, true>(c.vcur); c.fr[5] = vfrag<5, true>(c.vcur);
    SBAR(); NOPS2(); SBAR();
    (tail_gap<MERGE, Ns>(c, bf, lt, f1, f2), ...);
    op_first<MERGE, 26>(c, bf, f1, f2);
}
template <int... Is> DI void op_last(AT& c, iseq<Is...>) { (op_quad<48 + Is>(c), ...); }
template <int K> DI void op_add(f32x4 (&acc)[4], const u32x4 (&sel)[2]) {
    constexpr int S = 2 * (K & 3) + (K >> 4), A = op_land(S) + 4 * ((K >> 2) & 3);
    if constexpr (S < 7) asm volatile("v_mfma_f32_16x16x32_bf16 %0, %1, a[%c2:%c3], %0" : "+v"(acc[K & 3]) : "v"(sel[S & 1]), "i"(A), "i"(A + 3)); }
template <int... Ks> DI void op_adds(f32x4 (&acc)[4], const u32x4 (&sel)[2], iseq<Ks...>) { (op_add<Ks>(acc, sel), ...); }

__global__ __launch_bounds__(256, 1) void k2_attn(unsigned char* ws, const bf16_t* x, bf16_t* out) {
    extern __shared__ __attribute__((aligned(16))) unsigned char shm[];
    ldsp lds = (ldsp)shm;
    const int tid = threadIdx.x, wid = rfl(tid >> 6), lane = tid & 63, r32 = lane & 31, hi = lane >> 5;
    const int b = blockIdx.x, xcd = b & 7, j = b >> 3, head = 2 * xcd + (j >> 4), w16 = j & 15;
    const bool isA = w16 >= 8; const int i = w16 & 7, bq = 15 - i;
    const int seg = head * 8 + i;
    ATW w;
    w.Kh = (const char*)ws + WS_K + (size_t)head * MB; w.Vh = (const char*)ws + WS_V + (size_t)head * MB;
    const char* Qh = (const char*)ws + WS_Q + (size_t)head * MB;
    w.lds = lds; w.n1 = 15 - 2 * i; w.vs = 0; w.lane = lane; w.wid = wid; w.r32 = r32; w.hi = hi; w.isA = isA; w.qrel = wid * 32 + r32 - 4 * hi;
    AT c;
    const unsigned lds0 = (unsigned)(uintptr_t)lds;
    c.part = (const char*)ws + WS_PART + (size_t)(seg * 4 + wid) * 16384; c.mlp = (const char*)ws + WS_ML + (seg * 4 + wid) * 256;
    c.flag = (unsigned*)(ws + WS_FLAGS) + seg * FLAG_STRIDE + wid;
    c.isA = isA; c.early = false; c.ml2 = (f32x2){0.f, 0.f};
    OPJ& o = c.o;
    o.cv = (unsigned)lane * 16u;
    c.goff = (unsigned)wid * 8192u + (unsigned)lane * 16u;
    c.kb = lds + AT_K + lane * 16;
    w.mk = lds0 + AT_K + wid * 8192; w.mv = lds0 + AT_V + wid * 8192;
    TS(ws, 1, 0);
    asm volatile("s_nop 4" ::: ACLOB);
    dma_tile(w.mk, c.goff, w.Kh + (size_t)tile_of(w, 0) * 32768);
    q_loads<0>((unsigned)lane * 16u, Qh + (size_t)(bq * 4 + wid) * 16384, make_iseq<16>{});
    dma_tile(w.mk + 32768, c.goff, w.Kh + (size_t)tile_of(w, 1) * 32768);
    dma_tile(w.mv, c.goff, w.Vh + (size_t)tile_of(w, 0) * 32768);
    if (wid == 3) CODE_TOUCH2(K2_SYM, lds0 + AT_V + 65536, j & 15, lane);
    const bool q2now = !isA && w.n1 <= 3;
    const char* q2p = Qh + (size_t)(i * 4 + wid) * 16384;
    if (q2now) q_loads<16>((unsigned)lane * 16u, q2p, make_iseq<16>{});
    a_zeros(make_iseq<128>{});
    c.cm[0] = f32x16{}; c.cm[1] = f32x16{}; c.m = 0.f; c.l = 0.f; c.rs0 = 0.f; c.rs1 = 0.f; c.alpha = 1.f; c.pend = false; c.drain = false;
    c.aone = hi ? 0.f : 1.f;
    PIN(c.cm[0]);
    if (q2now) asm volatile("s_waitcnt vmcnt(32)" ::: "memory"); else asm volatile("s_waitcnt vmcnt(16)" ::: "memory");
    asm volatile("s_barrier" ::: "memory");
    SBAR();
    c.fr[0] = kfrag<0, 0>(c); c.fr[1] = kfrag<0, 1>(c); c.fr[2] = kfrag<0, 2>(c); c.fr[3] = kfrag<0, 3>(c); c.fr[4] = kfrag<0, 4>(c); c.fr[5] = kfrag<0, 5>(c);
    SBAR();
    phaseA<1, 0>(c, make_iseq<32>{});
    dma_tile(w.mv + 32768, c.goff, w.Vh + (size_t)tile_of(w, 1) * 32768);
    SBAR(); asm volatile("s_nop 7\n\ts_nop 7" : "+v"(c.s[0][0]), "+v"(c.s[0][1])); SBAR();
    c.first = true;
    maxsteps1(c, make_iseq<8>{});
    decide<1>(c);
    exps_from<0, 0>(c, make_iseq<NEB>{});
    asm volatile("s_waitcnt vmcnt(8) lgkmcnt(0)\n\ts_barrier" ::: "memory");
    SBAR();
    c.fr[0] = kfrag<1, 0>(c); c.fr[1] = kfrag<1, 1>(c); c.fr[2] = kfrag<1, 2>(c); c.fr[3] = kfrag<1, 3>(c); c.fr[4] = kfrag<1, 4>(c); c.fr[5] = kfrag<1, 5>(c);
    TS(ws, 1, 1);
    const int q2at = isA || q2now ? -1 : 2;
    for (int g0 = 0, gstop = isA ? 14 : w.n1 - 1; ; g0 += 2, gstop = 14) {
        for (; g0 < gstop; g0 += 2) {
            if (g0 == q2at) q_loads<16>(c.o.cv, q2p, make_iseq<16>{});
            pair<false, false>(c, w, g0);
        }
        if (g0 >= 14) break;
        pair<false, true>(c, w, g0);
    }
    TS(ws, 1, 2);
    if (!isA && w.n1 == 15) pair<true, true>(c, w, 14); else pair<true, false>(c, w, 14);
    TS(ws, 1, 3);
    int h2 = head; asm volatile("" : "+s"(h2), "+s"(ws));
    const int rq = isA ? bq : i, fn = lane & 15, rg = lane >> 4;
    const int trow = rq * 128 + (h2 >> 1) * 16, tcol = (h2 & 1) * 32 + wid * 8;
    unsigned* flags = (unsigned*)(ws + WS_FLAGS) + (N_AFLAGS + rq * 4) * FLAG_STRIDE;
    o.wbase = (const char*)ws + WS_WI + (size_t)h2 * 229376 + wid * 8192;
    o.lds0 = lds0 + wid * 8192; o.wp = lds + lane * 16; o.fl = flags + (h2 >> 2) * FLAG_STRIDE; o.pub = flags + wid * FLAG_STRIDE + h2; o.done = 0u;
    o.xp = (const char*)x + ((size_t)trow * DM + tcol) * 2; o.xo = (unsigned)(fn * DM + (rg >> 1) * 64 + (rg & 1) * 4) * 2u;
    o.yp = (const char*)ws + WS_Y2 + ((size_t)tcol * SEQ + trow) * 2; o.yo = (unsigned)(((rg >> 1) * 64 + (rg & 1) * 4) * SEQ + fn) * 2u;
    o.gp = (const char*)ws + WS_G + trow * 4; o.ws = ws;
    o.pb = (const char*)ws + WS_OP + (size_t)(rq * 64 + (h2 >> 2)) * 28672 + ((h2 & 1) * 2 + (wid & 1)) * 1024 + ((wid >> 1) * 32 + (h2 & 2) * 8) * 16;
    o.po = (unsigned)(rg * 114688 + fn * 16);
    c.vcur = lds + AT_V + 32768 + lane * 16;
    SBAR();
    exps_from<0, NEB>(c, make_iseq<32 - NEB>{});
    packs<0>(c, make_iseq<16>{});
    c.l += c.rs0 + c.rs1;
    float lt = halfsum(c.l);
    PIN(c.p[0]); PIN(c.p[1]); PIN(c.p[2]); PIN(c.p[3]); PIN(lt);
    SBAR();
    u32x4 bf[16];
    const char* opart = (const char*)ws + WS_OP + (size_t)((rq * 16 + h2) * 4 + wid) * 28672;
    if (!isA) tail_fast<false>(c, bf, lt, make_iseq<32>{});
    else if (c.early) tail_fast<true>(c, bf, lt, make_iseq<32>{});
    else {
        c.fr[0] = vfrag<0>(c.vcur); c.fr[1] = vfrag<1>(c.vcur); c.fr[2] = vfrag<2>(c.vcur); c.fr[3] = vfrag<3>(c.vcur); c.fr[4] = vfrag<4>(c.vcur); c.fr[5] = vfrag<5>(c.vcur);
        asm volatile("s_waitcnt vmcnt(0)" ::: "memory");
        c.early = rfl(__float_as_uint(a_read<128>())) != 0u;
        __builtin_amdgcn_fence(__ATOMIC_ACQUIRE, "wavefront");
        if (c.early) { ml_load(c); p_loads(c, make_iseq<16>{}); }
        SBAR(); NOPS2(); SBAR();
        phaseB<0, 64>(c, make_iseq<32>{});
        SBAR();
        if (!c.early) {
            unsigned spins = 0;
            while (rfl(__hip_atomic_load(c.flag, RLX_AGENT)) == 0u) { __builtin_amdgcn_s_sleep(1); if (++spins > (1u << 22)) break; }
            __builtin_amdgcn_fence(__ATOMIC_ACQUIRE, "wavefront");
            ml_load(c);
            p_loads(c, make_iseq<16>{});
            asm volatile("s_waitcnt vmcnt(0)" : "+v"(c.ml2) :: "memory");
        }
        asm volatile("s_waitcnt vmcnt(32)" : "+v"(c.ml2) :: "memory");
        float f1, f2;
        merge_factors(c, lt, f1, f2);
        SBAR(); NOPS16(); SBAR();
        ctx_quad<true, 0>(f1, f2, bf); ctx_quad<true, 1>(f1, f2, bf); ctx_quad<true, 2>(f1, f2, bf); ctx_quad<true, 3>(f1, f2, bf);
        op_first<true, 4>(c, bf, f1, f2);
    }
    TS(ws, 1, 4);
    c.part = opart;
    op_steps<false, 32, 32>(c, bf, 0.f, 0.f, make_iseq<192>{});
    NOPS16(); SBAR();
    op_last(c, make_iseq<8>{});
    op_look(o);
    TS(ws, 1, 5);
#pragma unroll
    for (int e = 0; e < 16; ++e) PIN(o.yw[e]);
    PIN(o.xw[0]); PIN(o.xw[1]); PIN(o.xw[2]); PIN(o.xw[3]); PIN(o.g);
    const float g = o.g, og = rbf(1.0f - g);
    float ta[16];
#pragma unroll
    for (int e = 0; e < 8; ++e) {
        ta[2 * e] = rbf(g * rbf(bf_lo(o.xw[e >> 1][e & 1]) * bf_lo(o.yw[2 * e]))); ta[2 * e + 1] = rbf(g * rbf(bf_hi(o.xw[e >> 1][e & 1]) * bf_lo(o.yw[2 * e + 1])));
        PIN(ta[2 * e]); PIN(ta[2 * e + 1]);
    }
    u32x4 sel[2]; f32x4 acc[4];
#pragma unroll
    for (int s2 = 0; s2 < 2; ++s2) {
#pragma unroll
        for (int e = 0; e < 4; ++e) sel[s2][e] = (fn >> 1) == 4 * s2 + e ? 0x3f80u << (16 * (fn & 1)) : 0u;
        acc[2 * s2] = f32x4{}; acc[2 * s2 + 1] = f32x4{}; PIN(sel[s2]); PIN(acc[2 * s2]); PIN(acc[2 * s2 + 1]);
    }
    SBAR();
    asm volatile("s_waitcnt vmcnt(6)" ::: "memory");
    op_publish(o, 6u);
    asm volatile("s_waitcnt vmcnt(2)" ::: "memory");
    op_publish(o, 7u);
    asm volatile("s_waitcnt vmcnt(2)\n\ts_barrier" ::: "memory");
    TS(ws, 1, 7);
    TS(ws, 2, 0);
    for (unsigned spins = 0; spins < (1u << 22); ++spins) {
        const unsigned d0 = o.done;
        const unsigned v = __float_as_uint(a_read<208>());
        o.look = fn == h2 ? 7u : v;
        PIN(o.look);
        op_look(o);
        op_takes(o, make_iseq<7>{});
        if (o.done == 7u) break;
        op_wait<6>(o.done - d0);
        if (o.done == d0) __builtin_amdgcn_s_sleep(1);
    }
    TS(ws, 2, 4);
    asm volatile("s_waitcnt vmcnt(0)" ::: "memory");
    TS(ws, 2, 1);
    op_adds(acc, sel, make_iseq<32>{});
    asm volatile("s_nop 7\n\ts_nop 7" : "+v"(acc[0]), "+v"(acc[1]), "+v"(acc[2]), "+v"(acc[3]));
#pragma unroll
    for (int f = 0; f < 4; ++f) {
        float r[4];
#pragma unroll
        for (int e = 0; e < 4; ++e) r[e] = ta[4 * f + e] + rbf(og * rbf(acc[f][e]));
        if (f < 3 || rg < 2) *(u32x2*)((char*)out + ((size_t)trow * DM + tcol) * 2 + o.xo + f * 256) = (u32x2){cvtpk(r[0], r[1]), cvtpk(r[2], r[3])};
    }
    TS(ws, 2, 7);
}

extern "C" __device__ __attribute__((used, noinline)) void text_end_mark() { asm volatile("s_nop 0"); }

extern "C" void kernel_launch(void* const* d_in, const int* in_sizes, int n_in, void* d_out, int out_size, void* d_ws, size_t ws_size,
                              hipStream_t stream) {
    (void)in_sizes; (void)n_in; (void)out_size; (void)ws_size;
    (void)hipFuncSetAttribute((const void*)k1_proj, hipFuncAttributeMaxDynamicSharedMemorySize, K1_LDS);
    (void)hipFuncSetAttribute((const void*)k2_attn, hipFuncAttributeMaxDynamicSharedMemorySize, AT_LDS);
    K1Args P;
    P.x = (const bf16_t*)d_in[0]; P.ak = (const bf16_t*)d_in[1]; P.av = (const bf16_t*)d_in[2]; P.shift_k = (const bf16_t*)d_in[3];
    P.ssm_k = (const bf16_t*)d_in[4]; P.ssm_d = (const bf16_t*)d_in[5]; P.ckv = (const bf16_t*)d_in[7]; P.wq = (const bf16_t*)d_in[8];
    P.wuk = (const bf16_t*)d_in[9]; P.wuv = (const bf16_t*)d_in[10]; P.gate_w = (const bf16_t*)d_in[12]; P.wo = (const bf16_t*)d_in[11]; P.ws = (unsigned char*)d_ws;
    k1_proj<<<256, 512, K1_LDS, stream>>>(P);
    k2_attn<<<256, 256, AT_LDS, stream>>>((unsigned char*)d_ws, (const bf16_t*)d_in[0], (bf16_t*)d_out);
}
```

```cpp
#include <hip/hip_runtime.h>
#include <hip/hip_bf16.h>
#include <stdint.h>
#include <utility>

#define LAS __attribute__((address_space(3)))
#define GAS __attribute__((address_space(1)))
#define CAS __attribute__((address_space(4)))
#define DI __device__ __forceinline__
typedef unsigned short bf16_t;
typedef short bf16x8 __attribute__((ext_vector_type(8)));
typedef short s16x4 __attribute__((ext_vector_type(4)));
typedef float f32x2 __attribute__((ext_vector_type(2)));
typedef float f32x4 __attribute__((ext_vector_type(4)));
typedef float f32x16 __attribute__((ext_vector_type(16)));
typedef unsigned u32x2 __attribute__((ext_vector_type(2)));
typedef unsigned u32x4 __attribute__((ext_vector_type(4)));
typedef __bf16 bfx2 __attribute__((ext_vector_type(2)));
typedef LAS unsigned char* ldsp;
typedef const LAS unsigned char* ldscp;

constexpr int SEQ = 2048, DM = 448, NH = 16, HD = 256, DC = 1024, NW_ = 4096;

constexpr size_t MB = 1024 * 1024;
constexpr size_t WS_Q = 0, WS_K = 16 * MB, WS_V = 32 * MB, WS_PART = 64 * MB, WS_ML = 80 * MB,
                 WS_Y2 = 81 * MB, WS_G = 84 * MB, WS_FLAGS = 100 * MB, WS_WI = 102 * MB, WS_OP = 128 * MB;
constexpr int FLAG_STRIDE = 64;
constexpr int N_AFLAGS = 128, N_CFLAGS = 64;

#define TS(wsp, kern, slot) do { } while (0)
#define TSI(wsp, idx) do { } while (0)
#define SBAR() __builtin_amdgcn_sched_barrier(0)
#define PIN(x) asm volatile("" : "+v"(x))
#define RLX_AGENT __ATOMIC_RELAXED, __HIP_MEMORY_SCOPE_AGENT

DI unsigned cvtpk(float lo, float hi) { f32x2 v = {lo, hi}; bfx2 b = __builtin_convertvector(v, bfx2); return __builtin_bit_cast(unsigned, b); }
DI float bf_lo(unsigned w) { return __uint_as_float(w << 16); }
DI float bf_hi(unsigned w) { return __uint_as_float(w & 0xffff0000u); }
DI float rbf(float x) { return bf_lo(cvtpk(x, 0.f)); }
DI unsigned rfl(unsigned x) { return __builtin_amdgcn_readfirstlane(x); }
DI s16x4 trread(ldscp p) { return __builtin_amdgcn_ds_read_tr16_b64_v4i16((LAS s16x4*)p); }
DI bf16x8 cat8(s16x4 lo, s16x4 hi) { return __builtin_shufflevector(lo, hi, 0, 1, 2, 3, 4, 5, 6, 7); }
DI bf16x8 ldsrd16(ldscp p) { return *(const LAS bf16x8*)p; }
#define MFMA16(a, b, c) __builtin_amdgcn_mfma_f32_16x16x32_bf16((a), (b), (c), 0, 0, 0)
#define MFMA32(a, b, c) __builtin_amdgcn_mfma_f32_32x32x16_bf16((a), (b), (c), 0, 0, 0)

DI void touch(unsigned sink, unsigned voff, const char* g) {
    asm volatile("s_mov_b32 m0, %0\n\ts_nop 0\n\tglobal_load_lds_dword %1, %2" :: "s"(sink), "v"(voff), "s"(g) : "memory");
}
#define CODE_TOUCH(SYM, END, sink, chunk, lane) do { const char* _p; unsigned _n; \
        asm volatile("s_getpc_b64 s[90:91]\n\ts_add_u32 s90, s90, " SYM "@rel32@lo+4\n\ts_addc_u32 s91, s91, " SYM "@rel32@hi+12\n\ts_mov_b64 %0, s[90:91]\n\ts_mov_b32 %1, " END "-" SYM \
                     : "=s"(_p), "=s"(_n) :: "s90", "s91", "scc"); \
        const unsigned _o = ((unsigned)(chunk) * 64u + (unsigned)(lane)) * 128u; \
        if ((unsigned)(chunk) * 8192u < _n && _n < 262144u) touch((sink), _o < _n - 4u ? _o : _n - 4u, _p); } while (0)
#define K1_SYM "_Z7k1_proj6K1Args", "_Z7k2_attnPhPKtPt"
#define K2_SYM "_Z7k2_attnPhPKtPt", "text_end_mark"
#define CODE_TOUCH2(S, sink, chunk, lane) CODE_TOUCH(S, sink, chunk, lane)

constexpr int HTB = 128 * 64 * 2;
constexpr int K1_X = 8 * HTB;
constexpr int K1_LDS = K1_X + 16384 + 2048;

DI int lds_byte(int r, int c) { const int st = (r >> 4) * 2 + (c >> 5), rr = r & 15, cc = c & 31, ob = rr * 64 + cc * 2; return st * 1024 + (ob ^ (((ob >> 9) & 1) << 5)); }
DI void stage_rc(int b, int& R, int& C) { const int st = b / 1024, sb = b % 1024, swz = sb ^ (((sb >> 9) & 1) << 5); R = (st >> 1) * 16 + swz / 64; C = (st & 1) * 32 + (swz % 64) / 2; }

struct K1Args {
    const bf16_t *x, *ak, *av, *shift_k, *ssm_k, *ssm_d, *ckv, *wq, *wuk, *wuv, *gate_w, *wo;
    unsigned char* ws;
};

#define SA(b, h) (((b) * 2 + (h)) * HTB)
#define SB(b, h) ((4 + (b) * 2 + (h)) * HTB)
constexpr int ring_slot(int u) {
    switch (u & 7) { case 5: return SB(0, 0); case 6: return SA(0, 0); case 7: return SB(0, 1); case 0: return SA(0, 1);
                     case 1: return SB(1, 0); case 2: return SA(1, 0); case 3: return SB(1, 1); default: return SA(1, 1); }
}
struct G1 {
    ldsp lds; unsigned ldsw  , voffA_kv, voffA_q, voffB, sv; int aoff, boff0, boff1;
    const char *A_kv, *A_q, *B_kv, *B_q, *sbase; unsigned sstep, sdst, sdstep; int sadd;
};
DI int lane_id() { int l; asm volatile("v_mbcnt_lo_u32_b32 %0, -1, 0\n\tv_mbcnt_hi_u32_b32 %0, -1, %0" : "=v"(l)); return l; }
template <int OFF> DI void dma16(unsigned lbase, unsigned voff, const char* g) {
    asm volatile("s_add_i32 m0, %0, %c3\n\ts_nop 0\n\tglobal_load_lds_dwordx4 %1, %2" :: "s"(lbase), "v"(voff), "s"(g), "i"(OFF) : "memory", "scc");
}
template <int BUF, int STRIDE, int H> DI void stage_a(const G1& g, const char* base, unsigned voff) {
    dma16<BUF>(g.ldsw, voff, base + H * 128 * STRIDE); dma16<BUF + 8192>(g.ldsw, voff, base + (H * 128 + 64) * STRIDE);
}
template <int BUF, int H> DI void stage_b(const G1& g, const char* base) {
    dma16<BUF>(g.ldsw, g.voffB, base + H * 256); dma16<BUF + 8192>(g.ldsw, g.voffB, base + H * 256 + 32 * NW_ * 2);
}
template <int U> DI void stage_unit(const G1& g) {
    if constexpr (U < 21) {
        constexpr int t = U / 3, r = U % 3;
        if constexpr (r == 0) stage_b<ring_slot(U), 0>(g, g.B_q + (size_t)t * (64 * NW_ * 2));
        else stage_a<ring_slot(U), DM * 2, r - 1>(g, g.A_q + t * 128, g.voffA_q);
    } else {
        constexpr int t = (U - 21) >> 2, r = (U - 21) & 3;
        if constexpr ((r & 1) == 0) stage_b<ring_slot(U), (r >> 1)>(g, g.B_kv + (size_t)t * (64 * NW_ * 2));
        else stage_a<ring_slot(U), DC * 2, (r >> 1)>(g, g.A_kv + t * 128, g.voffA_kv);
    }
}
DI void side(const G1& g, int t) {
    const int tt = t + g.sadd > 15 ? 15 : t + g.sadd;
    asm volatile("s_mov_b32 m0, %0\n\ts_nop 0\n\tglobal_load_lds_dword %1, %2" :: "s"(g.sdst + (unsigned)t * g.sdstep), "v"(g.sv), "s"(g.sbase + rfl((unsigned)tt * g.sstep)) : "memory");
}
DI void lda(bf16x8 (&dst)[4][2], const G1& g, int buf) {
#pragma unroll
    for (int m = 0; m < 4; ++m)
#pragma unroll
        for (int k = 0; k < 2; ++k) dst[m][k] = ldsrd16(g.lds + buf + g.aoff + m * 2048 + k * 1024);
}
DI void ldb(bf16x8 (&dst)[2][2], const G1& g, int buf) {
#pragma unroll
    for (int n = 0; n < 2; ++n)
#pragma unroll
        for (int k = 0; k < 2; ++k) { ldscp p = g.lds + buf + (n ? g.boff1 : g.boff0) + k * 8192; dst[n][k] = cat8(trread(p), trread(p + 256)); }
}
template <bool VT> DI void mma(f32x4 (&acc)[4][2], const bf16x8 (&At)[4][2], const bf16x8 (&Bt)[2][2]) {
    __builtin_amdgcn_s_setprio(1);
#pragma unroll
    for (int m = 0; m < 4; ++m)
#pragma unroll
        for (int n = 0; n < 2; ++n)
#pragma unroll
            for (int k = 0; k < 2; ++k) acc[m][n] = VT ? MFMA16(At[m][k], Bt[n][k], acc[m][n]) : MFMA16(Bt[n][k], At[m][k], acc[m][n]);
    __builtin_amdgcn_s_setprio(0);
}
#define WAIT_V(n) asm volatile("s_waitcnt vmcnt(" #n ")" ::: "memory")
#define WAIT_L(n) asm volatile("s_waitcnt lgkmcnt(" #n ")" ::: "memory")
#define BAR __builtin_amdgcn_s_barrier()
DI void zero8(f32x4 (&a)[4][2]) {
#pragma unroll
    for (int m = 0; m < 4; ++m) { a[m][0] = (f32x4){0.f, 0.f, 0.f, 0.f}; a[m][1] = (f32x4){0.f, 0.f, 0.f, 0.f}; }
}

DI void pack_kq(const f32x4 (&acc)[4][2], u32x4* pk, float scale) {
#pragma unroll
    for (int m = 0; m < 4; ++m) {
        const f32x4 a0 = acc[m][0] * scale, a1 = acc[m][1] * scale;
        const unsigned w0 = cvtpk(a0[0], a0[1]), w1 = cvtpk(a0[2], a0[3]), x0 = cvtpk(a1[0], a1[1]), x1 = cvtpk(a1[2], a1[3]);
        auto r0 = __builtin_amdgcn_permlane16_swap(w0, x0, false, false);
        auto r1 = __builtin_amdgcn_permlane16_swap(w1, x1, false, false);
        pk[m] = (u32x4){r0[0], r1[0], r0[1], r1[1]};
    }
}
DI unsigned kq_off(int pm, int wr, int wc, int fr, int fq) {
    return (unsigned)((pm * 8 + wr * 2) * 16384 + fr * 16 + (2 * wc + (fq & 1)) * 1024 + (fq >> 1) * 512);
}
DI void pack_v(const f32x4 (&acc)[4][2], u32x4* pk) {
#pragma unroll
    for (int n = 0; n < 2; ++n)
#pragma unroll
        for (int mm = 0; mm < 2; ++mm) {
            const f32x4 a0 = acc[2 * mm][n], a1 = acc[2 * mm + 1][n];
            const unsigned w0 = cvtpk(a0[0], a0[1]), w1 = cvtpk(a0[2], a0[3]), x0 = cvtpk(a1[0], a1[1]), x1 = cvtpk(a1[2], a1[3]);
            auto r0 = __builtin_amdgcn_permlane32_swap(w0, x0, false, false);
            auto r1 = __builtin_amdgcn_permlane32_swap(w1, x1, false, false);
            pk[2 * mm + n] = (u32x4){r0[0], r1[0], r0[1], r1[1]};
        }
}
constexpr int ST_AUX = 16;
struct Out1 { u32x4 pk[16]; __amdgpu_buffer_rsrc_t rs; unsigned voff; int sbj, shi; };
template <int I> DI void st_piece(const Out1& o) {
    __builtin_amdgcn_raw_buffer_store_b128(o.pk[I], o.rs, o.voff + (I & 1) * 256, (I >> 3) * 65536 + ((I >> 2) & 1) * o.sbj + ((I >> 1) & 1) * o.shi, ST_AUX);
}
template <int A, int B> DI void st_range(const Out1& o) { if constexpr (A < B) { st_piece<A>(o); st_range<A + 1, B>(o); } }

template <int N, class W> DI void qphase(const G1& g, f32x4 (&a0)[4][2], f32x4 (&a1)[4][2], bf16x8 (&At)[4][2], bf16x8 (&B0)[2][2], W& warm) {
    constexpr int t = N >> 1;
    if constexpr ((N & 1) == 0) {
        ldb(B0, g, ring_slot(3 * t)); SBAR(); lda(At, g, ring_slot(3 * t + 1));
        if constexpr (t < 5) warm(t);
        if constexpr (N >= 2) stage_unit<3 * t + 6>(g);
        if constexpr (N == 0) WAIT_V(10); else WAIT_V(8);
        WAIT_L(8); BAR; WAIT_L(0); mma<false>(a0, At, B0); BAR; SBAR();
    } else {
        lda(At, g, ring_slot(3 * t + 2));
        if constexpr (t >= 1) stage_unit<3 * t + 7>(g);
        stage_unit<3 * t + 8>(g);
        WAIT_V(8);
        if constexpr (N == 13) WAIT_L(0);
        BAR; WAIT_L(0); mma<false>(a1, At, B0); BAR; SBAR();
    }
}
template <class W, int... Ns> DI void qphases(const G1& g, f32x4 (&a0)[4][2], f32x4 (&a1)[4][2], bf16x8 (&At)[4][2], bf16x8 (&B0)[2][2], W& warm, std::integer_sequence<int, Ns...>) {
    (qphase<Ns>(g, a0, a1, At, B0, warm), ...);
}
template <int... Us> DI void stage_units(const G1& g, std::integer_sequence<int, Us...>) { (stage_unit<Us>(g), ...); }

template <bool VT, int POS, class W>
DI void mainpair(const G1& g, int t, f32x4 (&acc)[2][2][4][2], bf16x8 (&At)[4][2], bf16x8 (&B0)[2][2], bf16x8 (&B1)[2][2], W& lastside) {
    const char* a1 = g.A_kv + (t + 1) * 128; const char* a2 = a1 + 128; const char* a3 = a1 + 256;
    const char* b2 = g.B_kv + (size_t)(t + 2) * (64 * NW_ * 2); const char* b3 = b2 + 64 * NW_ * 2;
    ldb(B0, g, SB(0, 0)); SBAR(); lda(At, g, SA(0, 0));
    if constexpr (POS != 0) stage_a<SA(1, 1), DC * 2, 1>(g, a1, g.voffA_kv); else WAIT_V(18);
    WAIT_L(8); BAR; WAIT_L(0); mma<VT>(acc[0][0], At, B0); BAR; SBAR();
    ldb(B1, g, SB(0, 1));
    if constexpr (POS == 2) lastside(0); else side(g, t);
    if constexpr (POS != 2) stage_b<SB(0, 0), 0>(g, b2);
    if constexpr (POS == 0) WAIT_V(19);
    BAR; WAIT_L(0); mma<VT>(acc[0][1], At, B1); BAR;
    lda(At, g, SA(0, 1));
    if constexpr (POS != 2) stage_a<SA(0, 0), DC * 2, 0>(g, a2, g.voffA_kv);
    BAR; WAIT_L(0); mma<VT>(acc[1][0], At, B0); BAR; SBAR();
    if constexpr (POS != 2) stage_b<SB(0, 1), 1>(g, b2);
    if constexpr (POS == 0) WAIT_V(15); else if constexpr (POS == 1) WAIT_V(7); else WAIT_V(1);
    BAR; mma<VT>(acc[1][1], At, B1); BAR;
    ldb(B0, g, SB(1, 0)); SBAR(); lda(At, g, SA(1, 0));
    if constexpr (POS != 2) stage_a<SA(0, 1), DC * 2, 1>(g, a2, g.voffA_kv);
    WAIT_L(8); BAR; WAIT_L(0); mma<VT>(acc[0][0], At, B0); BAR; SBAR();
    ldb(B1, g, SB(1, 1));
    if constexpr (POS == 2) lastside(1); else side(g, t + 1);
    if constexpr (POS != 2) stage_b<SB(1, 0), 0>(g, b3);
    BAR; WAIT_L(0); mma<VT>(acc[0][1], At, B1); BAR;
    lda(At, g, SA(1, 1));
    if constexpr (POS != 2) stage_a<SA(1, 0), DC * 2, 0>(g, a3, g.voffA_kv);
    BAR; WAIT_L(0); mma<VT>(acc[1][0], At, B0); BAR; SBAR();
    if constexpr (POS != 2) { stage_b<SB(1, 1), 1>(g, b3); WAIT_V(7); }
    BAR; mma<VT>(acc[1][1], At, B1); BAR;
}

constexpr int CV_RAW = SA(0, 0);
constexpr int K1_WO = SA(0, 1);
constexpr int CV_HSTR = 4256;
constexpr int CV_H = 2 * HTB;
constexpr int CV_USTR = 4608;
constexpr int CV_U = CV_H + 8 * CV_HSTR;
constexpr int CV_R = CV_U + 2 * CV_USTR;
static_assert(CV_R + 65536 <= K1_LDS && CV_R <= K1_X, "lds");

DI const char* wo_block(const K1Args& P, int e) { const int eb = e < 224 ? e : 0, kb = eb / 7, cn = eb - kb * 7; return (const char*)P.wo + ((size_t)kb * 128 * DM + cn * 64) * 2; }
DI void tail_stage(const G1& g, const K1Args& P, int c0, int e, int wid, int lane) {
    dma16<CV_RAW>(g.ldsw, (unsigned)lane * 16u, (const char*)P.ssm_k + (size_t)c0 * (SEQ * 2) + wid * 1024);
    const char* wg = wo_block(P, e);
    const unsigned vo = (unsigned)((wid * 8 + (lane >> 3)) * DM + (lane & 7) * 8) * 2u;
    dma16<K1_WO>(g.ldsw, vo, wg); dma16<K1_WO + 8192>(g.ldsw, vo, wg + 64 * DM * 2);
}
DI void wo_image(ldsp lds, const K1Args& P, int e, int wid, int lane) {
    const int fr = lane & 15, fq = lane >> 4, kb = e / 7, cn = e - kb * 7;
    if (e < 224) {
        ldscp wp = lds + K1_WO + (wid * 16 + 4 * (fq >> 1) + (fr >> 2)) * 128 + (fr & 1) * 32 + (fq & 1) * 16 + (fr & 2) * 4;
        unsigned char* op = P.ws + WS_WI + (size_t)((kb >> 1) * 7 + cn) * 32768 + ((kb & 1) * 8 + wid) * 2048 + lane * 16;
#pragma unroll
        for (int cb = 0; cb < 2; ++cb) *(bf16x8*)(op + cb * 1024) = cat8(trread(wp + cb * 64), trread(wp + cb * 64 + 1024));
    }
}
DI void conv_copies(ldsp q, const u32x4 d, const u32x2 e) {
    const unsigned a0 = __builtin_amdgcn_alignbit(d[1], d[0], 16), a1 = __builtin_amdgcn_alignbit(d[2], d[1], 16), a2 = __builtin_amdgcn_alignbit(d[3], d[2], 16),
                   a3 = __builtin_amdgcn_alignbit(e[0], d[3], 16), a4 = __builtin_amdgcn_alignbit(e[1], e[0], 16);
    *(LAS u32x4*)(q) = (u32x4){a0, a1, a2, a3};
    *(LAS u32x4*)(q + CV_HSTR) = (u32x4){d[1], d[2], d[3], e[0]};
    *(LAS u32x4*)(q + 2 * CV_HSTR) = (u32x4){a1, a2, a3, a4};
    *(LAS u32x4*)(q + 3 * CV_HSTR) = (u32x4){d[2], d[3], e[0], e[1]};
}
DI void conv_frags(bf16x8 (&A)[8], bf16x8 (&B)[8], ldscp hp, ldscp up) {
#pragma unroll
    for (int v = 0; v < 8; ++v) {
        B[v] = ldsrd16(up + (7 - v) * 512);
        const s16x4 lo = *(const LAS s16x4*)(hp + v * 512), hi = *(const LAS s16x4*)(hp + v * 512 + 8);
        A[v] = cat8(lo, hi);
    }
}
DI void conv_mma(f32x4 (&acc)[8], const bf16x8 (&A)[8], const bf16x8 (&B)[8]) {
#pragma unroll
    for (int J = 0; J < 8; ++J)
#pragma unroll
        for (int v = 0; v <= J; ++v) acc[J] = MFMA16(A[v], B[J - v], acc[J]);
}
DI void conv_part(ldsp lds, const K1Args& P, int c0, const u32x4 sw, const unsigned dw, int wid, int lane, const Out1& o) {
    const int tid = wid * 64 + lane, fr = lane & 15, fq = lane >> 4;
    {
        const int cq = tid & 255;
        const u32x4 d = *(const LAS u32x4*)(lds + CV_RAW + tid * 16);
        u32x2 e = *(const LAS u32x2*)(lds + CV_RAW + tid * 16 + 16);
        const u32x4 z4 = {0u, 0u, 0u, 0u}; const u32x2 z2 = {0u, 0u};
        if (cq == 255) e = z2;
        ldsp q = lds + CV_H + (tid >> 8) * 4 * CV_HSTR + (cq + 2) * 16;
        conv_copies(q, d, e);
        if (cq == 0) { conv_copies(q - 16, z4, (u32x2){d[0], d[1]}); conv_copies(q - 32, z4, z2); }
        if (cq == 255) { conv_copies(q + 16, z4, z2); conv_copies(q + 32, z4, z2); }
    }
    SBAR(); st_range<4, 6>(o); SBAR();
    {
        ldscp kp = lds + K1_X + tid * 16;
        u32x4 a = *(const LAS u32x4*)(kp - 16);
        const u32x4 b = *(const LAS u32x4*)(kp), vr = *(const LAS u32x4*)(kp + 8192);
        if (tid == 0) a = (u32x4){0u, 0u, 0u, 0u};
        const unsigned kr[7] = {a[1], a[2], a[3], b[0], b[1], b[2], b[3]};
#pragma unroll
        for (int c = 0; c < 2; ++c) {
            const float w0 = bf_lo(sw[2 * c]), w1 = bf_hi(sw[2 * c]), w2 = bf_lo(sw[2 * c + 1]), w3 = bf_hi(sw[2 * c + 1]);
            float kc[7], kv[4];
#pragma unroll
            for (int i = 0; i < 7; ++i) kc[i] = c ? bf_hi(kr[i]) : bf_lo(kr[i]);
#pragma unroll
            for (int i = 0; i < 4; ++i) {
                float s = rbf(kc[i] * w3);
                s = rbf(s + rbf(kc[i + 1] * w2));
                s = rbf(s + rbf(kc[i + 2] * w1));
                s = rbf(s + rbf(kc[i + 3] * w0));
                kv[i] = s * (c ? bf_hi(vr[i]) : bf_lo(vr[i]));
            }
            u32x2 wv = {cvtpk(kv[3], kv[2]), cvtpk(kv[1], kv[0])};
            *(LAS u32x2*)(lds + CV_U + c * CV_USTR + (2044 - 4 * tid) * 2) = wv;
            SBAR(); if (c == 0) st_range<6, 8>(o); else st_range<8, 10>(o); SBAR();
        }
        if (tid < 120) { const int c = tid / 60, j = tid - c * 60; *(LAS u32x2*)(lds + CV_U + c * CV_USTR + 4096 + j * 8) = (u32x2){0u, 0u}; }
    }
    __syncthreads();
    TS(P.ws, 0, 5);
    const int ch = wid >> 2, qw = wid & 3;
    {
        const int y1 = 24 + fr - 8 * fq;
        ldscp hb = lds + CV_H + ch * 4 * CV_HSTR + (y1 & 3) * CV_HSTR + (y1 & ~3) * 2 + qw * 128;
        ldscp ub = lds + CV_U + ch * CV_USTR + (2056 - 256 * 7 - 16 * fr - 8 * fq) * 2 + qw * 128;
        f32x4 acc[8];
#pragma unroll
        for (int i = 0; i < 8; ++i) acc[i] = (f32x4){0.f, 0.f, 0.f, 0.f};
        bf16x8 A0[8], B0[8], A1[8], B1[8];
        conv_frags(A0, B0, hb, ub); SBAR();
        conv_frags(A1, B1, hb + 64, ub + 64); SBAR();
        st_range<10, 12>(o); SBAR();
        conv_mma(acc, A0, B0); SBAR();
        st_range<12, 14>(o); SBAR();
        conv_mma(acc, A1, B1); SBAR();
        st_range<14, 16>(o); SBAR();
#pragma unroll
        for (int i = 0; i < 8; ++i) *(LAS f32x4*)(lds + CV_R + wid * 8192 + i * 1024 + lane * 16) = acc[i];
    }
    __syncthreads();
    TS(P.ws, 0, 6);
    {
        const float dd = ch ? bf_hi(dw) : bf_lo(dw);
#pragma unroll
        for (int i = 0; i < 2; ++i) {
            ldscp rp = lds + CV_R + ch * 32768 + (2 * qw + i) * 1024 + lane * 16;
            const f32x4 y = ((*(const LAS f32x4*)rp + *(const LAS f32x4*)(rp + 8192)) + *(const LAS f32x4*)(rp + 16384)) + *(const LAS f32x4*)(rp + 24576);
            const int t = 256 * (2 * qw + i) + 16 * fr + 4 * fq;
            const u32x2 uw = *(const LAS u32x2*)(lds + CV_U + ch * CV_USTR + (2044 - t) * 2);
            const float kv[4] = {bf_hi(uw[1]), bf_lo(uw[1]), bf_hi(uw[0]), bf_lo(uw[0])};
            float r[4];
#pragma unroll
            for (int e = 0; e < 4; ++e) r[e] = rbf(y[e]) + rbf(kv[e] * dd);
            u32x2 w = {cvtpk(r[0], r[1]), cvtpk(r[2], r[3])};
            *(u32x2*)(P.ws + WS_Y2 + ((size_t)(c0 + ch) * SEQ + t) * 2) = w;
        }
    }
}

DI void gate_part(const K1Args& P, int row0, int wid, int lane) {
    const int lc = lane < 56 ? lane : 0;
    u32x4 gw = *(const u32x4*)(P.gate_w + lc * 8);
    float* gp = (float*)(P.ws + WS_G);
    u32x4 xv[8];
#pragma unroll
    for (int r = 0; r < 8; ++r) xv[r] = *(const u32x4*)(P.x + (size_t)(row0 + wid * 8 + r) * DM + lc * 8);
    float mine = 0.f;
#pragma unroll
    for (int r = 0; r < 8; ++r) {
        float s = 0.f;
#pragma unroll
        for (int j = 0; j < 4; ++j) { s += rbf(bf_lo(xv[r][j]) * bf_lo(gw[j])); s += rbf(bf_hi(xv[r][j]) * bf_hi(gw[j])); }
        if (lane >= 56) s = 0.f;
#pragma unroll
        for (int o = 32; o >= 1; o >>= 1) s += __shfl_xor(s, o);
        if (lane == r) mine = s;
    }
    const float z = rbf(mine);
    const float g = rbf(1.0f / (1.0f + __expf(-z)));
    if (lane < 8) gp[row0 + wid * 8 + lane] = g;
}

template <bool VT>
DI void big_tile(const G1& g, const K1Args& P, int pm, int head, int c0, int e, int wid, bf16x8 (&At)[4][2], bf16x8 (&B0)[2][2], Out1& o) {
    f32x4 acc[2][2][4][2];
    zero8(acc[0][0]); zero8(acc[0][1]); zero8(acc[1][0]); zero8(acc[1][1]);
    bf16x8 B1[2][2];
    auto lastside = [&](int s) {
        if (wid < 4) side(g, 14 + s);
        else if (s == 0) {
            const unsigned l = (unsigned)lane_id();
            if (wid == 4) touch(g.sdst, l * 128u, (const char*)P.ssm_k + (size_t)c0 * (SEQ * 2));
            else touch(g.sdst, l * (DM * 2), wo_block(P, e) + (wid & 1) * (64 * DM * 2));
        }
    };
    mainpair<VT, 0>(g, 0, acc, At, B0, B1, lastside);
    for (int t = 2; t < 14; t += 2) mainpair<VT, 1>(g, t, acc, At, B0, B1, lastside);
    mainpair<VT, 2>(g, 14, acc, At, B0, B1, lastside);
    TS(P.ws, 0, 3);
    const int lane = lane_id();
    tail_stage(g, P, c0, e, wid, lane);
    if (wid == 7) CODE_TOUCH2(K2_SYM, g.sdst, e & 15, lane);
    SBAR();
    const int wr = wid >> 2, wc = wid & 3, fr = lane & 15, fq = lane >> 4;
    if (VT) {
        o.voff = (unsigned)((pm * 4 + wr) * 32768 + wc * 4096 + (fq >> 1) * 1024 + (fq & 1) * 512 + fr * 16);
        pack_v(acc[0][0], o.pk); pack_v(acc[0][1], o.pk + 4); pack_v(acc[1][0], o.pk + 8); pack_v(acc[1][1], o.pk + 12);
    } else {
        o.voff = kq_off(pm, wr, wc, fr, fq);
        pack_kq(acc[0][0], o.pk, 1.0f); pack_kq(acc[0][1], o.pk + 4, 1.0f); pack_kq(acc[1][0], o.pk + 8, 1.0f); pack_kq(acc[1][1], o.pk + 12, 1.0f);
    }
}

__global__ __launch_bounds__(512, 2) void k1_proj(K1Args P) {
    extern __shared__ __attribute__((aligned(16))) unsigned char shm[];
    ldsp lds = (ldsp)shm;
    const int b = blockIdx.x, xcd = b & 7, j = b >> 3, li = j & 15, pm = li & 7, head = 2 * xcd + (li >> 3), vt = j >> 4;
    const int tid = threadIdx.x, wid = rfl(tid >> 6), lane = tid & 63, wr = wid >> 2, wc = wid & 3, fr = lane & 15, fq = lane >> 4;
    const int c0 = (xcd * 28 + (j < 28 ? j : 0)) * 2, e = xcd * 32 + j;
    TS(P.ws, 0, 0);
    if (wid == 7) CODE_TOUCH2(K1_SYM, (unsigned)(uintptr_t)lds + K1_X + 16384 + 7 * 256, j, lane);
    u32x4 sw = *(const CAS u32x4*)(uintptr_t)(P.shift_k + c0 * 4); unsigned dw = *(const CAS unsigned*)(uintptr_t)(P.ssm_d + c0);
    G1 g;
    const unsigned lds0 = (unsigned)(uintptr_t)lds;
    g.lds = lds; g.ldsw = lds0 + (unsigned)wid * 1024u;
    { int R, C; stage_rc(tid * 16, R, C); g.voffA_kv = (unsigned)(R * DC + C) * 2u; g.voffA_q = (unsigned)(R * DM + C) * 2u; }
    { const int kg = wid >> 1, row = (lane >> 2) & 7, ch = (lane & 3) ^ (2 * (kg & 1)), n = 32 * (2 * (wid & 1) + (lane >> 5)) + 8 * ch;
      g.voffB = (unsigned)((8 * kg + row) * NW_ + n) * 2u; }
    g.aoff = lds_byte(wr * 64 + fr, fq * 8);
    g.boff0 = fq * 2048 + wc * 512 + (fr >> 2) * 64 + (32 * (fq & 1)) + 8 * (fr & 3);
    g.boff1 = g.boff0 ^ 32;
    g.A_kv = (const char*)P.ckv + (size_t)pm * 256 * DC * 2;
    g.A_q = (const char*)P.x + (size_t)pm * 256 * DM * 2;
    g.B_kv = (const char*)(vt ? P.wuv : P.wuk) + head * 512;
    g.B_q = (const char*)P.wq + head * 512 + vt * 256;
    if (wid < 4) {
        g.sbase = (const char*)(wid < 2 ? P.ak : P.av) + (wid & 1) * (64 * DM * 2) + c0 * 2; g.sstep = 128 * DM * 2; g.sv = (unsigned)lane * (DM * 2);
        g.sadd = 0; g.sdst = lds0 + K1_X + (wid >> 1) * 8192 + (wid & 1) * 256; g.sdstep = 512;
    } else {
        if (wid == 4) { g.sbase = g.A_kv + (size_t)((head & 1) * 2 + vt) * 64 * (DC * 2); g.sstep = 128; g.sv = (unsigned)lane * (DC * 2); }
        else if (wid == 5) { g.sbase = g.B_kv + (size_t)pm * 8 * (NW_ * 2); g.sstep = 64 * NW_ * 2; g.sv = (unsigned)((lane >> 2) & 7) * (NW_ * 2) + (lane & 3) * 128; }
        else { g.sbase = (const char*)(wid == 6 ? P.ak : P.av) + (size_t)j * 4 * (DM * 2) + xcd * 112; g.sstep = 128 * DM * 2; g.sv = (unsigned)(lane & 3) * (DM * 2) + ((lane >> 2) & 1) * 108; }
        g.sadd = 4; g.sdst = lds0 + K1_X + 16384 + wid * 256; g.sdstep = 0;
    }
    PIN(g.voffA_kv); PIN(g.voffB); PIN(g.sv); PIN(g.aoff); PIN(g.boff0); PIN(g.boff1);
    stage_units(g, std::make_integer_sequence<int, 8>{});
    WAIT_V(12);
    auto warm = [&](int slot) {
        const unsigned sink = lds0 + K1_X + 16384 + wid * 256;
        if (slot > 0) { if (wid >= 4) side(g, slot - 5); }
        else if (wid < 3) touch(sink, (unsigned)lane * (DM * 2), g.A_q + (size_t)((head & 1) * 2 + vt) * 64 * (DM * 2) + (4 + wid) * 128);
        else if (wid == 3) touch(sink, (unsigned)((lane < 48 ? lane : 0) >> 1) * (NW_ * 2) + (lane & 1) * 128, g.B_q + (size_t)(256 + pm * 24) * (NW_ * 2));
    };
    asm volatile("" : "+s"(sw), "+s"(dw));
    if (b == 0 && tid < N_AFLAGS + N_CFLAGS) {
#pragma unroll
        for (int q = 0; q < 4; ++q) *(u32x4*)((unsigned*)(P.ws + WS_FLAGS) + tid * FLAG_STRIDE + 4 * q) = (u32x4){0u, 0u, 0u, 0u};
    }
    bf16x8 At[4][2], B0[2][2];
    BAR;
    if (wr == 1) BAR;
    TS(P.ws, 0, 1);
    {
        f32x4 q0[4][2], q1[4][2];
        zero8(q0); zero8(q1);
        qphases(g, q0, q1, At, B0, warm, std::make_integer_sequence<int, 14>{});
        stage_unit<27>(g); stage_unit<28>(g); SBAR();
        Out1 o;
        o.rs = __builtin_amdgcn_make_buffer_rsrc(P.ws + WS_Q + (size_t)head * MB, (short)0, (int)MB, 0x00020000);
        o.voff = kq_off(pm, wr, wc, fr, fq) + vt * 8192; o.sbj = 65536; o.shi = 16384;
        pack_kq(q0, o.pk, 0.0625f * 1.4426950408889634f); pack_kq(q1, o.pk + 4, 0.0625f * 1.4426950408889634f);
        st_range<0, 8>(o);
        SBAR();
    }
    TS(P.ws, 0, 2);
    Out1 o;
    if (vt) big_tile<true>(g, P, pm, head, c0, e, wid, At, B0, o); else big_tile<false>(g, P, pm, head, c0, e, wid, At, B0, o);
    o.rs = __builtin_amdgcn_make_buffer_rsrc(P.ws + (vt ? WS_V : WS_K) + (size_t)head * MB, (short)0, (int)MB, 0x00020000);
    o.sbj = vt ? 16384 : 8192; o.shi = vt ? 2048 : 16384;
    SBAR(); st_range<0, 4>(o); SBAR();
    WAIT_V(4);
    if (wr == 0) BAR;
    BAR;
    TS(P.ws, 0, 4);
    const int lane2 = lane_id();
    wo_image(lds, P, e, wid, lane2);
    if (j < 28) conv_part(lds, P, c0, sw, dw, wid, lane2, o);
    else { st_range<4, 16>(o); gate_part(P, (xcd * 4 + (j - 28)) * 64, wid, lane2); }
    TS(P.ws, 0, 7);
}
#undef SA
#undef SB

constexpr int AT_K = 0, AT_V = 65536, AT_LDS = 163840;
constexpr float THR = 8.0f;
constexpr int PD = 6;
constexpr int NEB = 24;
template <int... Is> using iseq = std::integer_sequence<int, Is...>;
template <int N> using make_iseq = std::make_integer_sequence<int, N>;
#define ACLOB "a0","a1","a2","a3","a4","a5","a6","a7","a8","a9","a10","a11","a12","a13","a14","a15","a16","a17","a18","a19","a20","a21","a22","a23","a24","a25","a26","a27","a28","a29","a30","a31","a32","a33","a34","a35","a36","a37","a38","a39","a40","a41","a42","a43","a44","a45","a46","a47","a48","a49","a50","a51","a52","a53","a54","a55","a56","a57","a58","a59","a60","a61","a62","a63","a64","a65","a66","a67","a68","a69","a70","a71","a72","a73","a74","a75","a76","a77","a78","a79","a80","a81","a82","a83","a84","a85","a86","a87","a88","a89","a90","a91","a92","a93","a94","a95","a96","a97","a98","a99","a100","a101","a102","a103","a104","a105","a106","a107","a108","a109","a110","a111","a112","a113","a114","a115","a116","a117","a118","a119","a120","a121","a122","a123","a124","a125","a126","a127","a128","a129","a130","a131","a132","a133","a134","a135","a136","a137","a138","a139","a140","a141","a142","a143","a144","a145","a146","a147","a148","a149","a150","a151","a152","a153","a154","a155","a156","a157","a158","a159","a160","a161","a162","a163","a164","a165","a166","a167","a168","a169","a170","a171","a172","a173","a174","a175","a176","a177","a178","a179","a180","a181","a182","a183","a184","a185","a186","a187","a188","a189","a190","a191","a192","a193","a194","a195","a196","a197","a198","a199","a200","a201","a202","a203","a204","a205","a206","a207","a208","a209","a210","a211","a212","a213","a214","a215","a216","a217","a218","a219","a220","a221","a222","a223","a224","a225","a226","a227","a228","a229","a230","a231","a232","a233","a234","a235","a236","a237","a238","a239","a240","a241","a242","a243","a244","a245","a246","a247","a248","a249","a250","a251","a252","a253","a254","a255"

constexpr int op_slot(int s) { switch (s % 5) { case 0: return 0; case 1: return 32768; case 2: return 65536; case 3: return 131072; default: return 98304; } }
static_assert(AT_V + 32768 == 98304 && AT_LDS == 163840, "lds");
struct OPJ {
    const char *wbase, *xp, *yp, *gp, *pb;
    unsigned cv, lds0, xo, yo, po; ldscp wp;
    const unsigned* fl; unsigned* pub;
    unsigned look, done;
    u32x2 xw[4]; unsigned yw[16]; float g; unsigned char* ws;
};
struct AT {
    OPJ o;
    u32x2 pk;
    f32x2 ml2;
    const char *part, *mlp;
    bool isA, early;
    unsigned* flag;
    f32x16 s[2][2];
    f32x16 cm[2];
    u32x4 p[4];
    bf16x8 fr[8];
    float m, l, rs0, rs1, mx0, mx1, alpha, aone;
    bool pend, first, drain;
    ldscp kb;
    ldscp vcur;
    unsigned goff;
    unsigned m0k, m0v;
    const char *gk, *gv;
};

DI void dma_piece(unsigned m0v, unsigned goff, const char* g, const int off) {
    switch (off) {
    case 0: asm volatile("s_mov_b32 m0, %0\n\ts_nop 0\n\tglobal_load_lds_dwordx4 %1, %2" :: "s"(m0v), "v"(goff), "s"(g) : "memory"); break;
    case 1: asm volatile("s_mov_b32 m0, %0\n\ts_nop 0\n\tglobal_load_lds_dwordx4 %1, %2 offset:1024" :: "s"(m0v), "v"(goff), "s"(g) : "memory"); break;
    case 2: asm volatile("s_mov_b32 m0, %0\n\ts_nop 0\n\tglobal_load_lds_dwordx4 %1, %2 offset:2048" :: "s"(m0v), "v"(goff), "s"(g) : "memory"); break;
    default: asm volatile("s_mov_b32 m0, %0\n\ts_nop 0\n\tglobal_load_lds_dwordx4 %1, %2 offset:3072" :: "s"(m0v), "v"(goff), "s"(g) : "memory"); break;
    }
}
template <int I> DI void dma8(unsigned m0base, unsigned goff, const char* g) { dma_piece(m0base + (I >> 2) * 4096, goff, g + (I >> 2) * 4096, I & 3); }
DI void dma_tile(unsigned m0base, unsigned goff, const char* g) {
    dma8<0>(m0base, goff, g); dma8<1>(m0base, goff, g); dma8<2>(m0base, goff, g); dma8<3>(m0base, goff, g);
    dma8<4>(m0base, goff, g); dma8<5>(m0base, goff, g); dma8<6>(m0base, goff, g); dma8<7>(m0base, goff, g);
}
DI float ex2(float x) { return __builtin_amdgcn_exp2f(x); }
DI float MX3(float a, float b, float c) { float r; asm("v_max3_f32 %0, %1, %2, %3" : "=v"(r) : "v"(a), "v"(b), "v"(c)); return r; }
DI float MX2(float a, float b) { float r; asm("v_max_f32_e32 %0, %1, %2" : "=v"(r) : "v"(a), "v"(b)); return r; }
template <int D0> DI void qk_init(f32x16& s, const bf16x8& k, const f32x16& c) {
    asm volatile("v_mfma_f32_32x32x16_bf16 %0, %1, a[%c3:%c4], %2" : "=&v"(s) : "v"(k), "v"(c), "i"(128 + 4 * D0), "i"(131 + 4 * D0)); }
template <int D0> DI void qk_acc(f32x16& s, const bf16x8& k) {
    asm volatile("v_mfma_f32_32x32x16_bf16 %0, %1, a[%c2:%c3], %0" : "+v"(s) : "v"(k), "i"(128 + 4 * D0), "i"(131 + 4 * D0)); }
template <int DB> DI void pv_acc(const bf16x8& v, const u32x4& p) {
    asm volatile("v_mfma_f32_32x32x16_bf16 a[%c2:%c3], %0, %1, a[%c2:%c3]" :: "v"(v), "v"(p), "i"(16 * DB), "i"(16 * DB + 15)); }
DI void add_bcast(f32x16& x, float aone, float b) { asm volatile("s_nop 1\n\tv_mfma_f32_32x32x2_f32 %0, %1, %2, %0" : "+v"(x) : "v"(aone), "v"(b)); }
#define NOPS16() asm volatile("s_nop 7\n\ts_nop 7" ::: "memory")
#define NOPS2() asm volatile("s_nop 1" ::: "memory")
template <int I> DI void a_zero() { asm volatile("v_accvgpr_write_b32 a[%c0], 0" :: "i"(I)); }
template <int I> DI void a_scale(float f) { float t; asm volatile("v_accvgpr_read_b32 %0, a[%c2]\n\tv_mul_f32 %0, %1, %0\n\tv_accvgpr_write_b32 a[%c2], %0" : "=&v"(t) : "v"(f), "i"(I)); }
template <int I> DI void a_qmov() { asm volatile("v_accvgpr_mov_b32 a[%c0], a[%c1]" :: "i"(128 + I), "i"(192 + I)); }
template <int I> DI float a_read() { float t; asm volatile("v_accvgpr_read_b32 %0, a[%c1]" : "=v"(t) : "i"(I)); return t; }
template <int F> DI void q_load(unsigned voff, const char* g) {
    asm volatile("global_load_dwordx4 a[%c2:%c3], %0, %1 offset:%c4" :: "v"(voff), "s"(g + ((F & 15) >> 2) * 4096), "i"(128 + 4 * F), "i"(131 + 4 * F), "i"((F & 3) * 1024) : "memory"); }
template <int K> DI void pk_store(const AT& c, const u32x4 pk) {
    asm volatile("global_store_dwordx4 %0, %1, %2 offset:%c3 sc1\n\ts_nop 1" :: "v"(c.o.cv), "v"(pk), "s"(c.part + (K >> 2) * 4096), "i"((K & 3) * 1024) : "memory"); }
template <int K> DI void p_load(const AT& c) {
    asm volatile("global_load_dwordx4 a[%c2:%c3], %0, %1 offset:%c4 sc1" :: "v"(c.o.cv), "s"(c.part + (K >> 2) * 4096), "i"(192 + 4 * K), "i"(195 + 4 * K), "i"((K & 3) * 1024) : "memory"); }
DI void ml_load(AT& c) { asm volatile("global_load_dwordx2 %0, %1, %2 sc1" : "+v"(c.ml2) : "v"((c.o.cv & 496u) >> 1), "s"(c.mlp) : "memory"); }
template <int I> DI void op_wpiece(const OPJ& o) {
    constexpr int S = I >> 3, H = (I & 4) * 1024;
    dma_piece(o.lds0 + op_slot(S) + H, o.cv, o.wbase + S * 32768 + H, I & 3);
}
template <int DB> DI void pv_new(const bf16x8& v, const u32x4& p) {
    asm volatile("v_mfma_f32_32x32x16_bf16 a[%c2:%c3], %0, %1, 0" :: "v"(v), "v"(p), "i"(16 * DB), "i"(16 * DB + 15)); }
template <int... Is> DI void a_zeros(iseq<Is...>) { (a_zero<Is>(), ...); }
template <int... Is> DI void a_scales(float f, iseq<Is...>) { (a_scale<Is>(f), ...); }
template <int... Is> DI void a_qmovs(iseq<Is...>) { (a_qmov<Is>(), ...); }
template <int B, int... Is> DI void q_loads(unsigned voff, const char* g, iseq<Is...>) { (q_load<B + Is>(voff, g), ...); }
template <int... Is> DI void p_loads(const AT& c, iseq<Is...>) { (p_load<Is>(c), ...); }
template <int B, int... Is> DI void a_reads(float (&o)[16], iseq<Is...>) { ((o[Is] = a_read<B + Is>()), ...); }

template <int KS, int R> DI bf16x8 kfrag(const AT& c) { return ldsrd16(c.kb + KS * 32768 + ((R & 1) * 16 + (R >> 1)) * 1024); }
template <int R, bool DBM = false> DI bf16x8 vfrag(ldscp v) {
    return ldsrd16(v + (DBM ? (R >> 2) * 4096 + (R & 3) * 1024 : (R & 7) * 4096 + (R >> 3) * 1024)); }
template <int Q> DI void pk_quad(AT& c) {
    const float t0 = a_read<4 * Q>(), t1 = a_read<4 * Q + 1>(), t2 = a_read<4 * Q + 2>(), t3 = a_read<4 * Q + 3>();
    if constexpr (Q & 1) pk_store<(Q >> 1)>(c, (u32x4){c.pk[0], c.pk[1], cvtpk(t0, t1), cvtpk(t2, t3)});
    else { c.pk = (u32x2){cvtpk(t0, t1), cvtpk(t2, t3)}; PIN(c.pk); }
}
template <int X, int E> DI void exp_sum(AT& c) {
    const float e = ex2(c.s[X][E >> 4][E & 15]); c.s[X][E >> 4][E & 15] = e;
    if constexpr (E & 1) c.rs1 += e; else c.rs0 += e;
}
template <int X, int W> DI void pack1(AT& c) {
    c.p[W >> 2][W & 3] = cvtpk(c.s[X][W >> 3][(2 * W) & 15], c.s[X][W >> 3][(2 * W + 1) & 15]);
}
template <int X, int MODE, int N> DI void gapA(AT& c) {
    constexpr int d0 = N >> 1, kb = N & 1, Y = X ^ 1;
    if constexpr (d0 == 0) qk_init<0>(c.s[Y][kb], c.fr[N & 7], c.cm[(MODE & 4) ? kb : 0]);
    else qk_acc<d0>(c.s[Y][kb], c.fr[N & 7]);
    constexpr int R = N + PD;
    if constexpr (R < 32) c.fr[R & 7] = kfrag<Y, R>(c); else if constexpr (MODE & 1) c.fr[R & 7] = vfrag<R - 32, (MODE & 18) == 18>(c.vcur);
    if constexpr (MODE & 1) {
        if constexpr (N < 32 - NEB) { exp_sum<X, NEB + N>(c); PIN(c.rs0); PIN(c.rs1); }
        if constexpr (N >= 10 && N < 26) { pack1<X, N - 10>(c); PIN(c.p[(N - 10) >> 2]); }
    }
    if constexpr ((MODE & 4) && N == 2) asm volatile("" :: "v"(c.cm[0]), "v"(c.cm[1]));
    if constexpr ((MODE & 18) == 16) pk_quad<N>(c);
    if constexpr ((MODE & 18) == 18 && N >= 24) pk_quad<N - 24>(c);
    if constexpr ((MODE & 2) && (N & 1)) {
        if constexpr (N < 16) dma8<(N >> 1)>(c.m0k, c.goff, c.gk); else dma8<((N - 16) >> 1)>(c.m0v, c.goff, c.gv);
    }
    SBAR();
}
template <int X> DI void decide(AT& c) {
    constexpr int Y = X ^ 1;
    float mm = MX2(c.mx0, c.mx1);
    auto rr = __builtin_amdgcn_permlane32_swap(__float_as_uint(mm), __float_as_uint(mm), false, false);
    const float rm = MX2(__uint_as_float(rr[0]), __uint_as_float(rr[1]));
    if (__builtin_expect(c.first || __any(rm > THR), 0)) {
        const float dl = c.first ? rm : MX2(rm, 0.f);
        c.m += dl; c.alpha = ex2(-dl); c.pend = !c.first;
        const float nd = -dl;
        add_bcast(c.s[Y][0], c.aone, nd); add_bcast(c.s[Y][1], c.aone, nd); add_bcast(c.cm[0], c.aone, nd);
        asm volatile("s_nop 7\n\ts_nop 7\n\ts_nop 3" : "+v"(c.s[Y][0]), "+v"(c.s[Y][1]), "+v"(c.cm[0]));
    }
}
template <int X, int I> DI void maxstep(AT& c) {
    constexpr int Y = X ^ 1, h = I >> 2, r = 4 * (I & 3);
    if constexpr (I == 0) { c.mx0 = MX2(c.s[Y][0][0], c.s[Y][0][1]); c.mx1 = MX2(c.s[Y][0][2], c.s[Y][0][3]); }
    else { c.mx0 = MX3(c.mx0, c.s[Y][h][r], c.s[Y][h][r + 1]); c.mx1 = MX3(c.mx1, c.s[Y][h][r + 2], c.s[Y][h][r + 3]); }
}
template <int X, int MODE, int N> DI void gapB(AT& c) {
    constexpr bool DBM = (MODE & 18) == 18;
    constexpr int ks = DBM ? N & 3 : N >> 3, db = DBM ? N >> 2 : N & 7, Y = X ^ 1;
    if constexpr ((MODE & 16) && ks == 0) pv_new<db>(c.fr[N & 7], c.p[ks]); else pv_acc<db>(c.fr[N & 7], c.p[ks]);
    if constexpr (DBM && N < 24) pk_quad<N + 8>(c);
    if constexpr ((MODE & 32) && N == 10) {
        if (c.isA) {
            asm volatile("s_waitcnt vmcnt(0)" ::: "memory");
            c.early = rfl(__float_as_uint(a_read<192>())) != 0u;
            __builtin_amdgcn_fence(__ATOMIC_ACQUIRE, "wavefront");
            if (c.early) ml_load(c);
        }
    }
    if constexpr ((MODE & 32) && N > 10 && N < 27) { if (c.early) p_load<N - 11>(c); }
    if constexpr ((MODE & 48) == 48 && N == 16) { asm volatile("s_waitcnt vmcnt(0)" ::: "memory"); __hip_atomic_store(c.flag, 1u, RLX_AGENT); }
    if constexpr ((MODE & 48) == 32 && N == 28) asm volatile("global_load_dword a128, %0, %1 sc1" :: "v"(0u), "s"(c.flag) : "memory");
    if constexpr (MODE & 64) op_wpiece<N>(c.o);
    constexpr int R = N + PD;
    if constexpr (R < 32) c.fr[R & 7] = vfrag<R, DBM>(c.vcur);
    if constexpr (MODE & 1) {
        if constexpr (MODE & 2) {
            if constexpr (N == 28) { c.fr[0] = kfrag<X, 0>(c); c.fr[1] = kfrag<X, 1>(c); }
            if constexpr (N == 29) { c.fr[2] = kfrag<X, 2>(c); c.fr[3] = kfrag<X, 3>(c); }
            if constexpr (N == 30) { c.fr[4] = kfrag<X, 4>(c); }
            if constexpr (N == 31) { c.fr[5] = kfrag<X, 5>(c); }
        }
        if constexpr (N == 1) { asm volatile("s_nop 7" : "+v"(c.s[Y][0]), "+v"(c.s[Y][1])); }
        if constexpr (N >= 2 && N < 6) { maxstep<X, 2 * (N - 2)>(c); maxstep<X, 2 * (N - 2) + 1>(c); PIN(c.mx0); PIN(c.mx1); }
        if constexpr (N == 6) { c.l += c.rs0 + c.rs1; c.rs0 = 0.f; c.rs1 = 0.f; PIN(c.l); }
        if constexpr (N == 7) decide<X>(c);
        if constexpr (N >= 8) { exp_sum<Y, N - 8>(c); PIN(c.rs0); PIN(c.rs1); }
    }
    SBAR();
    if constexpr ((MODE & 1) && N == 27) {
        if constexpr (MODE & 2) {
            if (X == 0 && c.drain) asm volatile("s_waitcnt vmcnt(0)" ::: "memory");
            if constexpr (MODE & 16) asm volatile("s_waitcnt vmcnt(24) lgkmcnt(0)\n\ts_barrier" ::: "memory");
            else asm volatile("s_waitcnt vmcnt(8) lgkmcnt(0)\n\ts_barrier" ::: "memory");
        } else {
            if (!c.isA) asm volatile("s_waitcnt vmcnt(0)" ::: "memory");
            asm volatile("s_waitcnt lgkmcnt(0)\n\ts_barrier" ::: "memory");
        }
        SBAR();
    }
}
template <int X, int MODE, int... Ns> DI void phaseA(AT& c, iseq<Ns...>) { (gapA<X, MODE, Ns>(c), ...); }
template <int X, int MODE, int... Ns> DI void phaseB(AT& c, iseq<Ns...>) { (gapB<X, MODE, Ns>(c), ...); }
template <int X, int B, int... Es> DI void exps_from(AT& c, iseq<Es...>) { (exp_sum<X, B + Es>(c), ...); }
template <int X, int... Ws> DI void packs(AT& c, iseq<Ws...>) { (pack1<X, Ws>(c), ...); }
template <int... Is> DI void maxsteps1(AT& c, iseq<Is...>) { (maxstep<1, Is>(c), ...); }
DI void rescale_o(AT& c) {
    if (__builtin_expect(c.pend, 0)) {
        NOPS16();
        a_scales(c.alpha, make_iseq<128>{});
        NOPS2();
        c.l *= c.alpha; c.pend = false;
    }
}
DI float halfsum(float x) { auto rr = __builtin_amdgcn_permlane32_swap(__float_as_uint(x), __float_as_uint(x), false, false); return __uint_as_float(rr[0]) + __uint_as_float(rr[1]); }
DI void build_mask(AT& c, int mrel) {
    const float NEG = -__builtin_inff(), nm = -c.m;
#pragma unroll
    for (int r = 0; r < 16; ++r) { const int kk = (r & 3) + 8 * (r >> 2); c.cm[0][r] = kk > mrel ? NEG : nm; c.cm[1][r] = kk + 32 > mrel ? NEG : nm; }
    PIN(c.cm[0]); PIN(c.cm[1]); NOPS2();
}

struct ATW {
    const char *Kh, *Vh; ldsp lds; unsigned mk, mv; int n1, vs, lane, wid, r32, hi, qrel; bool isA;
};
DI int tile_of(const ATW& w, int g) { return w.isA ? w.n1 + g : (g < w.n1 ? g : g - w.n1); }
template <int X, bool DMA> DI void setup(AT& c, const ATW& w, int g) {
    c.vcur = w.lds + AT_V + w.vs * 32768 + w.lane * 16;
    if constexpr (DMA) {
        const int v2 = w.vs == 0 ? 2 : w.vs - 1, tn = tile_of(w, g + 2);
        c.gk = w.Kh + (size_t)tn * 32768; c.gv = w.Vh + (size_t)tn * 32768; c.m0k = w.mk + X * 32768; c.m0v = w.mv + v2 * 32768;
    }
}
template <bool LAST, bool seam> DI void pair(AT& c, ATW& w, int g0) {
    setup<0, true>(c, w, g0);
    c.first = seam;
    const float m_old = c.m;
    if constexpr (seam) { a_qmovs(make_iseq<64>{}); NOPS2(); }
    int qr = w.qrel;
    if constexpr (LAST) {
        if constexpr (seam) asm volatile("; last pair, two segments" : "+v"(qr), "+v"(c.s[0][0]), "+v"(c.s[0][1]), "+v"(c.m));
        else asm volatile("; last pair" : "+v"(qr), "+v"(c.s[0][0]), "+v"(c.s[0][1]), "+v"(c.m));
        build_mask(c, qr);
    }
    SBAR();
    phaseA<0, LAST ? 7 : 3>(c, make_iseq<32>{});
    phaseB<0, 3>(c, make_iseq<32>{});
    rescale_o(c);
    if (c.drain) { __hip_atomic_store(c.flag, 1u, RLX_AGENT); c.drain = false; }
    c.first = false;
    w.vs = w.vs == 2 ? 0 : w.vs + 1;
    setup<1, !LAST>(c, w, g0 + 1);
    if constexpr (LAST || seam) asm volatile("" : "+s"(c.part));
    if constexpr (LAST) {
        asm volatile("global_load_dword a192, %0, %1 sc1" :: "v"(0u), "s"(c.flag) : "memory");
        PIN(qr); build_mask(c, qr - 64);
    }
    if constexpr (seam) {
        const float lt = halfsum(c.l);
        __hip_atomic_store((unsigned long long*)c.mlp + w.r32, ((unsigned long long)__float_as_uint(lt) << 32) | __float_as_uint(m_old), RLX_AGENT);
        c.l = 0.f;
        asm volatile("; second segment" : "+v"(c.s[1][0]), "+v"(c.s[1][1]), "+v"(c.rs0), "+v"(c.rs1), "+v"(c.l));
        SBAR();
        phaseA<1, (LAST ? 5 : 3) | 16>(c, make_iseq<32>{});
        phaseB<1, (LAST ? 33 : 3) | 16>(c, make_iseq<32>{});
        if constexpr (!LAST) c.drain = true;
    } else {
        SBAR();
        phaseA<1, LAST ? 5 : 3>(c, make_iseq<32>{});
        phaseB<1, LAST ? 33 : 3>(c, make_iseq<32>{});
    }
    rescale_o(c);
    w.vs = w.vs == 2 ? 0 : w.vs + 1;
}

constexpr int op_blk(int mb) { return (mb + 8) % 14; }
template <int R> DI bf16x8 op_wfrag(const OPJ& o) { return ldsrd16(o.wp + op_slot(R >> 5) + (R & 31) * 1024); }
template <int I> DI void op_eload(OPJ& o) {
    if constexpr (I < 12) asm volatile("global_load_ushort %0, %1, %2" : "=v"(o.yw[I]) : "v"(o.yo), "s"(o.yp + ((I >> 2) * 128 + (I & 3)) * (SEQ * 2)) : "memory");
    else if constexpr (I < 16) asm volatile("global_load_ushort %0, %1, %2" : "=v"(o.yw[I]) : "v"(o.yo & 0x3ffffu), "s"(o.yp + (384 + (I & 3)) * (SEQ * 2)) : "memory");
    else if constexpr (I < 19) asm volatile("global_load_dwordx2 %0, %1, %2 offset:%c3" : "=v"(o.xw[I - 16]) : "v"(o.xo), "s"(o.xp), "i"((I - 16) * 256) : "memory");
    else if constexpr (I == 19) asm volatile("global_load_dwordx2 %0, %1, %2 offset:768" : "=v"(o.xw[3]) : "v"(o.xo - ((o.cv >> 2) & 128u)), "s"(o.xp) : "memory");
    else asm volatile("global_load_dword %0, %1, %2" : "=v"(o.g) : "v"((o.cv & 240u) >> 2), "s"(o.gp) : "memory");
}
constexpr int op_land(int st) { return st < 2 ? 224 + 16 * st : 96 + 16 * st; }
template <int I> DI void op_pload(const OPJ& o) {
    asm volatile("global_load_dwordx4 a[%c2:%c3], %0, %1 sc1" :: "v"(o.po), "s"(o.pb + (I & 3) * 458752 + (I >> 2) * 4096),
                 "i"(op_land(I >> 2) + 4 * (I & 3)), "i"(op_land(I >> 2) + 4 * (I & 3) + 3) : "memory"); }
template <int S> DI void op_take(OPJ& o) {
    if (o.done == S && ((unsigned)__ballot(o.look > (unsigned)S) & 0xffffu) == 0xffffu) {
        op_pload<4 * S>(o); op_pload<4 * S + 1>(o); op_pload<4 * S + 2>(o); op_pload<4 * S + 3>(o); o.done = S + 1; }
}
template <int... Ss> DI void op_takes(OPJ& o, iseq<Ss...>) { __builtin_amdgcn_fence(__ATOMIC_ACQUIRE, "wavefront"); (op_take<Ss>(o), ...); }
DI void op_look(const OPJ& o) { asm volatile("global_load_dword a208, %0, %1 sc1" :: "v"((o.cv >> 2) & 60u), "s"(o.fl) : "memory"); }
template <int N> DI void op_wait(unsigned n) {
    if (N == 0 || n >= N) asm volatile("s_waitcnt vmcnt(%c0)" :: "i"(4 * N) : "memory");
    else if constexpr (N > 0) op_wait<N - 1>(n);
}
DI void op_publish(const OPJ& o, unsigned n) { asm volatile("global_store_dword %0, %1, %2 sc1" :: "v"(0u), "v"(n), "s"(o.pub) : "memory"); }
template <int Q> DI void op_quad(AT& c) {
    constexpr int A = 16 * op_blk(Q >> 2) + 4 * (Q & 3);
    const float t0 = a_read<A>(), t1 = a_read<A + 1>(), t2 = a_read<A + 2>(), t3 = a_read<A + 3>();
    if constexpr (Q & 1) pk_store<(Q >> 1)>(c, (u32x4){c.pk[0], c.pk[1], cvtpk(t0, t1), cvtpk(t2, t3)});
    else { c.pk = (u32x2){cvtpk(t0, t1), cvtpk(t2, t3)}; PIN(c.pk); }
}
template <bool MERGE, int Q> DI void ctx_quad(float f1, float f2, u32x4 (&bf)[16]) {
    const float o0 = a_read<4 * Q>(), o1 = a_read<4 * Q + 1>(), o2 = a_read<4 * Q + 2>(), o3 = a_read<4 * Q + 3>();
    if constexpr (MERGE) {
        const unsigned p0 = __float_as_uint(a_read<192 + 2 * Q>()), p1 = __float_as_uint(a_read<193 + 2 * Q>());
        bf[Q >> 1][2 * (Q & 1)] = cvtpk(o0 * f1 + bf_lo(p0) * f2, o1 * f1 + bf_hi(p0) * f2);
        bf[Q >> 1][2 * (Q & 1) + 1] = cvtpk(o2 * f1 + bf_lo(p1) * f2, o3 * f1 + bf_hi(p1) * f2);
    } else { bf[Q >> 1][2 * (Q & 1)] = cvtpk(o0 * f1, o1 * f1); bf[Q >> 1][2 * (Q & 1) + 1] = cvtpk(o2 * f1, o3 * f1); }
    if constexpr (Q & 1) PIN(bf[Q >> 1]);
}
template <bool MERGE, int G> DI void op_step(AT& c, u32x4 (&bf)[16], float f1, float f2) {
    constexpr int S = G >> 5, N = G & 31;
    if constexpr (N < 2) pv_new<op_blk(2 * S + N)>(c.fr[G & 7], bf[0]); else pv_acc<op_blk(2 * S + (N & 1))>(c.fr[G & 7], bf[N >> 1]);
    if constexpr (N == 32 - PD && S < 4) {
        constexpr int CNT[4] = {45, 41, 45, 4};
        asm volatile("s_waitcnt vmcnt(%c0)\n\ts_barrier" :: "i"(CNT[S]) : "memory");
    }
    if constexpr (N == 32 - PD && S >= 3) {
        if constexpr (S > 3) asm volatile("s_waitcnt vmcnt(5)" ::: "memory");
        op_publish(c.o, S - 1);
    }
    if constexpr (G + PD < 224) c.fr[(G + PD) & 7] = op_wfrag<G + PD>(c.o);
    if constexpr (G < 8) op_wpiece<32 + G>(c.o);
    if constexpr (G >= 5 && G < 26) op_eload<G - 5>(c.o);
    if constexpr (G < 28) ctx_quad<MERGE, G + 4>(f1, f2, bf);
    if constexpr (G >= 58 && G < 66) op_wpiece<40 + G - 58>(c.o);
    if constexpr (G >= 90 && G < 98) op_wpiece<48 + G - 90>(c.o);
    if constexpr (S >= 1 && N >= 2 && N <= 23 && (N - 2) % 3 == 0) op_quad<8 * (S - 1) + (N - 2) / 3>(c);
    SBAR();
}
template <bool MERGE, int B, int... Is> DI void op_steps(AT& c, u32x4 (&bf)[16], float f1, float f2, iseq<Is...>) { (op_step<MERGE, B + Is>(c, bf, f1, f2), ...); }
template <bool MERGE> DI void op_first(AT& c, u32x4 (&bf)[16], float f1, float f2) {
    SBAR(); NOPS16(); SBAR();
    ctx_quad<MERGE, 0>(f1, f2, bf); ctx_quad<MERGE, 1>(f1, f2, bf); ctx_quad<MERGE, 2>(f1, f2, bf); ctx_quad<MERGE, 3>(f1, f2, bf);
    SBAR();
    asm volatile("s_waitcnt vmcnt(24)\n\ts_barrier" ::: "memory");
    c.fr[0] = op_wfrag<0>(c.o); c.fr[1] = op_wfrag<1>(c.o); c.fr[2] = op_wfrag<2>(c.o); c.fr[3] = op_wfrag<3>(c.o); c.fr[4] = op_wfrag<4>(c.o); c.fr[5] = op_wfrag<5>(c.o);
    SBAR();
    op_steps<MERGE, 0>(c, bf, f1, f2, make_iseq<32>{});
}
template <int... Is> DI void op_last(AT& c, iseq<Is...>) { (op_quad<48 + Is>(c), ...); }
template <int K> DI void op_add(f32x4 (&acc)[4], const u32x4 (&sel)[2]) {
    constexpr int S = 2 * (K & 3) + (K >> 4), A = op_land(S) + 4 * ((K >> 2) & 3);
    if constexpr (S < 7) asm volatile("v_mfma_f32_16x16x32_bf16 %0, %1, a[%c2:%c3], %0" : "+v"(acc[K & 3]) : "v"(sel[S & 1]), "i"(A), "i"(A + 3)); }
template <int... Ks> DI void op_adds(f32x4 (&acc)[4], const u32x4 (&sel)[2], iseq<Ks...>) { (op_add<Ks>(acc, sel), ...); }

__global__ __launch_bounds__(256, 1) void k2_attn(unsigned char* ws, const bf16_t* x, bf16_t* out) {
    extern __shared__ __attribute__((aligned(16))) unsigned char shm[];
    ldsp lds = (ldsp)shm;
    const int tid = threadIdx.x, wid = rfl(tid >> 6), lane = tid & 63, r32 = lane & 31, hi = lane >> 5;
    const int b = blockIdx.x, xcd = b & 7, j = b >> 3, head = 2 * xcd + (j >> 4), w16 = j & 15;
    const bool isA = w16 >= 8; const int i = w16 & 7, bq = 15 - i;
    const int seg = head * 8 + i;
    ATW w;
    w.Kh = (const char*)ws + WS_K + (size_t)head * MB; w.Vh = (const char*)ws + WS_V + (size_t)head * MB;
    const char* Qh = (const char*)ws + WS_Q + (size_t)head * MB;
    w.lds = lds; w.n1 = 15 - 2 * i; w.vs = 0; w.lane = lane; w.wid = wid; w.r32 = r32; w.hi = hi; w.isA = isA; w.qrel = wid * 32 + r32 - 4 * hi;
    AT c;
    const unsigned lds0 = (unsigned)(uintptr_t)lds;
    c.part = (const char*)ws + WS_PART + (size_t)(seg * 4 + wid) * 16384; c.mlp = (const char*)ws + WS_ML + (seg * 4 + wid) * 256;
    c.flag = (unsigned*)(ws + WS_FLAGS) + seg * FLAG_STRIDE + wid;
    c.isA = isA; c.early = false; c.ml2 = (f32x2){0.f, 0.f};
    OPJ& o = c.o;
    o.cv = (unsigned)lane * 16u;
    c.goff = (unsigned)wid * 8192u + (unsigned)lane * 16u;
    c.kb = lds + AT_K + lane * 16;
    w.mk = lds0 + AT_K + wid * 8192; w.mv = lds0 + AT_V + wid * 8192;
    TS(ws, 1, 0);
    asm volatile("s_nop 4" ::: ACLOB);
    dma_tile(w.mk, c.goff, w.Kh + (size_t)tile_of(w, 0) * 32768);
    q_loads<0>((unsigned)lane * 16u, Qh + (size_t)(bq * 4 + wid) * 16384, make_iseq<16>{});
    dma_tile(w.mk + 32768, c.goff, w.Kh + (size_t)tile_of(w, 1) * 32768);
    dma_tile(w.mv, c.goff, w.Vh + (size_t)tile_of(w, 0) * 32768);
    if (wid == 3) CODE_TOUCH2(K2_SYM, lds0 + AT_V + 65536, j & 15, lane);
    const bool q2now = !isA && w.n1 <= 3;
    const char* q2p = Qh + (size_t)(i * 4 + wid) * 16384;
    if (q2now) q_loads<16>((unsigned)lane * 16u, q2p, make_iseq<16>{});
    a_zeros(make_iseq<128>{});
    c.cm[0] = f32x16{}; c.cm[1] = f32x16{}; c.m = 0.f; c.l = 0.f; c.rs0 = 0.f; c.rs1 = 0.f; c.alpha = 1.f; c.pend = false; c.drain = false;
    c.aone = hi ? 0.f : 1.f;
    PIN(c.cm[0]);
    if (q2now) asm volatile("s_waitcnt vmcnt(32)" ::: "memory"); else asm volatile("s_waitcnt vmcnt(16)" ::: "memory");
    asm volatile("s_barrier" ::: "memory");
    SBAR();
    c.fr[0] = kfrag<0, 0>(c); c.fr[1] = kfrag<0, 1>(c); c.fr[2] = kfrag<0, 2>(c); c.fr[3] = kfrag<0, 3>(c); c.fr[4] = kfrag<0, 4>(c); c.fr[5] = kfrag<0, 5>(c);
    SBAR();
    phaseA<1, 0>(c, make_iseq<32>{});
    dma_tile(w.mv + 32768, c.goff, w.Vh + (size_t)tile_of(w, 1) * 32768);
    SBAR(); asm volatile("s_nop 7\n\ts_nop 7" : "+v"(c.s[0][0]), "+v"(c.s[0][1])); SBAR();
    c.first = true;
    maxsteps1(c, make_iseq<8>{});
    decide<1>(c);
    exps_from<0, 0>(c, make_iseq<NEB>{});
    asm volatile("s_waitcnt vmcnt(8) lgkmcnt(0)\n\ts_barrier" ::: "memory");
    SBAR();
    c.fr[0] = kfrag<1, 0>(c); c.fr[1] = kfrag<1, 1>(c); c.fr[2] = kfrag<1, 2>(c); c.fr[3] = kfrag<1, 3>(c); c.fr[4] = kfrag<1, 4>(c); c.fr[5] = kfrag<1, 5>(c);
    TS(ws, 1, 1);
    const int q2at = isA || q2now ? -1 : 2;
    for (int g0 = 0, gstop = isA ? 14 : w.n1 - 1; ; g0 += 2, gstop = 14) {
        for (; g0 < gstop; g0 += 2) {
            if (g0 == q2at) q_loads<16>(c.o.cv, q2p, make_iseq<16>{});
            pair<false, false>(c, w, g0);
        }
        if (g0 >= 14) break;
        pair<false, true>(c, w, g0);
    }
    TS(ws, 1, 2);
    if (!isA && w.n1 == 15) pair<true, true>(c, w, 14); else pair<true, false>(c, w, 14);
    TS(ws, 1, 3);
    int h2 = head; asm volatile("" : "+s"(h2), "+s"(ws));
    const int rq = isA ? bq : i, fn = lane & 15, rg = lane >> 4;
    const int trow = rq * 128 + (h2 >> 1) * 16, tcol = (h2 & 1) * 32 + wid * 8;
    unsigned* flags = (unsigned*)(ws + WS_FLAGS) + (N_AFLAGS + rq * 4) * FLAG_STRIDE;
    o.wbase = (const char*)ws + WS_WI + (size_t)h2 * 229376 + wid * 8192;
    o.lds0 = lds0 + wid * 8192; o.wp = lds + lane * 16; o.fl = flags + (h2 >> 2) * FLAG_STRIDE; o.pub = flags + wid * FLAG_STRIDE + h2; o.done = 0u;
    o.xp = (const char*)x + ((size_t)trow * DM + tcol) * 2; o.xo = (unsigned)(fn * DM + (rg >> 1) * 64 + (rg & 1) * 4) * 2u;
    o.yp = (const char*)ws + WS_Y2 + ((size_t)tcol * SEQ + trow) * 2; o.yo = (unsigned)(((rg >> 1) * 64 + (rg & 1) * 4) * SEQ + fn) * 2u;
    o.gp = (const char*)ws + WS_G + trow * 4; o.ws = ws;
    o.pb = (const char*)ws + WS_OP + (size_t)(rq * 64 + (h2 >> 2)) * 28672 + ((h2 & 1) * 2 + (wid & 1)) * 1024 + ((wid >> 1) * 32 + (h2 & 2) * 8) * 16;
    o.po = (unsigned)(rg * 114688 + fn * 16);
    c.vcur = lds + AT_V + 32768 + lane * 16;
    c.fr[0] = vfrag<0>(c.vcur); c.fr[1] = vfrag<1>(c.vcur); c.fr[2] = vfrag<2>(c.vcur); c.fr[3] = vfrag<3>(c.vcur); c.fr[4] = vfrag<4>(c.vcur); c.fr[5] = vfrag<5>(c.vcur);
    SBAR();
    exps_from<0, NEB>(c, make_iseq<32 - NEB>{});
    packs<0>(c, make_iseq<16>{});
    c.l += c.rs0 + c.rs1;
    PIN(c.p[0]); PIN(c.p[1]); PIN(c.p[2]); PIN(c.p[3]);
    SBAR();
    if (isA && !c.early) {
        asm volatile("s_waitcnt vmcnt(0)" ::: "memory");
        c.early = rfl(__float_as_uint(a_read<128>())) != 0u;
        __builtin_amdgcn_fence(__ATOMIC_ACQUIRE, "wavefront");
        if (c.early) { ml_load(c); p_loads(c, make_iseq<16>{}); }
    }
    SBAR(); NOPS2(); SBAR();
    phaseB<0, 64>(c, make_iseq<32>{});
    SBAR();
    if (isA && !c.early) {
        unsigned spins = 0;
        while (rfl(__hip_atomic_load(c.flag, RLX_AGENT)) == 0u) { __builtin_amdgcn_s_sleep(1); if (++spins > (1u << 22)) break; }
        __builtin_amdgcn_fence(__ATOMIC_ACQUIRE, "wavefront");
        ml_load(c);
        p_loads(c, make_iseq<16>{});
        asm volatile("s_waitcnt vmcnt(0)" : "+v"(c.ml2) :: "memory");
    }
    const float lt = halfsum(c.l);
    TS(ws, 1, 4);
    u32x4 bf[16];
    c.part = (const char*)ws + WS_OP + (size_t)((rq * 16 + h2) * 4 + wid) * 28672;
    if (isA) {
        asm volatile("s_waitcnt vmcnt(32)" : "+v"(c.ml2) :: "memory");
        const float mn = __builtin_fmaxf(c.m, c.ml2[0]);
        const float w1 = ex2(c.m - mn), w2 = ex2(c.ml2[0] - mn);
        const float inv = 1.0f / (lt * w1 + c.ml2[1] * w2);
        op_first<true>(c, bf, w1 * inv, w2 * inv);
    } else op_first<false>(c, bf, 1.0f / lt, 0.f);
    op_steps<false, 32>(c, bf, 0.f, 0.f, make_iseq<192>{});
    NOPS16(); SBAR();
    op_last(c, make_iseq<8>{});
    op_look(o);
    TS(ws, 1, 5);
#pragma unroll
    for (int e = 0; e < 16; ++e) PIN(o.yw[e]);
    PIN(o.xw[0]); PIN(o.xw[1]); PIN(o.xw[2]); PIN(o.xw[3]); PIN(o.g);
    const float g = o.g, og = rbf(1.0f - g);
    float ta[16];
#pragma unroll
    for (int e = 0; e < 8; ++e) {
        ta[2 * e] = rbf(g * rbf(bf_lo(o.xw[e >> 1][e & 1]) * bf_lo(o.yw[2 * e]))); ta[2 * e + 1] = rbf(g * rbf(bf_hi(o.xw[e >> 1][e & 1]) * bf_lo(o.yw[2 * e + 1])));
        PIN(ta[2 * e]); PIN(ta[2 * e + 1]);
    }
    u32x4 sel[2]; f32x4 acc[4];
#pragma unroll
    for (int s2 = 0; s2 < 2; ++s2) {
#pragma unroll
        for (int e = 0; e < 4; ++e) sel[s2][e] = (fn >> 1) == 4 * s2 + e ? 0x3f80u << (16 * (fn & 1)) : 0u;
        acc[2 * s2] = f32x4{}; acc[2 * s2 + 1] = f32x4{}; PIN(sel[s2]); PIN(acc[2 * s2]); PIN(acc[2 * s2 + 1]);
    }
    SBAR();
    asm volatile("s_waitcnt vmcnt(6)" ::: "memory");
    op_publish(o, 6u);
    asm volatile("s_waitcnt vmcnt(2)" ::: "memory");
    op_publish(o, 7u);
    asm volatile("s_waitcnt vmcnt(2)\n\ts_barrier" ::: "memory");
    TS(ws, 1, 7);
    TS(ws, 2, 0);
    for (unsigned spins = 0; spins < (1u << 22); ++spins) {
        const unsigned d0 = o.done;
        const unsigned v = __float_as_uint(a_read<208>());
        o.look = fn == h2 ? 7u : v;
        PIN(o.look);
        op_look(o);
        op_takes(o, make_iseq<7>{});
        if (o.done == 7u) break;
        op_wait<6>(o.done - d0);
        if (o.done == d0) __builtin_amdgcn_s_sleep(1);
    }
    TS(ws, 2, 4);
    asm volatile("s_waitcnt vmcnt(0)" ::: "memory");
    TS(ws, 2, 1);
    op_adds(acc, sel, make_iseq<32>{});
    asm volatile("s_nop 7\n\ts_nop 7" : "+v"(acc[0]), "+v"(acc[1]), "+v"(acc[2]), "+v"(acc[3]));
#pragma unroll
    for (int f = 0; f < 4; ++f) {
        float r[4];
#pragma unroll
        for (int e = 0; e < 4; ++e) r[e] = ta[4 * f + e] + rbf(og * rbf(acc[f][e]));
        if (f < 3 || rg < 2) *(u32x2*)((char*)out + ((size_t)trow * DM + tcol) * 2 + o.xo + f * 256) = (u32x2){cvtpk(r[0], r[1]), cvtpk(r[2], r[3])};
    }
    TS(ws, 2, 7);
}

extern "C" __device__ __attribute__((used, noinline)) void text_end_mark() { asm volatile("s_nop 0"); }

extern "C" void kernel_launch(void* const* d_in, const int* in_sizes, int n_in, void* d_out, int out_size, void* d_ws, size_t ws_size,
                              hipStream_t stream) {
    (void)in_sizes; (void)n_in; (void)out_size; (void)ws_size;
    (void)hipFuncSetAttribute((const void*)k1_proj, hipFuncAttributeMaxDynamicSharedMemorySize, K1_LDS);
    (void)hipFuncSetAttribute((const void*)k2_attn, hipFuncAttributeMaxDynamicSharedMemorySize, AT_LDS);
    K1Args P;
    P.x = (const bf16_t*)d_in[0]; P.ak = (const bf16_t*)d_in[1]; P.av = (const bf16_t*)d_in[2]; P.shift_k = (const bf16_t*)d_in[3];
    P.ssm_k = (const bf16_t*)d_in[4]; P.ssm_d = (const bf16_t*)d_in[5]; P.ckv = (const bf16_t*)d_in[7]; P.wq = (const bf16_t*)d_in[8];
    P.wuk = (const bf16_t*)d_in[9]; P.wuv = (const bf16_t*)d_in[10]; P.gate_w = (const bf16_t*)d_in[12]; P.wo = (const bf16_t*)d_in[11]; P.ws = (unsigned char*)d_ws;
    k1_proj<<<256, 512, K1_LDS, stream>>>(P);
    k2_attn<<<256, 256, AT_LDS, stream>>>((unsigned char*)d_ws, (const bf16_t*)d_in[0], (bf16_t*)d_out);
}
```
